```python
import torch

def _softmax(x, axis=-1):
    return torch.softmax(x, dim=axis)
def _gelu(x):
    return torch.nn.functional.gelu(x, approximate="tanh")
def _silu(x):
    return torch.nn.functional.silu(x)
def _sigmoid(x):
    return torch.sigmoid(x)
def _layernorm(x, axis=-1, eps=1e-5):
    m = x.mean(dim=axis, keepdim=True)
    v = x.var(dim=axis, keepdim=True, unbiased=False)
    return (x - m) / torch.sqrt(v + eps)
def _scatter_add(tgt, idx, src, axis=0):
    return tgt.clone().index_add_(axis, idx, src)
def _segment_sum(src, seg, n):
    out = torch.zeros((n,) + src.shape[1:], dtype=src.dtype)
    return out.index_add_(0, seg, src)
def _pad(x, pw):
    flat = tuple(v for pair in reversed(pw) for v in pair)
    return torch.nn.functional.pad(x, flat)

kernel_name = 'gated_mix__sliding_local_dilated__sliding_local_dilated__prompt'

def setup_inputs():

    _w = (lambda a: (lambda t: t.to(torch.bfloat16) if t.is_floating_point() else t)(torch.as_tensor(a)))
    def _A_setup():
        B, S, D = 1, 2048, 576
        return {
            "x": _w((lambda *s: torch.randn(*s, dtype=torch.bfloat16) * 0.05)(B, S, D)),
            "wqkv": _w((lambda *s: torch.randn(*s, dtype=torch.bfloat16) * 0.8)(D, 3 * D)),
            "wo": _w((lambda *s: torch.randn(*s, dtype=torch.bfloat16) * 0.05)(D, D)),
        }
    _A_in = _A_setup()

    def _B_setup():
        B, S, D = 1, 2048, 576
        return {
            "x": _w((lambda *s: torch.randn(*s, dtype=torch.bfloat16) * 0.05)(B, S, D)),
            "wqkv": _w((lambda *s: torch.randn(*s, dtype=torch.bfloat16) * 0.8)(D, 3 * D)),
            "wo": _w((lambda *s: torch.randn(*s, dtype=torch.bfloat16) * 0.05)(D, D)),
        }
    _B_in = _B_setup()

    return {
        "A_x": _A_in["x"],
        "A_wqkv": _A_in["wqkv"],
        "A_wo": _A_in["wo"],
        "B_x": _B_in["x"],
        "B_wqkv": _B_in["wqkv"],
        "B_wo": _B_in["wo"],
        "gate_w": _w((lambda *s: torch.randn(*s, dtype=torch.bfloat16) * 0.05)(576) * 16.0),
    }

def reference(A_x, A_wqkv, A_wo, B_x, B_wqkv, B_wo, gate_w):
    def _A_ref(x, wqkv, wo):
        B, S, D = x.shape
        H = 32; Dh = D // H
        W, dil = 8, 2
        qkv = (lambda x, *d: x.permute(*d))((x @ wqkv).reshape(B, S, 3, H, Dh), 2, 0, 3, 1, 4)
        q, k, v = qkv[0], qkv[1], qkv[2]
        scale = Dh ** -0.5
        pos = torch.arange(S)
        off = torch.arange(W) * dil
        idx = pos[:, None] - off[None, :]
        mask = idx >= 0
        idx_c = torch.where(mask, idx, 0).reshape(-1)
        kg = k[:, :, idx_c, :].reshape(B, H, S, W, Dh)
        vg = v[:, :, idx_c, :].reshape(B, H, S, W, Dh)
        logits = torch.einsum("bhsd,bhswd->bhsw", q, kg) * scale
        logits = torch.where(mask[None, None, :, :], logits, -1e9)
        attn = _softmax(logits, axis=-1)
        o = torch.einsum("bhsw,bhswd->bhsd", attn, vg)
        return (lambda x, *d: x.permute(*d))(o, 0, 2, 1, 3).reshape(B, S, D) @ wo

    def _B_ref(x, wqkv, wo):
        B, S, D = x.shape
        H = 32; Dh = D // H
        W, dil = 8, 2
        qkv = (lambda x, *d: x.permute(*d))((x @ wqkv).reshape(B, S, 3, H, Dh), 2, 0, 3, 1, 4)
        q, k, v = qkv[0], qkv[1], qkv[2]
        scale = Dh ** -0.5
        pos = torch.arange(S)
        off = torch.arange(W) * dil
        idx = pos[:, None] - off[None, :]
        mask = idx >= 0
        idx_c = torch.where(mask, idx, 0).reshape(-1)
        kg = k[:, :, idx_c, :].reshape(B, H, S, W, Dh)
        vg = v[:, :, idx_c, :].reshape(B, H, S, W, Dh)
        logits = torch.einsum("bhsd,bhswd->bhsw", q, kg) * scale
        logits = torch.where(mask[None, None, :, :], logits, -1e9)
        attn = _softmax(logits, axis=-1)
        o = torch.einsum("bhsw,bhswd->bhsd", attn, vg)
        return (lambda x, *d: x.permute(*d))(o, 0, 2, 1, 3).reshape(B, S, D) @ wo

    x = A_x
    g = _sigmoid((x * gate_w).sum(-1, keepdims=True))
    a = _A_ref(x, A_wqkv, A_wo)
    b = _B_ref(x, B_wqkv, B_wo)
    return g * a + (1 - g) * b
```

```cpp
#define PRE0 4
#include <hip/hip_runtime.h>
#include <hip/hip_bf16.h>
#include <stdint.h>

typedef unsigned short u16;
typedef unsigned long long u64;
typedef short s16x4 __attribute__((ext_vector_type(4)));
typedef float f32x2 __attribute__((ext_vector_type(2)));
typedef float f32x4 __attribute__((ext_vector_type(4)));
typedef unsigned u32x2 __attribute__((ext_vector_type(2)));
typedef unsigned u32x4 __attribute__((ext_vector_type(4)));
typedef u32x4 frag;
typedef __bf16 bf16x2 __attribute__((ext_vector_type(2)));
typedef __bf16 bfx8 __attribute__((ext_vector_type(8)));
#define LDSP __attribute__((address_space(3)))
#define GAS __attribute__((address_space(1)))
#define DI __device__ __forceinline__

constexpr int WROW = 3456;
constexpr int XROW = 1152;
constexpr int PITCH = 432;
constexpr int KO = 32 * PITCH;
constexpr int STG_W = 64 * PITCH;
constexpr int STG = STG_W + 144 * 128;
constexpr int G_OFF = 3 * STG;
constexpr int SCR_OFF = G_OFF + 4608;
constexpr int EX_OFF = 143360;
constexpr int LDS_BYTES = 163840;
constexpr int O_BYTES = 128 * 36 * 1024;
constexpr int FLAG_OFF = 8 << 20;
constexpr int EPOCH_OFF = FLAG_OFF + (1 << 20);
#define ROT 2
#define NBX 22
#define FS1 128
#define FS2 8
#define PSLEEP 1
#define CODEPF 17792
#define NB1 5

#define MFMA(a, b, c) __builtin_amdgcn_mfma_f32_16x16x32_bf16(__builtin_bit_cast(bfx8, (a)), __builtin_bit_cast(bfx8, (b)), (c), 0, 0, 0)
#define RLX_AGENT __ATOMIC_RELAXED, __HIP_MEMORY_SCOPE_AGENT
#define HOFF(h) (36 * (h) + ((h) & 1) * 4)

DI void glds16(unsigned voff, const void* sbase, unsigned lds_addr) {
  asm volatile("s_mov_b32 m0, %2\n\ts_nop 0\n\tglobal_load_lds_dwordx4 %0, %1" ::"v"(voff), "s"(sbase), "s"(lds_addr) : "memory");
}
DI u32x2 trd(unsigned a) { return __builtin_bit_cast(u32x2, __builtin_amdgcn_ds_read_tr16_b64_v4i16((LDSP s16x4*)(size_t)a)); }
DI frag ldW(unsigned a0, unsigned a1) {
  u32x2 lo = trd(a0), hi = trd(a1);
  return __builtin_shufflevector(lo, hi, 0, 1, 2, 3);
}
DI frag ld128(unsigned a) { return *(LDSP frag*)(size_t)a; }
DI u32x2 ld64(unsigned a) { return *(LDSP u32x2*)(size_t)a; }
DI void st64(unsigned a, u32x2 v) { *(LDSP u32x2*)(size_t)a = v; }
DI unsigned cvtpk(float a, float b) {
  f32x2 f = {a, b};
  return __builtin_bit_cast(unsigned, __builtin_convertvector(f, bf16x2));
}
DI u32x2 pack4(f32x4 a) { return u32x2{cvtpk(a[0], a[1]), cvtpk(a[2], a[3])}; }
DI frag cat(u32x2 a, u32x2 b) { return __builtin_shufflevector(a, b, 0, 1, 2, 3); }
DI void swap32(float& a, float& b) {
  auto r = __builtin_amdgcn_permlane32_swap(__float_as_uint(a), __float_as_uint(b), false, false);
  a = __uint_as_float(r[0]); b = __uint_as_float(r[1]);
}
DI void swap16(float& a, float& b) {
  auto r = __builtin_amdgcn_permlane16_swap(__float_as_uint(a), __float_as_uint(b), false, false);
  a = __uint_as_float(r[0]); b = __uint_as_float(r[1]);
}
DI float fmx(float a, float b) { return __builtin_elementwise_maximum(a, b); }
DI float fmx3(float a, float b, float c) { return fmx(fmx(a, b), c); }
#define STAMP(i)
#define RSTAMP(i)
#define STAMP_OUT()
#define STAMP_ARG
#define STAMP_PASS
#define STAMP_DECL
#define SGB(m, n) __builtin_amdgcn_sched_group_barrier(m, n, 0)
#define SCHED0() __builtin_amdgcn_sched_barrier(0)
#define WAIT_LGKM0() do { __builtin_amdgcn_s_waitcnt(0xC07F); asm volatile("" ::: "memory"); } while (0)
#define WAIT_VM0() asm volatile("s_waitcnt vmcnt(0)" ::: "memory")
#define BARRIER() __builtin_amdgcn_s_barrier()
#define PIN(x) asm volatile("" : "+v"(x))
#define TAG_SCALAR() do { unsigned lo_ = __builtin_amdgcn_readfirstlane((unsigned)tag), hi_ = __builtin_amdgcn_readfirstlane((unsigned)(tag >> 32)); \
    tag = (((u64)hi_ << 32) | lo_) + 1; asm volatile("" : "+s"(tag)); } while (0)
#define PINS(x) asm volatile("" : "+s"(x))
#define WR2(addr, a, b, o0, o1) asm volatile("ds_write2st64_b64 %0, %1, %2 offset0:%3 offset1:%4" ::"v"(addr), "v"(a), "v"(b), "i"(o0), "i"(o1) : "memory")

DI void pv_store(u32x4 pk, const frag (&V)[5], u32x4 sel, unsigned vmain, unsigned vleft, __amdgpu_buffer_rsrc_t ors, unsigned so, unsigned sl, bool g0) {
  const f32x4 z = {0.f, 0.f, 0.f, 0.f};
  frag P[4];
  f32x4 o[4], os[4];
  auto mkP = [&](int h) {
    unsigned u = pk[h];
    P[h] = frag{__builtin_amdgcn_perm(u, u, sel[0]), __builtin_amdgcn_perm(u, u, sel[1]), __builtin_amdgcn_perm(u, u, sel[2]), __builtin_amdgcn_perm(u, u, sel[3])};
    PIN(P[h]);
  };
  mkP(0); mkP(1);
  SCHED0();
  u32x4 f0, f1;
  os[0] = MFMA(V[4], P[0], z); PIN(os[0]); o[0] = MFMA(V[0], P[0], z); PIN(o[0]); mkP(2); SCHED0();
  os[1] = MFMA(V[4], P[1], z); PIN(os[1]); o[1] = MFMA(V[1], P[1], z); PIN(o[1]); mkP(3); SCHED0();
  os[2] = MFMA(V[4], P[2], z); PIN(os[2]); os[3] = MFMA(V[4], P[3], z); PIN(os[3]);
  o[2] = MFMA(V[2], P[2], z); PIN(o[2]); o[3] = MFMA(V[3], P[3], z); PIN(o[3]); SCHED0();
  f0 = u32x4{cvtpk(o[0][0], o[0][1]), cvtpk(o[0][2], o[0][3]), cvtpk(o[1][0], o[1][1]), cvtpk(o[1][2], o[1][3])};
  PIN(f0); SCHED0();
  __builtin_amdgcn_raw_buffer_store_b128(f0, ors, vmain, so, 16);
  SCHED0();
  u32x2 f2 = {cvtpk(g0 ? os[0][0] : os[2][0], g0 ? os[0][1] : os[2][1]), cvtpk(g0 ? os[1][2] : os[3][2], g0 ? os[1][3] : os[3][3])};
  PIN(f2); SCHED0();
  f1 = u32x4{cvtpk(o[2][0], o[2][1]), cvtpk(o[2][2], o[2][3]), cvtpk(o[3][0], o[3][1]), cvtpk(o[3][2], o[3][3])};
  PIN(f1); PIN(f0); SCHED0();
  __builtin_amdgcn_raw_buffer_store_b128(f1, ors, vmain + 1024, so, 16);
  __builtin_amdgcn_raw_buffer_store_b64(f2, ors, vleft, sl, 16);
  SCHED0();
}

template <int MT>
DI void phase2(unsigned L, char* ws, __amdgpu_buffer_rsrc_t ors, u64 tag, int tt0, int mrow, int kq, int rot2, int lane STAMP_ARG) {
  const int g = lane >> 4, q = (lane >> 2) & 3, p = lane & 3, c = lane & 15;
  const unsigned sw = (2 * g + (q >> 1)) & 3;
  unsigned bw[4];
#pragma unroll
  for (int n = 0; n < 4; ++n) bw[n] = L + kq * 36864 + (4 * g + q) * 128 + 8 * p + ((n ^ sw) * 32);
  frag wf[9][4];
  auto rdw = [&](int i) {
#pragma unroll
    for (int n = 0; n < 4; ++n) wf[i][n] = ldW(bw[n] + i * 4096, bw[n] + i * 4096 + 2048);
  };
  rdw(0); rdw(1); rdw(2); rdw(3); rdw(4);
  SCHED0();
  unsigned so[9], vo[MT];
#pragma unroll
  for (int i = 0; i < 9; ++i) so[i] = (i < 8 ? 8 * kq + ((i + rot2) & 7) : 32 + kq) * 1024;
#pragma unroll
  for (int m = 0; m < MT; ++m) { int tt = tt0 + m; tt = tt > 127 ? 127 : tt; vo[m] = lane * 16 + tt * 36864; }
  int tf = (lane & 4) ? tt0 + MT - 1 : tt0; tf = tf > 127 ? 127 : tf;
  GAS u64* fp = (GAS u64*)(ws + FLAG_OFF + (kq * 64 + (tf >> 1)) * FS1 + (lane & 3) * FS2);
  unsigned rb[4];
#pragma unroll
  for (int n = 0; n < 4; ++n) rb[n] = L + kq * 36864 + (mrow + c) * 256 + (((4 * n + g) ^ c) * 16);
#pragma unroll
  for (int i = 0; i < 9; ++i) PINS(so[i]);
#pragma unroll
  for (int m = 0; m < MT; ++m) PIN(vo[m]);
#pragma unroll
  for (int n = 0; n < 4; ++n) PIN(rb[n]);
  PIN(fp);
  WAIT_LGKM0();
  BARRIER();
  STAMP(27);
#pragma clang loop unroll(disable)
  for (unsigned spins = 0; spins < (1u << 17); ++spins) {
    u64 v = __hip_atomic_load(fp, RLX_AGENT);
    if (__all(v == tag)) break;
    __builtin_amdgcn_s_sleep(PSLEEP);
  }
  __builtin_amdgcn_fence(__ATOMIC_ACQUIRE, "wavefront");
  asm volatile("" ::: "memory");
  STAMP(28);
  RSTAMP(62);
  frag of[9][MT];
  constexpr int WIN = 2;
#pragma unroll
  for (int i = 0; i < WIN; ++i)
#pragma unroll
    for (int m = 0; m < MT; ++m) { of[i][m] = __builtin_amdgcn_raw_buffer_load_b128(ors, vo[m], so[i], 16); SCHED0(); }
  f32x4 acc[MT][4];
#pragma unroll
  for (int i = 0; i < 9; ++i) {
    const bool rd = i >= 2 && i + 3 < 9;
    if (rd) rdw(i + 3);
#pragma unroll
    for (int m = 0; m < MT; ++m) {
#pragma unroll
      for (int n = 0; n < 4; ++n) acc[m][n] = MFMA(wf[i][n], of[i][m], i == 0 ? (f32x4{0, 0, 0, 0}) : acc[m][n]);
      if (i + WIN < 9) of[i + WIN][m] = __builtin_amdgcn_raw_buffer_load_b128(ors, vo[m], so[i + WIN], 16);
    }
#pragma unroll
    for (int m = 0; m < MT; ++m) {
#pragma unroll
      for (int n = 0; n < 4; ++n) { SGB(0x8, 1); if (m < 2 && rd) SGB(0x100, 1); }
      if (i + WIN < 9) SGB(0x20, 1);
    }
    SCHED0();
  }
  STAMP(29);
#pragma unroll
  for (int m = 0; m < MT; ++m)
#pragma unroll
    for (int n = 0; n < 4; ++n) *(LDSP f32x4*)(size_t)(rb[n] + m * 4096) = acc[m][n];
  WAIT_LGKM0();
  BARRIER();
}

__global__ __launch_bounds__(512, 2) void fused_kernel(const u16* __restrict__ x, const u16* __restrict__ wA, const u16* __restrict__ wB,
                                                        const u16* __restrict__ woA, const u16* __restrict__ woB, const u16* __restrict__ gw,
                                                        char* __restrict__ ws, u16* __restrict__ out) {
  __shared__ __attribute__((aligned(1024))) char lds[LDS_BYTES];
  const unsigned L = (unsigned)(size_t)(LDSP char*)lds;
  const int bid = blockIdx.x;
  const int xcd = bid & 7, idx = bid >> 3;
  const int tl = idx & 7, gl = idx >> 3;
  const int tile = (xcd & 1) * 8 + tl;
  const int grp = (xcd >> 1) * 4 + gl;
  int rot = ROT == 1 ? tl + 2 * gl : ROT == 2 ? (tl >> 1) * 2 + 2 * gl : 0;
  if (rot >= 9) rot -= 9;
  const int tid = threadIdx.x, lane = tid & 63;
  const int wave = __builtin_amdgcn_readfirstlane(tid >> 6);
  const int vl = lane >> 3;
  STAMP_DECL;
  STAMP(0);
  RSTAMP(60);
  if (tid < 6) {
    u64 pc0;
    asm volatile("s_getpc_b64 %0" : "=s"(pc0));
    unsigned ln = idx * 6 + tid;
    ln = ln < CODEPF / 128 ? ln : 0;
    asm volatile("s_mov_b32 m0, %2\n\ts_nop 0\n\tglobal_load_lds_dword %0, %1" ::"v"(ln * 128), "s"(pc0 & ~127ull), "s"(L + SCR_OFF) : "memory");
  }
  asm volatile("" ::"s"(x), "s"(wA), "s"(wB), "s"(woA), "s"(woB), "s"(gw), "s"(ws), "s"(out));
  u64 tag;
  u32x2 gv = {0u, 0u};
  if (tid < 144) gv = *(const u32x2*)(gw + tid * 4);
  const int lin0 = xcd * 30 + idx;
  const bool act2 = idx < 30 && lin0 < 234;
  const int lin = act2 ? lin0 : 0;
  const int rt = (lin * 57) >> 9, ct = lin - rt * 9;
  const int rot2 = 3 * (rt - ((rt * 171) >> 9) * 3);
  const float sg = (grp >= 8) ? 1.4426950408889634f : -1.4426950408889634f;
  const unsigned wsch = ((((lane & 7) >> 1) ^ ((vl >> 1) & 3)) << 5) | ((lane & 1) << 4);
  const unsigned offn = vl * XROW + wsch, offl = ((vl >> 2) * 72 + (vl & 3)) * XROW + wsch;
  auto wo_piece = [&](int kq, int i, int j) {
    const char* b = (const char*)(kq >= 2 ? woB : woA) + ct * 128 + (kq & 1) * (288 * XROW);
    if (i == 8) b += ((j & 1) * 144 + ((j >> 1) ? 52 : 16)) * XROW;
    else b += (((i + rot2) & 7) * 36 + (j >> 1) * 20 + 8 * (j & 1)) * XROW;
    glds16(i == 8 ? offl : offn, b, L + (kq * 9 + i) * 4096 + j * 1024);
  };
  const __amdgpu_buffer_rsrc_t ors = __builtin_amdgcn_make_buffer_rsrc(ws, 0, O_BYTES, 0x00020000);
  const int g = lane >> 4, q = (lane >> 2) & 3, p = lane & 3, c = lane & 15;
  const int cw = wave & 3;

  if (__builtin_expect(wave >= 4, 1)) {
    __builtin_amdgcn_s_setprio(2);
    const int lw = cw, tl256 = tid - 256;
    const unsigned offx = (tile * 128 + 8 * lw + vl) * XROW + ((lane & 7) ^ ((4 * lw + (vl >> 1)) & 7)) * 16;
    const char* w = (const char*)((grp >= 8) ? wB : wA) + (grp & 7) * 144;
    unsigned offw[7], offh;
    auto kchunk = [&](int st) { int kc = st + rot; return kc >= 9 ? kc - 9 : kc; };
    auto issueX = [&](int kc, unsigned sb, int jj) { glds16(offx, (const char*)x + kc * 128 + jj * (32 * XROW), sb + STG_W + (2 + lw + 4 * jj) * 1024); };
    auto issueW = [&](int kc, unsigned sb, int j) { glds16(offw[j], w + kc * (64 * WROW), sb + (lw + 4 * j) * 1024); };
    auto issue6 = [&](int kc, unsigned sb) {
      if (lw < 3) issueW(kc, sb, 6);
      else { glds16(offw[6], (const char*)x + kc * 128, sb + STG_W); glds16(offh, (const char*)x + kc * 128, sb + STG_W + 1024); }
    };
    const int kc0 = rot, kc1 = kchunk(1);
    const unsigned sb0 = L, sb1 = L + STG;
    auto calcw = [&](int j) {
      unsigned qq = 256 * j + tl256;
      unsigned u = (qq * 2428u) >> 16, cc = qq - u * 27;
      unsigned row = (u & 0x38) | ((u & 1) << 2) | ((u >> 1) & 3);
      offw[j] = row * WROW + cc * 16 + ((cc * 57u) >> 9) * 1008;
    };
    issueX(kc0, sb0, 0);
#pragma unroll
    for (int j = 0; j < 6; ++j) {
      SCHED0();
      calcw(j);
      SCHED0();
      issueW(kc0, sb0, j);
      if (j < 3) issueX(kc0, sb0, j + 1);
    }
    calcw(6);
    {
      int t0 = tile * 128 - 16 + vl, t1 = t0 + 8;
      t0 = t0 < 0 ? 0 : t0; t1 = t1 < 0 ? 0 : t1;
      unsigned h0 = t0 * XROW + ((lane & 7) ^ (vl >> 1)) * 16;
      offh = t1 * XROW + ((lane & 7) ^ (4 + (vl >> 1))) * 16;
      offw[6] = lw == 3 ? h0 : offw[6];
    }
    issue6(kc0, sb0);
    STAMP(1);
#pragma unroll
    for (int j = 0; j < PRE0; ++j) issueW(kc1, sb1, j);
    STAMP(2);
    f32x4 he = {0, 0, 0, 0};
    const unsigned aE = L + (8 * g + 2 * q) * PITCH + (lw == 0 ? 144 : 288) + ((p & 1) ? 104 : 32);
    const unsigned aX = L + STG_W + c * 128 + ((g ^ (c >> 1)) * 16);
    frag fe[2], fx[2];
    auto halo_rd = [&](unsigned so) {
      if (lw < 2) {
#pragma unroll
        for (int ks = 0; ks < 2; ++ks) {
          fx[ks] = ld128((aX ^ (ks * 64)) + so);
          fe[ks] = ldW(aE + so + ks * KO, aE + so + ks * KO + PITCH);
        }
      }
    };
    auto halo_mma = [&]() {
      if (lw == 0) { he = MFMA(fe[0], fx[0], he); he = MFMA(fe[1], fx[1], he); }
      if (lw == 1) { he = MFMA(fx[0], fe[0], he); he = MFMA(fx[1], fe[1], he); }
    };
    int slot = 0, kn = kc1;
    asm volatile("s_waitcnt vmcnt(%0)" ::"i"(PRE0) : "memory");
#pragma clang loop unroll(disable)
    for (int st = 0; st < 8; ++st) {
      BARRIER();
      STAMP(3 + st);
      if (st == 0) {
#pragma unroll
        for (int j = PRE0; j < 6; ++j) issueW(kc1, sb1, j);
        tag = __hip_atomic_load((GAS u64*)(ws + EPOCH_OFF), RLX_AGENT);
      }
      const unsigned b1 = L + (slot == 2 ? 0 : slot + 1) * STG, b2 = L + (slot == 0 ? 2 : slot - 1) * STG;
      issue6(kn, b1);
      issueX(kn, b1, 0);
      halo_rd(slot * STG);
      issueX(kn, b1, 1); issueX(kn, b1, 2); issueX(kn, b1, 3);
      SCHED0();
      halo_mma();
      SCHED0();
      kn = kn == 8 ? 0 : kn + 1;
      if (st < 7) {
#pragma unroll
        for (int j = 0; j < 6; ++j) issueW(kn, b2, j);
      } else {
#pragma unroll
        for (int I = 0; I < 6; ++I) wo_piece(0, I, lw);
      }
      slot = slot == 2 ? 0 : slot + 1;
      WAIT_LGKM0(); asm volatile("s_waitcnt vmcnt(6)" ::: "memory");
    }
    BARRIER();
    STAMP(11);
    halo_rd(2 * STG);
    wo_piece(0, 6, lw); wo_piece(0, 7, lw);
    SCHED0();
    halo_mma();
    SCHED0();
    if (lw < 2) {
      u32x2 v = pack4(he);
      if (lw == 0 && tile == 0) v = u32x2{0u, 0u};
      st64(L + EX_OFF + lane * 8 + (lw == 0 ? 4 : 9) * 512, v);
    }
    __builtin_amdgcn_s_setprio(0);
#pragma unroll
    for (int I = 8; I < NBX; ++I) wo_piece(I / 9, I % 9, lw);
    WAIT_LGKM0();
    STAMP(12);
    BARRIER();
    STAMP(13);
#pragma unroll
    for (int I = NBX; I < 34; ++I) wo_piece(I / 9, I % 9, lw);
    WAIT_VM0();
    TAG_SCALAR();
    STAMP(14);
  } else {
    const unsigned wrow = L + (8 * g + 2 * q) * PITCH;
    tag = __hip_atomic_load((GAS u64*)(ws + EPOCH_OFF), RLX_AGENT);
    f32x4 maskP, maskC, maskP0;
    {
      const float ninf = -__builtin_inff();
#pragma unroll
      for (int r = 0; r < 4; ++r) {
        bool pm = (r & 1) == (c & 1);
        maskC[r] = (pm && 4 * g + r <= c) ? 0.f : ninf;
        maskP[r] = (pm && 4 * g + r > c) ? 0.f : ninf;
      }
      maskP0 = maskP + ((tile == 0 && wave == 0) ? -998244352.f * 4.242640687119285f : 0.f);
      asm volatile("" : "+v"(maskP), "+v"(maskC), "+v"(maskP0));
    }
    u32x4 sel;
    {
      const int par = c & 1;
      const bool cur1 = 4 * g + par <= c, cur2 = 4 * g + 2 + par <= c;
      const unsigned s1 = par ? 0x01000c0cu : 0x0c0c0100u;
      const unsigned s2 = par ? 0x03020c0cu : 0x0c0c0302u;
      sel[0] = cur1 ? 0x0c0c0c0cu : s1;
      sel[1] = cur2 ? 0x0c0c0c0cu : s2;
      sel[2] = cur1 ? s1 : 0x0c0c0c0cu;
      sel[3] = cur2 ? s2 : 0x0c0c0c0cu;
    }
    unsigned vmain = lane * 16, vleft = lane < 32 ? c * 16 + g * 8 : 0x40000000u;
    unsigned osoff = (tile * 8 + 2 * wave) * 36864 + grp * 2048, oloff = (tile * 8 + 2 * wave) * 36864 + 32768 + grp * 256;
    GAS u64* myflag = (GAS u64*)(ws + FLAG_OFF + ((grp >> 2) * 64 + tile * 4 + cw) * FS1 + (grp & 3) * FS2);
    PINS(myflag);
    unsigned osoff1 = osoff + 36864, oloff1 = oloff + 36864;
    PINS(osoff); PINS(oloff); PINS(osoff1); PINS(oloff1);
    asm volatile("" : "+v"(sel), "+v"(vmain), "+v"(vleft));
    f32x4 aK[2][4], aQ[2][4], aKQ[2], aV[2][5], hk = {0, 0, 0, 0}, hv = {0, 0, 0, 0};
#pragma unroll
    for (int t = 0; t < 2; ++t) {
#pragma unroll
      for (int f = 0; f < 4; ++f) { aK[t][f] = f32x4{0, 0, 0, 0}; aQ[t][f] = f32x4{0, 0, 0, 0}; }
      aKQ[t] = f32x4{0, 0, 0, 0};
#pragma unroll
      for (int f = 0; f < 5; ++f) aV[t][f] = f32x4{0, 0, 0, 0};
    }
    unsigned cW = wrow + 8 * p;
    unsigned cKQ = wrow + (p < 2 ? 144 : 0) + ((p & 1) ? 104 : 32);
    unsigned cXh0 = L + STG_W + c * 128 + ((g ^ (c >> 1)) * 16), cXh1 = cXh0 ^ 64;
    unsigned cX0 = cXh0 + (1 + 2 * wave) * 2048, cX1 = cX0 ^ 64;
    unsigned cH = cW + 144 + HOFF(wave);
    unsigned cV[4];
#pragma unroll
    for (int i = 0; i < 4; ++i)
      cV[i] = p < 2 ? wrow + 288 + (p ? 104 : 32) + (i >> 1) * KO + (i & 1) * PITCH : L + G_OFF + rot * 512 + (8 * g + q) * 8 + (i >> 1) * 256 + (i & 1) * 32;
    frag cv[5], xb[2];
#pragma unroll
    for (int f = 0; f < 5; ++f) cv[f] = frag{0, 0, 0, 0};
    xb[0] = frag{0, 0, 0, 0}; xb[1] = frag{0, 0, 0, 0};
    int slot = 0, kc = rot;
    unsigned exr = L + EX_OFF + wave * 5120 + lane * 8, exh = L + EX_OFF + wave * 512 + lane * 8;
    unsigned exgw = L + SCR_OFF + wave * 128 + g * 16, exgr = L + SCR_OFF + wave * 128 + c * 4;
    asm volatile("" : "+v"(aK[0][0]), "+v"(aK[0][1]), "+v"(aK[0][2]), "+v"(aK[0][3]), "+v"(aK[1][0]), "+v"(aK[1][1]), "+v"(aK[1][2]), "+v"(aK[1][3]),
                      "+v"(aQ[0][0]), "+v"(aQ[0][1]), "+v"(aQ[0][2]), "+v"(aQ[0][3]), "+v"(aQ[1][0]), "+v"(aQ[1][1]), "+v"(aQ[1][2]), "+v"(aQ[1][3]),
                      "+v"(aKQ[0]), "+v"(aKQ[1]), "+v"(hk), "+v"(hv), "+v"(xb[0]), "+v"(xb[1]), "+v"(exr), "+v"(exh), "+v"(exgw), "+v"(exgr));
    asm volatile("" : "+v"(aV[0][0]), "+v"(aV[0][1]), "+v"(aV[0][2]), "+v"(aV[0][3]), "+v"(aV[0][4]), "+v"(aV[1][0]), "+v"(aV[1][1]), "+v"(aV[1][2]),
                      "+v"(aV[1][3]), "+v"(aV[1][4]), "+v"(cv[0]), "+v"(cv[1]), "+v"(cv[2]), "+v"(cv[3]), "+v"(cv[4]),
                      "+v"(cW), "+v"(cKQ), "+v"(cX0), "+v"(cX1), "+v"(cH), "+v"(cXh0), "+v"(cXh1), "+v"(cV[0]), "+v"(cV[1]), "+v"(cV[2]), "+v"(cV[3]));
    SCHED0();
    STAMP(1);
    if (tid < 144) {
      unsigned a0 = __builtin_amdgcn_perm(gv[0], gv[0], 0x01000100), a1 = __builtin_amdgcn_perm(gv[0], gv[0], 0x03020302);
      unsigned a2 = __builtin_amdgcn_perm(gv[1], gv[1], 0x01000100), a3 = __builtin_amdgcn_perm(gv[1], gv[1], 0x03020302);
      *(LDSP u32x4*)(size_t)(L + G_OFF + tid * 32) = u32x4{a0, a0, a1, a1};
      *(LDSP u32x4*)(size_t)(L + G_OFF + tid * 32 + 16) = u32x4{a2, a2, a3, a3};
    }
    STAMP(2);
    WAIT_LGKM0();
#pragma clang loop unroll(disable)
    for (int st = 0; st < 9; ++st) {
      BARRIER();
      SCHED0();
      frag xa[2], W0[14], W1[9], xh[2], Hk[2], Hv[2];
      xa[0] = ld128(cX0); xa[1] = ld128(cX0 + 2048);
#pragma unroll
      for (int f = 0; f < 4; ++f) W0[f] = ldW(cW + 144 + HOFF(f), cW + 144 + HOFF(f) + PITCH);
      W0[4] = ldW(cKQ, cKQ + PITCH);
#pragma unroll
      for (int f = 0; f < 5; ++f) { aV[0][f] = MFMA(xb[0], cv[f], aV[0][f]); aV[1][f] = MFMA(xb[1], cv[f], aV[1][f]); }
#pragma unroll
      for (int f = 0; f < 4; ++f) W0[5 + f] = ldW(cW + HOFF(f), cW + HOFF(f) + PITCH);
      xh[0] = ld128(cXh0);
      Hk[0] = ldW(cH, cH + PITCH); Hv[0] = ldW(cH + 144, cH + 144 + PITCH);
#pragma unroll
      for (int f = 0; f < 4; ++f) W0[9 + f] = ldW(cW + 288 + HOFF(f), cW + 288 + HOFF(f) + PITCH);
      W0[13] = ldW(cV[0], cV[1]);
      xb[0] = ld128(cX1); xb[1] = ld128(cX1 + 2048);
#pragma unroll
      for (int f = 0; f < 4; ++f) W1[f] = ldW(cW + KO + 144 + HOFF(f), cW + KO + 144 + HOFF(f) + PITCH);
      W1[4] = ldW(cKQ + KO, cKQ + KO + PITCH);
      xh[1] = ld128(cXh1);
      Hk[1] = ldW(cH + KO, cH + KO + PITCH); Hv[1] = ldW(cH + KO + 144, cH + KO + 144 + PITCH);
#pragma unroll
      for (int f = 0; f < 4; ++f) W1[5 + f] = ldW(cW + KO + HOFF(f), cW + KO + HOFF(f) + PITCH);
#pragma unroll
      for (int f = 0; f < 4; ++f) cv[f] = ldW(cW + KO + 288 + HOFF(f), cW + KO + 288 + HOFF(f) + PITCH);
      cv[4] = ldW(cV[2], cV[3]);
#pragma unroll
      for (int f = 0; f < 4; ++f) { aK[0][f] = MFMA(W0[f], xa[0], aK[0][f]); aK[1][f] = MFMA(W0[f], xa[1], aK[1][f]); }
      aKQ[0] = MFMA(W0[4], xa[0], aKQ[0]); aKQ[1] = MFMA(W0[4], xa[1], aKQ[1]);
#pragma unroll
      for (int f = 0; f < 4; ++f) { aQ[0][f] = MFMA(W0[5 + f], xa[0], aQ[0][f]); aQ[1][f] = MFMA(W0[5 + f], xa[1], aQ[1][f]); }
      hk = MFMA(Hk[0], xh[0], hk); hv = MFMA(xh[0], Hv[0], hv);
#pragma unroll
      for (int f = 0; f < 5; ++f) { aV[0][f] = MFMA(xa[0], W0[9 + f], aV[0][f]); aV[1][f] = MFMA(xa[1], W0[9 + f], aV[1][f]); }
#pragma unroll
      for (int f = 0; f < 4; ++f) { aK[0][f] = MFMA(W1[f], xb[0], aK[0][f]); aK[1][f] = MFMA(W1[f], xb[1], aK[1][f]); }
      aKQ[0] = MFMA(W1[4], xb[0], aKQ[0]); aKQ[1] = MFMA(W1[4], xb[1], aKQ[1]);
      hk = MFMA(Hk[1], xh[1], hk); hv = MFMA(xh[1], Hv[1], hv);
#pragma unroll
      for (int f = 0; f < 4; ++f) { aQ[0][f] = MFMA(W1[5 + f], xb[0], aQ[0][f]); aQ[1][f] = MFMA(W1[5 + f], xb[1], aQ[1][f]); }
      const int ds = slot == 2 ? -2 * STG : STG, dg = kc == 8 ? -8 * 512 : 512;
      slot = slot == 2 ? 0 : slot + 1;
      kc = kc == 8 ? 0 : kc + 1;
      const int dv = p < 2 ? ds : dg;
      cW += ds; cKQ += ds; cX0 += ds; cX1 += ds; cH += ds; cXh0 += ds; cXh1 += ds;
#pragma unroll
      for (int i = 0; i < 4; ++i) cV[i] += dv;
      SGB(0x100, 2);
#pragma unroll
      for (int i = 0; i < 54; ++i) {
        SGB(0x8, 1);
        if (i < 2 || (i >= 4 && i <= 48 && (i % 4) == 0)) SGB(0x100, 2); else SGB(0x100, 1);
        if (i >= 41 && i < 53 && (i % 4) != 0) SGB(0x4, 1);
      }
#pragma unroll
      for (int i = 0; i < 5; ++i) { SGB(0x8, 1); SGB(0x2, 2); }
      SGB(0x2, 4);
      SGB(0x8, 1);
      SCHED0();
      WAIT_LGKM0();
    }
    STAMP(12);
    const bool g0 = g == 0;
    frag K[3][4], Q[2][4], Vb[5], Va[5];
    u32x2 ks[2], qs[2];
    aV[1][4] = MFMA(xb[1], cv[4], aV[1][4]); PIN(aV[1][4]);
    aV[0][4] = MFMA(xb[0], cv[4], aV[0][4]); PIN(aV[0][4]);
#pragma unroll
    for (int t = 1; t >= 0; --t) {
      u32x2 kq = pack4(aKQ[t]);
      auto r0 = __builtin_amdgcn_permlane32_swap(kq[0], 0u, false, false);
      auto r1 = __builtin_amdgcn_permlane32_swap(kq[1], 0u, false, false);
      ks[t] = u32x2{r0[0], r1[0]}; qs[t] = u32x2{r0[1], r1[1]};
      PIN(ks[t]); PIN(qs[t]);
    }
    SCHED0();
#pragma unroll
    for (int f = 0; f < 4; ++f) { aV[1][f] = MFMA(xb[1], cv[f], aV[1][f]); PIN(aV[1][f]); K[2][f] = cat(pack4(aK[1][f]), ks[1]); PIN(K[2][f]); SCHED0(); }
    {
      u32x2 h = pack4(hk), hh = pack4(hv);
      if (tile == 0) h = u32x2{0u, 0u};
      WR2(exh, h, hh, 0, 5);
    }
    if (c >= 8) { *(LDSP f32x4*)(size_t)(exgw) = aV[0][4]; *(LDSP f32x4*)(size_t)(exgw + 64) = aV[1][4]; }
    SCHED0();
#pragma unroll
    for (int f = 0; f < 4; ++f) {
      aV[0][f] = MFMA(xb[0], cv[f], aV[0][f]); PIN(aV[0][f]);
      u32x2 v = pack4(aV[1][f]); Vb[f][2] = v[0]; Vb[f][3] = v[1]; PIN(Vb[f]); SCHED0();
    }
    { u32x2 v = pack4(aV[1][4]); Vb[4][2] = v[0]; Vb[4][3] = v[1]; PIN(Vb[4]); }
    if (wave < 3) {
#pragma unroll
      for (int f = 0; f < 4; ++f) WR2(exr, (u32x2{K[2][f][0], K[2][f][1]}), (u32x2{Vb[f][2], Vb[f][3]}), 10 + f, 15 + f);
      WR2(exr, ks[1], (u32x2{Vb[4][2], Vb[4][3]}), 14, 19);
    }
    SCHED0();
#pragma unroll
    for (int h = 0; h < 4; ++h) {
      K[1][h] = cat(pack4(aK[0][h]), ks[0]); PIN(K[1][h]);
      u32x2 qt = pack4(aQ[1][h]);
      unsigned e = ((h < 2) == g0) ? qs[1][h & 1] : 0u;
      Q[1][h] = (h & 1) ? frag{qt[0], qt[1], 0u, e} : frag{qt[0], qt[1], e, 0u}; PIN(Q[1][h]);
    }
    SCHED0();
    TAG_SCALAR();
    WAIT_VM0(); WAIT_LGKM0();
    STAMP(23);
    BARRIER();
    STAMP(24);
    SCHED0();
    u32x2 gz;
    asm volatile("ds_read2_b32 %0, %1 offset0:0 offset1:16" : "=v"(gz) : "v"(exgr) : "memory");
#pragma unroll
    for (int h = 0; h < 4; ++h) asm volatile("ds_read2st64_b64 %0, %1 offset0:%2 offset1:4" : "=v"(K[0][h]) : "v"(exr), "i"(h) : "memory");
    f32x4 sc[2][4], sp[2][4];
#pragma unroll
    for (int h = 0; h < 4; ++h) {
      sc[1][h] = MFMA(K[2][h], Q[1][h], maskC); PIN(sc[1][h]);
      sp[1][h] = MFMA(K[1][h], Q[1][h], maskP); PIN(sp[1][h]);
      u32x2 qt = pack4(aQ[0][h]);
      unsigned e = ((h < 2) == g0) ? qs[0][h & 1] : 0u;
      Q[0][h] = (h & 1) ? frag{qt[0], qt[1], 0u, e} : frag{qt[0], qt[1], e, 0u}; PIN(Q[0][h]);
      SCHED0();
    }
    asm volatile("s_waitcnt lgkmcnt(0)" : "+v"(gz), "+v"(K[0][0]), "+v"(K[0][1]), "+v"(K[0][2]), "+v"(K[0][3])::"memory");
    float v[2][4][2];
#pragma unroll
    for (int h = 0; h < 4; ++h) {
      sc[0][h] = MFMA(K[1][h], Q[0][h], maskC); PIN(sc[0][h]);
      sp[0][h] = MFMA(K[0][h], Q[0][h], maskP0); PIN(sp[0][h]);
      v[1][h][0] = fmx(fmx3(sp[1][h][0], sp[1][h][1], sc[1][h][0]), sc[1][h][1]); PIN(v[1][h][0]);
      v[1][h][1] = fmx(fmx3(sp[1][h][2], sp[1][h][3], sc[1][h][2]), sc[1][h][3]); PIN(v[1][h][1]);
      SCHED0();
    }
#pragma unroll
    for (int f = 0; f < 5; ++f) {
      u32x2 a = pack4(aV[0][f]); Vb[f][0] = a[0]; Vb[f][1] = a[1]; PIN(Vb[f]);
      Va[f] = __builtin_shufflevector(Vb[f], Vb[f], -1, -1, 0, 1); PIN(Va[f]);
    }
#pragma unroll
    for (int f = 0; f < 5; ++f) { u32x2 n = *(volatile LDSP u32x2*)(size_t)(exr + (5 + f) * 512); Va[f][0] = n[0]; Va[f][1] = n[1]; }
    u32x4 pk[2];
    auto softmax = [&](float (&w)[4][2], float gate_logit) {
      swap32(w[0][0], w[2][0]); swap32(w[1][0], w[3][0]); swap32(w[0][1], w[2][1]); swap32(w[1][1], w[3][1]);
      swap16(w[0][0], w[1][0]); swap16(w[2][0], w[3][0]); swap16(w[0][1], w[1][1]); swap16(w[2][1], w[3][1]);
      const float cs = 0.23570226039551584f * 1.4426950408889634f;
      float m = fmx(fmx3(fmx3(w[0][0], w[1][0], w[2][0]), w[3][0], w[0][1]), fmx3(w[1][1], w[2][1], w[3][1]));
      float mc = -m * cs;
      f32x2 e[4];
#pragma unroll
      for (int i = 0; i < 4; ++i) {
        f32x2 a = __builtin_elementwise_fma(f32x2{w[i][0], w[i][1]}, f32x2{cs, cs}, f32x2{mc, mc});
        e[i] = f32x2{__builtin_amdgcn_exp2f(a[0]), __builtin_amdgcn_exp2f(a[1])};
      }
      f32x2 s2 = (e[0] + e[1]) + (e[2] + e[3]);
      float s = s2[0] + s2[1];
      float r = __builtin_amdgcn_rcpf(__builtin_fmaf(s, __builtin_amdgcn_exp2f(gate_logit * sg), s));
      float pf[4];
#pragma unroll
      for (int i = 0; i < 4; ++i) { f32x2 pp = e[i] * r; pf[i] = __uint_as_float(cvtpk(pp[0], pp[1])); }
      swap32(pf[0], pf[2]); swap32(pf[1], pf[3]);
      swap16(pf[0], pf[1]); swap16(pf[2], pf[3]);
      return u32x4{__float_as_uint(pf[0]), __float_as_uint(pf[1]), __float_as_uint(pf[2]), __float_as_uint(pf[3])};
    };
    SCHED0();
#pragma unroll
    for (int h = 0; h < 4; ++h) {
      v[0][h][0] = fmx(fmx3(sp[0][h][0], sp[0][h][1], sc[0][h][0]), sc[0][h][1]);
      v[0][h][1] = fmx(fmx3(sp[0][h][2], sp[0][h][3], sc[0][h][2]), sc[0][h][3]);
    }
    pk[1] = softmax(v[1], __uint_as_float(gz[1]));
#pragma unroll
    for (int h = 0; h < 4; ++h) { PIN(v[0][h][0]); PIN(v[0][h][1]); }
    SCHED0();
    pv_store(pk[1], Vb, sel, vmain, vleft, ors, osoff1, oloff1, g0);
    pk[0] = softmax(v[0], __uint_as_float(gz[0]));
    SCHED0();
    pv_store(pk[0], Va, sel, vmain, vleft, ors, osoff, oloff, g0);
    STAMP(25);
    WAIT_VM0();
    if (lane == 0) __hip_atomic_store(myflag, tag, RLX_AGENT);
    STAMP(26);
    RSTAMP(61);
  }
  BARRIER();
  if (!act2) {
    if (bid == 255) {
      if (wave < 4) {
        const int b = wave * 64 + lane;
        GAS u64* fp = (GAS u64*)(ws + FLAG_OFF + (((b & 15) >> 2) * 64 + (b >> 4) * 4) * FS1 + (b & 3) * FS2);
#pragma clang loop unroll(disable)
        for (unsigned spins = 0; spins < (1u << 17); ++spins) {
          u64 v = __hip_atomic_load(fp, RLX_AGENT);
          if (__all(v == tag)) break;
          __builtin_amdgcn_s_sleep(8);
        }
      }
      BARRIER();
      if (tid == 0) __hip_atomic_store((GAS u64*)(ws + EPOCH_OFF), tag, RLX_AGENT);
    }
    return;
  }
  if (cw == 3) {
#pragma unroll
    for (int j = 0; j < 4; ++j) { wo_piece(3, 7, j); wo_piece(3, 8, j); }
  }
  if (wave < 4) phase2<2>(L, ws, ors, tag, rt * 5 + 3, 48, cw, rot2, lane STAMP_PASS);
  else phase2<3>(L, ws, ors, tag, rt * 5, 0, cw, rot2, lane STAMP_PASS);
  STAMP(30);
  if (wave < 5 && rt * 5 + wave < 128) {
    f32x4 s[2][2];
#pragma unroll
    for (int it = 0; it < 2; ++it) {
      const int r = it * 8 + vl;
      const unsigned a0 = L + (wave * 16 + r) * 256 + ((((lane & 7) * 2) ^ r) * 16);
#pragma unroll
      for (int w = 0; w < 4; ++w) {
        f32x4 u0 = *(LDSP f32x4*)(size_t)(a0 + w * 36864), u1 = *(LDSP f32x4*)(size_t)((a0 ^ 16) + w * 36864);
        s[it][0] = w ? s[it][0] + u0 : u0; s[it][1] = w ? s[it][1] + u1 : u1;
      }
    }
#pragma unroll
    for (int it = 0; it < 2; ++it) {
      u32x4 o = {cvtpk(s[it][0][0], s[it][0][1]), cvtpk(s[it][0][2], s[it][0][3]), cvtpk(s[it][1][0], s[it][1][1]), cvtpk(s[it][1][2], s[it][1][3])};
      *(u32x4*)((char*)out + (size_t)(rt * 80 + wave * 16 + it * 8 + vl) * XROW + ct * 128 + (lane & 7) * 16) = o;
    }
  }
  STAMP(31);
  RSTAMP(63);
  STAMP_OUT();
}

extern "C" void kernel_launch(void* const* d_in, const int* in_sizes, int n_in, void* d_out, int out_size, void* d_ws, size_t ws_size,
                              hipStream_t stream) {
  fused_kernel<<<256, 512, 0, stream>>>((const u16*)d_in[0], (const u16*)d_in[1], (const u16*)d_in[4], (const u16*)d_in[2], (const u16*)d_in[5],
                                        (const u16*)d_in[6], (char*)d_ws, (u16*)d_out);
}
```

```cpp
#define ROT 10
#include <hip/hip_runtime.h>
#include <hip/hip_bf16.h>
#include <stdint.h>

typedef unsigned short u16;
typedef unsigned long long u64;
typedef short s16x4 __attribute__((ext_vector_type(4)));
typedef float f32x2 __attribute__((ext_vector_type(2)));
typedef float f32x4 __attribute__((ext_vector_type(4)));
typedef unsigned u32x2 __attribute__((ext_vector_type(2)));
typedef unsigned u32x4 __attribute__((ext_vector_type(4)));
typedef u32x4 frag;
typedef __bf16 bf16x2 __attribute__((ext_vector_type(2)));
typedef __bf16 bfx8 __attribute__((ext_vector_type(8)));
#define LDSP __attribute__((address_space(3)))
#define GAS __attribute__((address_space(1)))
#define DI __device__ __forceinline__

constexpr int WROW = 3456;
constexpr int XROW = 1152;
constexpr int PITCH = 432;
constexpr int KO = 32 * PITCH;
constexpr int STG_W = 64 * PITCH;
constexpr int STG = STG_W + 144 * 128;
constexpr int G_OFF = 3 * STG;
constexpr int SCR_OFF = G_OFF + 4608;
constexpr int EX_OFF = 143360;
constexpr int LDS_BYTES = 163840;
constexpr int O_BYTES = 128 * 36 * 1024;
constexpr int FLAG_OFF = 8 << 20;
constexpr int EPOCH_OFF = FLAG_OFF + (1 << 20);
#define PRE0 5
#define NBX 22
#define FS1 128
#define FS2 8
#define PSLEEP 1
#define CODEPF 17792
#define NB1 5

#define MFMA(a, b, c) __builtin_amdgcn_mfma_f32_16x16x32_bf16(__builtin_bit_cast(bfx8, (a)), __builtin_bit_cast(bfx8, (b)), (c), 0, 0, 0)
#define RLX_AGENT __ATOMIC_RELAXED, __HIP_MEMORY_SCOPE_AGENT
#define HOFF(h) (36 * (h) + ((h) & 1) * 4)

DI void glds16(unsigned voff, const void* sbase, unsigned lds_addr) {
  asm volatile("s_mov_b32 m0, %2\n\ts_nop 0\n\tglobal_load_lds_dwordx4 %0, %1" ::"v"(voff), "s"(sbase), "s"(lds_addr) : "memory");
}
DI u32x2 trd(unsigned a) { return __builtin_bit_cast(u32x2, __builtin_amdgcn_ds_read_tr16_b64_v4i16((LDSP s16x4*)(size_t)a)); }
DI frag ldW(unsigned a0, unsigned a1) {
  u32x2 lo = trd(a0), hi = trd(a1);
  return __builtin_shufflevector(lo, hi, 0, 1, 2, 3);
}
DI frag ld128(unsigned a) { return *(LDSP frag*)(size_t)a; }
DI u32x2 ld64(unsigned a) { return *(LDSP u32x2*)(size_t)a; }
DI void st64(unsigned a, u32x2 v) { *(LDSP u32x2*)(size_t)a = v; }
DI unsigned cvtpk(float a, float b) {
  f32x2 f = {a, b};
  return __builtin_bit_cast(unsigned, __builtin_convertvector(f, bf16x2));
}
DI u32x2 pack4(f32x4 a) { return u32x2{cvtpk(a[0], a[1]), cvtpk(a[2], a[3])}; }
DI frag cat(u32x2 a, u32x2 b) { return __builtin_shufflevector(a, b, 0, 1, 2, 3); }
DI void swap32(float& a, float& b) {
  auto r = __builtin_amdgcn_permlane32_swap(__float_as_uint(a), __float_as_uint(b), false, false);
  a = __uint_as_float(r[0]); b = __uint_as_float(r[1]);
}
DI void swap16(float& a, float& b) {
  auto r = __builtin_amdgcn_permlane16_swap(__float_as_uint(a), __float_as_uint(b), false, false);
  a = __uint_as_float(r[0]); b = __uint_as_float(r[1]);
}
DI float fmx(float a, float b) { return __builtin_elementwise_maximum(a, b); }
DI float fmx3(float a, float b, float c) { return fmx(fmx(a, b), c); }
#define STAMP(i)
#define RSTAMP(i)
#define STAMP_OUT()
#define STAMP_ARG
#define STAMP_PASS
#define STAMP_DECL
#define SGB(m, n) __builtin_amdgcn_sched_group_barrier(m, n, 0)
#define SCHED0() __builtin_amdgcn_sched_barrier(0)
#define WAIT_LGKM0() do { __builtin_amdgcn_s_waitcnt(0xC07F); asm volatile("" ::: "memory"); } while (0)
#define WAIT_VM0() asm volatile("s_waitcnt vmcnt(0)" ::: "memory")
#define BARRIER() __builtin_amdgcn_s_barrier()
#define PIN(x) asm volatile("" : "+v"(x))
#define TAG_SCALAR() do { unsigned lo_ = __builtin_amdgcn_readfirstlane((unsigned)tag), hi_ = __builtin_amdgcn_readfirstlane((unsigned)(tag >> 32)); \
    tag = (((u64)hi_ << 32) | lo_) + 1; asm volatile("" : "+s"(tag)); } while (0)
#define PINS(x) asm volatile("" : "+s"(x))
#define WR2(addr, a, b, o0, o1) asm volatile("ds_write2st64_b64 %0, %1, %2 offset0:%3 offset1:%4" ::"v"(addr), "v"(a), "v"(b), "i"(o0), "i"(o1) : "memory")

DI void pv_store(u32x4 pk, const frag (&V)[5], u32x4 sel, unsigned vmain, unsigned vleft, __amdgpu_buffer_rsrc_t ors, unsigned so, unsigned sl, bool g0) {
  const f32x4 z = {0.f, 0.f, 0.f, 0.f};
  frag P[4];
  f32x4 o[4], os[4];
  auto mkP = [&](int h) {
    unsigned u = pk[h];
    P[h] = frag{__builtin_amdgcn_perm(u, u, sel[0]), __builtin_amdgcn_perm(u, u, sel[1]), __builtin_amdgcn_perm(u, u, sel[2]), __builtin_amdgcn_perm(u, u, sel[3])};
    PIN(P[h]);
  };
  mkP(0); mkP(1);
  SCHED0();
  u32x4 f0, f1;
  os[0] = MFMA(V[4], P[0], z); PIN(os[0]); o[0] = MFMA(V[0], P[0], z); PIN(o[0]); mkP(2); SCHED0();
  os[1] = MFMA(V[4], P[1], z); PIN(os[1]); o[1] = MFMA(V[1], P[1], z); PIN(o[1]); mkP(3); SCHED0();
  os[2] = MFMA(V[4], P[2], z); PIN(os[2]); os[3] = MFMA(V[4], P[3], z); PIN(os[3]);
  o[2] = MFMA(V[2], P[2], z); PIN(o[2]); o[3] = MFMA(V[3], P[3], z); PIN(o[3]); SCHED0();
  f0 = u32x4{cvtpk(o[0][0], o[0][1]), cvtpk(o[0][2], o[0][3]), cvtpk(o[1][0], o[1][1]), cvtpk(o[1][2], o[1][3])};
  PIN(f0); SCHED0();
  __builtin_amdgcn_raw_buffer_store_b128(f0, ors, vmain, so, 16);
  SCHED0();
  u32x2 f2 = {cvtpk(g0 ? os[0][0] : os[2][0], g0 ? os[0][1] : os[2][1]), cvtpk(g0 ? os[1][2] : os[3][2], g0 ? os[1][3] : os[3][3])};
  PIN(f2); SCHED0();
  f1 = u32x4{cvtpk(o[2][0], o[2][1]), cvtpk(o[2][2], o[2][3]), cvtpk(o[3][0], o[3][1]), cvtpk(o[3][2], o[3][3])};
  PIN(f1); PIN(f0); SCHED0();
  __builtin_amdgcn_raw_buffer_store_b128(f1, ors, vmain + 1024, so, 16);
  __builtin_amdgcn_raw_buffer_store_b64(f2, ors, vleft, sl, 16);
  SCHED0();
}

template <int MT>
DI void phase2(unsigned L, char* ws, __amdgpu_buffer_rsrc_t ors, u64 tag, int tt0, int mrow, int kq, int rot2, int lane STAMP_ARG) {
  const int g = lane >> 4, q = (lane >> 2) & 3, p = lane & 3, c = lane & 15;
  const unsigned sw = (2 * g + (q >> 1)) & 3;
  unsigned bw[4];
#pragma unroll
  for (int n = 0; n < 4; ++n) bw[n] = L + kq * 36864 + (4 * g + q) * 128 + 8 * p + ((n ^ sw) * 32);
  frag wf[9][4];
  auto rdw = [&](int i) {
#pragma unroll
    for (int n = 0; n < 4; ++n) wf[i][n] = ldW(bw[n] + i * 4096, bw[n] + i * 4096 + 2048);
  };
  rdw(0); rdw(1); rdw(2); rdw(3); rdw(4);
  SCHED0();
  unsigned so[9], vo[MT];
#pragma unroll
  for (int i = 0; i < 9; ++i) so[i] = (i < 8 ? 8 * kq + ((i + rot2) & 7) : 32 + kq) * 1024;
#pragma unroll
  for (int m = 0; m < MT; ++m) { int tt = tt0 + m; tt = tt > 127 ? 127 : tt; vo[m] = lane * 16 + tt * 36864; }
  int tf = (lane & 4) ? tt0 + MT - 1 : tt0; tf = tf > 127 ? 127 : tf;
  GAS u64* fp = (GAS u64*)(ws + FLAG_OFF + (kq * 64 + (tf >> 1)) * FS1 + (lane & 3) * FS2);
  unsigned rb[4];
#pragma unroll
  for (int n = 0; n < 4; ++n) rb[n] = L + kq * 36864 + (mrow + c) * 256 + (((4 * n + g) ^ c) * 16);
#pragma unroll
  for (int i = 0; i < 9; ++i) PINS(so[i]);
#pragma unroll
  for (int m = 0; m < MT; ++m) PIN(vo[m]);
#pragma unroll
  for (int n = 0; n < 4; ++n) PIN(rb[n]);
  PIN(fp);
  WAIT_LGKM0();
  BARRIER();
  STAMP(27);
#pragma clang loop unroll(disable)
  for (unsigned spins = 0; spins < (1u << 17); ++spins) {
    u64 v = __hip_atomic_load(fp, RLX_AGENT);
    if (__all(v == tag)) break;
    __builtin_amdgcn_s_sleep(PSLEEP);
  }
  __builtin_amdgcn_fence(__ATOMIC_ACQUIRE, "wavefront");
  asm volatile("" ::: "memory");
  STAMP(28);
  RSTAMP(62);
  frag of[9][MT];
  constexpr int WIN = 2;
#pragma unroll
  for (int i = 0; i < WIN; ++i)
#pragma unroll
    for (int m = 0; m < MT; ++m) { of[i][m] = __builtin_amdgcn_raw_buffer_load_b128(ors, vo[m], so[i], 16); SCHED0(); }
  f32x4 acc[MT][4];
#pragma unroll
  for (int i = 0; i < 9; ++i) {
    const bool rd = i >= 2 && i + 3 < 9;
    if (rd) rdw(i + 3);
#pragma unroll
    for (int m = 0; m < MT; ++m) {
#pragma unroll
      for (int n = 0; n < 4; ++n) acc[m][n] = MFMA(wf[i][n], of[i][m], i == 0 ? (f32x4{0, 0, 0, 0}) : acc[m][n]);
      if (i + WIN < 9) of[i + WIN][m] = __builtin_amdgcn_raw_buffer_load_b128(ors, vo[m], so[i + WIN], 16);
    }
#pragma unroll
    for (int m = 0; m < MT; ++m) {
#pragma unroll
      for (int n = 0; n < 4; ++n) { SGB(0x8, 1); if (m < 2 && rd) SGB(0x100, 1); }
      if (i + WIN < 9) SGB(0x20, 1);
    }
    SCHED0();
  }
  STAMP(29);
#pragma unroll
  for (int m = 0; m < MT; ++m)
#pragma unroll
    for (int n = 0; n < 4; ++n) *(LDSP f32x4*)(size_t)(rb[n] + m * 4096) = acc[m][n];
  WAIT_LGKM0();
  BARRIER();
}

__global__ __launch_bounds__(512, 2) void fused_kernel(const u16* __restrict__ x, const u16* __restrict__ wA, const u16* __restrict__ wB,
                                                        const u16* __restrict__ woA, const u16* __restrict__ woB, const u16* __restrict__ gw,
                                                        char* __restrict__ ws, u16* __restrict__ out) {
  __shared__ __attribute__((aligned(1024))) char lds[LDS_BYTES];
  const unsigned L = (unsigned)(size_t)(LDSP char*)lds;
  const int bid = blockIdx.x;
  const int xcd = bid & 7, idx = bid >> 3;
  const int tl = idx & 7, gl = idx >> 3;
  const int tile = (xcd & 1) * 8 + tl;
  const int grp = (xcd >> 1) * 4 + gl;
  int rot = ROT == 1 ? tl + 2 * gl : ROT == 2 ? (tl >> 1) * 2 + 2 * gl : ROT == 6 ? (tl % 3) * 3 + (gl % 3) * 3 : ROT == 7 ? (((tl >> 1) + gl) & 3) * 2 + (tl & 1 & (gl >> 1))
          : ROT == 8 ? (((tl + gl) & 3) * 9) >> 2 : ROT == 9 ? ((tl + gl + (xcd >> 1)) % 3) * 3 : ROT == 10 ? (((tl + gl) & 3) * 9 >> 2) + (xcd & 1) : 0;
  if (rot >= 9) rot -= 9;
  const int tid = threadIdx.x, lane = tid & 63;
  const int wave = __builtin_amdgcn_readfirstlane(tid >> 6);
  const int vl = lane >> 3;
  STAMP_DECL;
  STAMP(0);
  RSTAMP(60);
  if (tid < 6) {
    u64 pc0;
    asm volatile("s_getpc_b64 %0" : "=s"(pc0));
    unsigned ln = idx * 6 + tid;
    ln = ln < CODEPF / 128 ? ln : 0;
    asm volatile("s_mov_b32 m0, %2\n\ts_nop 0\n\tglobal_load_lds_dword %0, %1" ::"v"(ln * 128), "s"(pc0 & ~127ull), "s"(L + SCR_OFF) : "memory");
  }
  asm volatile("" ::"s"(x), "s"(wA), "s"(wB), "s"(woA), "s"(woB), "s"(gw), "s"(ws), "s"(out));
  u64 tag;
  u32x2 gv = {0u, 0u};
  if (tid < 144) gv = *(const u32x2*)(gw + tid * 4);
  const int lin0 = xcd * 30 + idx;
  const bool act2 = idx < 30 && lin0 < 234;
  const int lin = act2 ? lin0 : 0;
  const int rt = (lin * 57) >> 9, ct = lin - rt * 9;
  const int rot2 = 3 * (rt - ((rt * 171) >> 9) * 3);
  const float sg = (grp >= 8) ? 1.4426950408889634f : -1.4426950408889634f;
  const unsigned wsch = ((((lane & 7) >> 1) ^ ((vl >> 1) & 3)) << 5) | ((lane & 1) << 4);
  const unsigned offn = vl * XROW + wsch, offl = ((vl >> 2) * 72 + (vl & 3)) * XROW + wsch;
  auto wo_piece = [&](int kq, int i, int j) {
    const char* b = (const char*)(kq >= 2 ? woB : woA) + ct * 128 + (kq & 1) * (288 * XROW);
    if (i == 8) b += ((j & 1) * 144 + ((j >> 1) ? 52 : 16)) * XROW;
    else b += (((i + rot2) & 7) * 36 + (j >> 1) * 20 + 8 * (j & 1)) * XROW;
    glds16(i == 8 ? offl : offn, b, L + (kq * 9 + i) * 4096 + j * 1024);
  };
  const __amdgpu_buffer_rsrc_t ors = __builtin_amdgcn_make_buffer_rsrc(ws, 0, O_BYTES, 0x00020000);
  const int g = lane >> 4, q = (lane >> 2) & 3, p = lane & 3, c = lane & 15;
  const int cw = wave & 3;

  if (__builtin_expect(wave >= 4, 1)) {
    __builtin_amdgcn_s_setprio(2);
    const int lw = cw, tl256 = tid - 256;
    const unsigned offx = (tile * 128 + 8 * lw + vl) * XROW + ((lane & 7) ^ ((4 * lw + (vl >> 1)) & 7)) * 16;
    const char* w = (const char*)((grp >= 8) ? wB : wA) + (grp & 7) * 144;
    unsigned offw[7], offh;
    auto kchunk = [&](int st) { int kc = st + rot; return kc >= 9 ? kc - 9 : kc; };
    auto issueX = [&](int kc, unsigned sb, int jj) { glds16(offx, (const char*)x + kc * 128 + jj * (32 * XROW), sb + STG_W + (2 + lw + 4 * jj) * 1024); };
    auto issueW = [&](int kc, unsigned sb, int j) { glds16(offw[j], w + kc * (64 * WROW), sb + (lw + 4 * j) * 1024); };
    auto issue6 = [&](int kc, unsigned sb) {
      if (lw < 3) issueW(kc, sb, 6);
      else { glds16(offw[6], (const char*)x + kc * 128, sb + STG_W); glds16(offh, (const char*)x + kc * 128, sb + STG_W + 1024); }
    };
    const int kc0 = rot, kc1 = kchunk(1);
    const unsigned sb0 = L, sb1 = L + STG;
    auto calcw = [&](int j) {
      unsigned qq = 256 * j + tl256;
      unsigned u = (qq * 2428u) >> 16, cc = qq - u * 27;
      unsigned row = (u & 0x38) | ((u & 1) << 2) | ((u >> 1) & 3);
      offw[j] = row * WROW + cc * 16 + ((cc * 57u) >> 9) * 1008;
    };
    issueX(kc0, sb0, 0);
#pragma unroll
    for (int j = 0; j < 6; ++j) {
      SCHED0();
      calcw(j);
      SCHED0();
      issueW(kc0, sb0, j);
      if (j < 3) issueX(kc0, sb0, j + 1);
    }
    calcw(6);
    {
      int t0 = tile * 128 - 16 + vl, t1 = t0 + 8;
      t0 = t0 < 0 ? 0 : t0; t1 = t1 < 0 ? 0 : t1;
      unsigned h0 = t0 * XROW + ((lane & 7) ^ (vl >> 1)) * 16;
      offh = t1 * XROW + ((lane & 7) ^ (4 + (vl >> 1))) * 16;
      offw[6] = lw == 3 ? h0 : offw[6];
    }
    issue6(kc0, sb0);
    STAMP(1);
#pragma unroll
    for (int j = 0; j < PRE0; ++j) issueW(kc1, sb1, j);
    STAMP(2);
    f32x4 he = {0, 0, 0, 0};
    const unsigned aE = L + (8 * g + 2 * q) * PITCH + (lw == 0 ? 144 : 288) + ((p & 1) ? 104 : 32);
    const unsigned aX = L + STG_W + c * 128 + ((g ^ (c >> 1)) * 16);
    frag fe[2], fx[2];
    auto halo_rd = [&](unsigned so) {
      if (lw < 2) {
#pragma unroll
        for (int ks = 0; ks < 2; ++ks) {
          fx[ks] = ld128((aX ^ (ks * 64)) + so);
          fe[ks] = ldW(aE + so + ks * KO, aE + so + ks * KO + PITCH);
        }
      }
    };
    auto halo_mma = [&]() {
      if (lw == 0) { he = MFMA(fe[0], fx[0], he); he = MFMA(fe[1], fx[1], he); }
      if (lw == 1) { he = MFMA(fx[0], fe[0], he); he = MFMA(fx[1], fe[1], he); }
    };
    int slot = 0, kn = kc1;
    asm volatile("s_waitcnt vmcnt(%0)" ::"i"(PRE0) : "memory");
#pragma clang loop unroll(disable)
    for (int st = 0; st < 8; ++st) {
      BARRIER();
      STAMP(3 + st);
      if (st == 0) {
#pragma unroll
        for (int j = PRE0; j < 6; ++j) issueW(kc1, sb1, j);
        tag = __hip_atomic_load((GAS u64*)(ws + EPOCH_OFF), RLX_AGENT);
      }
      const unsigned b1 = L + (slot == 2 ? 0 : slot + 1) * STG, b2 = L + (slot == 0 ? 2 : slot - 1) * STG;
      issue6(kn, b1);
      issueX(kn, b1, 0);
      halo_rd(slot * STG);
      issueX(kn, b1, 1); issueX(kn, b1, 2); issueX(kn, b1, 3);
      SCHED0();
      halo_mma();
      SCHED0();
      kn = kn == 8 ? 0 : kn + 1;
      if (st < 7) {
#pragma unroll
        for (int j = 0; j < 6; ++j) issueW(kn, b2, j);
      } else {
#pragma unroll
        for (int I = 0; I < 6; ++I) wo_piece(0, I, lw);
      }
      slot = slot == 2 ? 0 : slot + 1;
      WAIT_LGKM0(); asm volatile("s_waitcnt vmcnt(6)" ::: "memory");
    }
    BARRIER();
    STAMP(11);
    halo_rd(2 * STG);
    wo_piece(0, 6, lw); wo_piece(0, 7, lw);
    SCHED0();
    halo_mma();
    SCHED0();
    if (lw < 2) {
      u32x2 v = pack4(he);
      if (lw == 0 && tile == 0) v = u32x2{0u, 0u};
      st64(L + EX_OFF + lane * 8 + (lw == 0 ? 4 : 9) * 512, v);
    }
    __builtin_amdgcn_s_setprio(0);
#pragma unroll
    for (int I = 8; I < NBX; ++I) wo_piece(I / 9, I % 9, lw);
    WAIT_LGKM0();
    STAMP(12);
    BARRIER();
    STAMP(13);
#pragma unroll
    for (int I = NBX; I < 34; ++I) wo_piece(I / 9, I % 9, lw);
    WAIT_VM0();
    TAG_SCALAR();
    STAMP(14);
  } else {
    const unsigned wrow = L + (8 * g + 2 * q) * PITCH;
    tag = __hip_atomic_load((GAS u64*)(ws + EPOCH_OFF), RLX_AGENT);
    f32x4 maskP, maskC, maskP0;
    {
      const float ninf = -__builtin_inff();
#pragma unroll
      for (int r = 0; r < 4; ++r) {
        bool pm = (r & 1) == (c & 1);
        maskC[r] = (pm && 4 * g + r <= c) ? 0.f : ninf;
        maskP[r] = (pm && 4 * g + r > c) ? 0.f : ninf;
      }
      maskP0 = maskP + ((tile == 0 && wave == 0) ? -998244352.f * 4.242640687119285f : 0.f);
      asm volatile("" : "+v"(maskP), "+v"(maskC), "+v"(maskP0));
    }
    u32x4 sel;
    {
      const int par = c & 1;
      const bool cur1 = 4 * g + par <= c, cur2 = 4 * g + 2 + par <= c;
      const unsigned s1 = par ? 0x01000c0cu : 0x0c0c0100u;
      const unsigned s2 = par ? 0x03020c0cu : 0x0c0c0302u;
      sel[0] = cur1 ? 0x0c0c0c0cu : s1;
      sel[1] = cur2 ? 0x0c0c0c0cu : s2;
      sel[2] = cur1 ? s1 : 0x0c0c0c0cu;
      sel[3] = cur2 ? s2 : 0x0c0c0c0cu;
    }
    unsigned vmain = lane * 16, vleft = lane < 32 ? c * 16 + g * 8 : 0x40000000u;
    unsigned osoff = (tile * 8 + 2 * wave) * 36864 + grp * 2048, oloff = (tile * 8 + 2 * wave) * 36864 + 32768 + grp * 256;
    GAS u64* myflag = (GAS u64*)(ws + FLAG_OFF + ((grp >> 2) * 64 + tile * 4 + cw) * FS1 + (grp & 3) * FS2);
    PINS(myflag);
    unsigned osoff1 = osoff + 36864, oloff1 = oloff + 36864;
    PINS(osoff); PINS(oloff); PINS(osoff1); PINS(oloff1);
    asm volatile("" : "+v"(sel), "+v"(vmain), "+v"(vleft));
    f32x4 aK[2][4], aQ[2][4], aKQ[2], aV[2][5], hk = {0, 0, 0, 0}, hv = {0, 0, 0, 0};
#pragma unroll
    for (int t = 0; t < 2; ++t) {
#pragma unroll
      for (int f = 0; f < 4; ++f) { aK[t][f] = f32x4{0, 0, 0, 0}; aQ[t][f] = f32x4{0, 0, 0, 0}; }
      aKQ[t] = f32x4{0, 0, 0, 0};
#pragma unroll
      for (int f = 0; f < 5; ++f) aV[t][f] = f32x4{0, 0, 0, 0};
    }
    unsigned cW = wrow + 8 * p;
    unsigned cKQ = wrow + (p < 2 ? 144 : 0) + ((p & 1) ? 104 : 32);
    unsigned cXh0 = L + STG_W + c * 128 + ((g ^ (c >> 1)) * 16), cXh1 = cXh0 ^ 64;
    unsigned cX0 = cXh0 + (1 + 2 * wave) * 2048, cX1 = cX0 ^ 64;
    unsigned cH = cW + 144 + HOFF(wave);
    unsigned cV[4];
#pragma unroll
    for (int i = 0; i < 4; ++i)
      cV[i] = p < 2 ? wrow + 288 + (p ? 104 : 32) + (i >> 1) * KO + (i & 1) * PITCH : L + G_OFF + rot * 512 + (8 * g + q) * 8 + (i >> 1) * 256 + (i & 1) * 32;
    frag cv[5], xb[2];
#pragma unroll
    for (int f = 0; f < 5; ++f) cv[f] = frag{0, 0, 0, 0};
    xb[0] = frag{0, 0, 0, 0}; xb[1] = frag{0, 0, 0, 0};
    int slot = 0, kc = rot;
    unsigned exr = L + EX_OFF + wave * 5120 + lane * 8, exh = L + EX_OFF + wave * 512 + lane * 8;
    unsigned exgw = L + SCR_OFF + wave * 128 + g * 16, exgr = L + SCR_OFF + wave * 128 + c * 4;
    asm volatile("" : "+v"(aK[0][0]), "+v"(aK[0][1]), "+v"(aK[0][2]), "+v"(aK[0][3]), "+v"(aK[1][0]), "+v"(aK[1][1]), "+v"(aK[1][2]), "+v"(aK[1][3]),
                      "+v"(aQ[0][0]), "+v"(aQ[0][1]), "+v"(aQ[0][2]), "+v"(aQ[0][3]), "+v"(aQ[1][0]), "+v"(aQ[1][1]), "+v"(aQ[1][2]), "+v"(aQ[1][3]),
                      "+v"(aKQ[0]), "+v"(aKQ[1]), "+v"(hk), "+v"(hv), "+v"(xb[0]), "+v"(xb[1]), "+v"(exr), "+v"(exh), "+v"(exgw), "+v"(exgr));
    asm volatile("" : "+v"(aV[0][0]), "+v"(aV[0][1]), "+v"(aV[0][2]), "+v"(aV[0][3]), "+v"(aV[0][4]), "+v"(aV[1][0]), "+v"(aV[1][1]), "+v"(aV[1][2]),
                      "+v"(aV[1][3]), "+v"(aV[1][4]), "+v"(cv[0]), "+v"(cv[1]), "+v"(cv[2]), "+v"(cv[3]), "+v"(cv[4]),
                      "+v"(cW), "+v"(cKQ), "+v"(cX0), "+v"(cX1), "+v"(cH), "+v"(cXh0), "+v"(cXh1), "+v"(cV[0]), "+v"(cV[1]), "+v"(cV[2]), "+v"(cV[3]));
    SCHED0();
    STAMP(1);
    if (tid < 144) {
      unsigned a0 = __builtin_amdgcn_perm(gv[0], gv[0], 0x01000100), a1 = __builtin_amdgcn_perm(gv[0], gv[0], 0x03020302);
      unsigned a2 = __builtin_amdgcn_perm(gv[1], gv[1], 0x01000100), a3 = __builtin_amdgcn_perm(gv[1], gv[1], 0x03020302);
      *(LDSP u32x4*)(size_t)(L + G_OFF + tid * 32) = u32x4{a0, a0, a1, a1};
      *(LDSP u32x4*)(size_t)(L + G_OFF + tid * 32 + 16) = u32x4{a2, a2, a3, a3};
    }
    STAMP(2);
    WAIT_LGKM0();
#pragma clang loop unroll(disable)
    for (int st = 0; st < 9; ++st) {
      BARRIER();
      SCHED0();
      frag xa[2], W0[14], W1[9], xh[2], Hk[2], Hv[2];
      xa[0] = ld128(cX0); xa[1] = ld128(cX0 + 2048);
#pragma unroll
      for (int f = 0; f < 4; ++f) W0[f] = ldW(cW + 144 + HOFF(f), cW + 144 + HOFF(f) + PITCH);
      W0[4] = ldW(cKQ, cKQ + PITCH);
#pragma unroll
      for (int f = 0; f < 5; ++f) { aV[0][f] = MFMA(xb[0], cv[f], aV[0][f]); aV[1][f] = MFMA(xb[1], cv[f], aV[1][f]); }
#pragma unroll
      for (int f = 0; f < 4; ++f) W0[5 + f] = ldW(cW + HOFF(f), cW + HOFF(f) + PITCH);
      xh[0] = ld128(cXh0);
      Hk[0] = ldW(cH, cH + PITCH); Hv[0] = ldW(cH + 144, cH + 144 + PITCH);
#pragma unroll
      for (int f = 0; f < 4; ++f) W0[9 + f] = ldW(cW + 288 + HOFF(f), cW + 288 + HOFF(f) + PITCH);
      W0[13] = ldW(cV[0], cV[1]);
      xb[0] = ld128(cX1); xb[1] = ld128(cX1 + 2048);
#pragma unroll
      for (int f = 0; f < 4; ++f) W1[f] = ldW(cW + KO + 144 + HOFF(f), cW + KO + 144 + HOFF(f) + PITCH);
      W1[4] = ldW(cKQ + KO, cKQ + KO + PITCH);
      xh[1] = ld128(cXh1);
      Hk[1] = ldW(cH + KO, cH + KO + PITCH); Hv[1] = ldW(cH + KO + 144, cH + KO + 144 + PITCH);
#pragma unroll
      for (int f = 0; f < 4; ++f) W1[5 + f] = ldW(cW + KO + HOFF(f), cW + KO + HOFF(f) + PITCH);
#pragma unroll
      for (int f = 0; f < 4; ++f) cv[f] = ldW(cW + KO + 288 + HOFF(f), cW + KO + 288 + HOFF(f) + PITCH);
      cv[4] = ldW(cV[2], cV[3]);
#pragma unroll
      for (int f = 0; f < 4; ++f) { aK[0][f] = MFMA(W0[f], xa[0], aK[0][f]); aK[1][f] = MFMA(W0[f], xa[1], aK[1][f]); }
      aKQ[0] = MFMA(W0[4], xa[0], aKQ[0]); aKQ[1] = MFMA(W0[4], xa[1], aKQ[1]);
#pragma unroll
      for (int f = 0; f < 4; ++f) { aQ[0][f] = MFMA(W0[5 + f], xa[0], aQ[0][f]); aQ[1][f] = MFMA(W0[5 + f], xa[1], aQ[1][f]); }
      hk = MFMA(Hk[0], xh[0], hk); hv = MFMA(xh[0], Hv[0], hv);
#pragma unroll
      for (int f = 0; f < 5; ++f) { aV[0][f] = MFMA(xa[0], W0[9 + f], aV[0][f]); aV[1][f] = MFMA(xa[1], W0[9 + f], aV[1][f]); }
#pragma unroll
      for (int f = 0; f < 4; ++f) { aK[0][f] = MFMA(W1[f], xb[0], aK[0][f]); aK[1][f] = MFMA(W1[f], xb[1], aK[1][f]); }
      aKQ[0] = MFMA(W1[4], xb[0], aKQ[0]); aKQ[1] = MFMA(W1[4], xb[1], aKQ[1]);
      hk = MFMA(Hk[1], xh[1], hk); hv = MFMA(xh[1], Hv[1], hv);
#pragma unroll
      for (int f = 0; f < 4; ++f) { aQ[0][f] = MFMA(W1[5 + f], xb[0], aQ[0][f]); aQ[1][f] = MFMA(W1[5 + f], xb[1], aQ[1][f]); }
      const int ds = slot == 2 ? -2 * STG : STG, dg = kc == 8 ? -8 * 512 : 512;
      slot = slot == 2 ? 0 : slot + 1;
      kc = kc == 8 ? 0 : kc + 1;
      const int dv = p < 2 ? ds : dg;
      cW += ds; cKQ += ds; cX0 += ds; cX1 += ds; cH += ds; cXh0 += ds; cXh1 += ds;
#pragma unroll
      for (int i = 0; i < 4; ++i) cV[i] += dv;
      SGB(0x100, 2);
#pragma unroll
      for (int i = 0; i < 54; ++i) {
        SGB(0x8, 1);
        if (i < 2 || (i >= 4 && i <= 48 && (i % 4) == 0)) SGB(0x100, 2); else SGB(0x100, 1);
        if (i >= 41 && i < 53 && (i % 4) != 0) SGB(0x4, 1);
      }
#pragma unroll
      for (int i = 0; i < 5; ++i) { SGB(0x8, 1); SGB(0x2, 2); }
      SGB(0x2, 4);
      SGB(0x8, 1);
      SCHED0();
      WAIT_LGKM0();
    }
    STAMP(12);
    const bool g0 = g == 0;
    frag K[3][4], Q[2][4], Vb[5], Va[5];
    u32x2 ks[2], qs[2];
    aV[1][4] = MFMA(xb[1], cv[4], aV[1][4]); PIN(aV[1][4]);
    aV[0][4] = MFMA(xb[0], cv[4], aV[0][4]); PIN(aV[0][4]);
#pragma unroll
    for (int t = 1; t >= 0; --t) {
      u32x2 kq = pack4(aKQ[t]);
      auto r0 = __builtin_amdgcn_permlane32_swap(kq[0], 0u, false, false);
      auto r1 = __builtin_amdgcn_permlane32_swap(kq[1], 0u, false, false);
      ks[t] = u32x2{r0[0], r1[0]}; qs[t] = u32x2{r0[1], r1[1]};
      PIN(ks[t]); PIN(qs[t]);
    }
    SCHED0();
#pragma unroll
    for (int f = 0; f < 4; ++f) { aV[1][f] = MFMA(xb[1], cv[f], aV[1][f]); PIN(aV[1][f]); K[2][f] = cat(pack4(aK[1][f]), ks[1]); PIN(K[2][f]); SCHED0(); }
    {
      u32x2 h = pack4(hk), hh = pack4(hv);
      if (tile == 0) h = u32x2{0u, 0u};
      WR2(exh, h, hh, 0, 5);
    }
    if (c >= 8) { *(LDSP f32x4*)(size_t)(exgw) = aV[0][4]; *(LDSP f32x4*)(size_t)(exgw + 64) = aV[1][4]; }
    SCHED0();
#pragma unroll
    for (int f = 0; f < 4; ++f) {
      aV[0][f] = MFMA(xb[0], cv[f], aV[0][f]); PIN(aV[0][f]);
      u32x2 v = pack4(aV[1][f]); Vb[f][2] = v[0]; Vb[f][3] = v[1]; PIN(Vb[f]); SCHED0();
    }
    { u32x2 v = pack4(aV[1][4]); Vb[4][2] = v[0]; Vb[4][3] = v[1]; PIN(Vb[4]); }
    if (wave < 3) {
#pragma unroll
      for (int f = 0; f < 4; ++f) WR2(exr, (u32x2{K[2][f][0], K[2][f][1]}), (u32x2{Vb[f][2], Vb[f][3]}), 10 + f, 15 + f);
      WR2(exr, ks[1], (u32x2{Vb[4][2], Vb[4][3]}), 14, 19);
    }
    SCHED0();
#pragma unroll
    for (int h = 0; h < 4; ++h) {
      K[1][h] = cat(pack4(aK[0][h]), ks[0]); PIN(K[1][h]);
      u32x2 qt = pack4(aQ[1][h]);
      unsigned e = ((h < 2) == g0) ? qs[1][h & 1] : 0u;
      Q[1][h] = (h & 1) ? frag{qt[0], qt[1], 0u, e} : frag{qt[0], qt[1], e, 0u}; PIN(Q[1][h]);
    }
    SCHED0();
    TAG_SCALAR();
    WAIT_VM0(); WAIT_LGKM0();
    STAMP(23);
    BARRIER();
    STAMP(24);
    SCHED0();
    u32x2 gz;
    asm volatile("ds_read2_b32 %0, %1 offset0:0 offset1:16" : "=v"(gz) : "v"(exgr) : "memory");
#pragma unroll
    for (int h = 0; h < 4; ++h) asm volatile("ds_read2st64_b64 %0, %1 offset0:%2 offset1:4" : "=v"(K[0][h]) : "v"(exr), "i"(h) : "memory");
    f32x4 sc[2][4], sp[2][4];
#pragma unroll
    for (int h = 0; h < 4; ++h) {
      sc[1][h] = MFMA(K[2][h], Q[1][h], maskC); PIN(sc[1][h]);
      sp[1][h] = MFMA(K[1][h], Q[1][h], maskP); PIN(sp[1][h]);
      u32x2 qt = pack4(aQ[0][h]);
      unsigned e = ((h < 2) == g0) ? qs[0][h & 1] : 0u;
      Q[0][h] = (h & 1) ? frag{qt[0], qt[1], 0u, e} : frag{qt[0], qt[1], e, 0u}; PIN(Q[0][h]);
      SCHED0();
    }
    asm volatile("s_waitcnt lgkmcnt(0)" : "+v"(gz), "+v"(K[0][0]), "+v"(K[0][1]), "+v"(K[0][2]), "+v"(K[0][3])::"memory");
    float v[2][4][2];
#pragma unroll
    for (int h = 0; h < 4; ++h) {
      sc[0][h] = MFMA(K[1][h], Q[0][h], maskC); PIN(sc[0][h]);
      sp[0][h] = MFMA(K[0][h], Q[0][h], maskP0); PIN(sp[0][h]);
      v[1][h][0] = fmx(fmx3(sp[1][h][0], sp[1][h][1], sc[1][h][0]), sc[1][h][1]); PIN(v[1][h][0]);
      v[1][h][1] = fmx(fmx3(sp[1][h][2], sp[1][h][3], sc[1][h][2]), sc[1][h][3]); PIN(v[1][h][1]);
      SCHED0();
    }
#pragma unroll
    for (int f = 0; f < 5; ++f) {
      u32x2 a = pack4(aV[0][f]); Vb[f][0] = a[0]; Vb[f][1] = a[1]; PIN(Vb[f]);
      Va[f] = __builtin_shufflevector(Vb[f], Vb[f], -1, -1, 0, 1); PIN(Va[f]);
    }
#pragma unroll
    for (int f = 0; f < 5; ++f) { u32x2 n = *(volatile LDSP u32x2*)(size_t)(exr + (5 + f) * 512); Va[f][0] = n[0]; Va[f][1] = n[1]; }
    u32x4 pk[2];
    auto softmax = [&](float (&w)[4][2], float gate_logit) {
      swap32(w[0][0], w[2][0]); swap32(w[1][0], w[3][0]); swap32(w[0][1], w[2][1]); swap32(w[1][1], w[3][1]);
      swap16(w[0][0], w[1][0]); swap16(w[2][0], w[3][0]); swap16(w[0][1], w[1][1]); swap16(w[2][1], w[3][1]);
      const float cs = 0.23570226039551584f * 1.4426950408889634f;
      float m = fmx(fmx3(fmx3(w[0][0], w[1][0], w[2][0]), w[3][0], w[0][1]), fmx3(w[1][1], w[2][1], w[3][1]));
      float mc = -m * cs;
      f32x2 e[4];
#pragma unroll
      for (int i = 0; i < 4; ++i) {
        f32x2 a = __builtin_elementwise_fma(f32x2{w[i][0], w[i][1]}, f32x2{cs, cs}, f32x2{mc, mc});
        e[i] = f32x2{__builtin_amdgcn_exp2f(a[0]), __builtin_amdgcn_exp2f(a[1])};
      }
      f32x2 s2 = (e[0] + e[1]) + (e[2] + e[3]);
      float s = s2[0] + s2[1];
      float r = __builtin_amdgcn_rcpf(__builtin_fmaf(s, __builtin_amdgcn_exp2f(gate_logit * sg), s));
      float pf[4];
#pragma unroll
      for (int i = 0; i < 4; ++i) { f32x2 pp = e[i] * r; pf[i] = __uint_as_float(cvtpk(pp[0], pp[1])); }
      swap32(pf[0], pf[2]); swap32(pf[1], pf[3]);
      swap16(pf[0], pf[1]); swap16(pf[2], pf[3]);
      return u32x4{__float_as_uint(pf[0]), __float_as_uint(pf[1]), __float_as_uint(pf[2]), __float_as_uint(pf[3])};
    };
    SCHED0();
#pragma unroll
    for (int h = 0; h < 4; ++h) {
      v[0][h][0] = fmx(fmx3(sp[0][h][0], sp[0][h][1], sc[0][h][0]), sc[0][h][1]);
      v[0][h][1] = fmx(fmx3(sp[0][h][2], sp[0][h][3], sc[0][h][2]), sc[0][h][3]);
    }
    pk[1] = softmax(v[1], __uint_as_float(gz[1]));
#pragma unroll
    for (int h = 0; h < 4; ++h) { PIN(v[0][h][0]); PIN(v[0][h][1]); }
    SCHED0();
    pv_store(pk[1], Vb, sel, vmain, vleft, ors, osoff1, oloff1, g0);
    pk[0] = softmax(v[0], __uint_as_float(gz[0]));
    SCHED0();
    pv_store(pk[0], Va, sel, vmain, vleft, ors, osoff, oloff, g0);
    STAMP(25);
    WAIT_VM0();
    if (lane == 0) __hip_atomic_store(myflag, tag, RLX_AGENT);
    STAMP(26);
    RSTAMP(61);
  }
  BARRIER();
  if (!act2) {
    if (bid == 255) {
      if (wave < 4) {
        const int b = wave * 64 + lane;
        GAS u64* fp = (GAS u64*)(ws + FLAG_OFF + (((b & 15) >> 2) * 64 + (b >> 4) * 4) * FS1 + (b & 3) * FS2);
#pragma clang loop unroll(disable)
        for (unsigned spins = 0; spins < (1u << 17); ++spins) {
          u64 v = __hip_atomic_load(fp, RLX_AGENT);
          if (__all(v == tag)) break;
          __builtin_amdgcn_s_sleep(8);
        }
      }
      BARRIER();
      if (tid == 0) __hip_atomic_store((GAS u64*)(ws + EPOCH_OFF), tag, RLX_AGENT);
    }
    return;
  }
  if (cw == 3) {
#pragma unroll
    for (int j = 0; j < 4; ++j) { wo_piece(3, 7, j); wo_piece(3, 8, j); }
  }
  if (wave < 4) phase2<2>(L, ws, ors, tag, rt * 5 + 3, 48, cw, rot2, lane STAMP_PASS);
  else phase2<3>(L, ws, ors, tag, rt * 5, 0, cw, rot2, lane STAMP_PASS);
  STAMP(30);
  if (wave < 5 && rt * 5 + wave < 128) {
    f32x4 s[2][2];
#pragma unroll
    for (int it = 0; it < 2; ++it) {
      const int r = it * 8 + vl;
      const unsigned a0 = L + (wave * 16 + r) * 256 + ((((lane & 7) * 2) ^ r) * 16);
#pragma unroll
      for (int w = 0; w < 4; ++w) {
        f32x4 u0 = *(LDSP f32x4*)(size_t)(a0 + w * 36864), u1 = *(LDSP f32x4*)(size_t)((a0 ^ 16) + w * 36864);
        s[it][0] = w ? s[it][0] + u0 : u0; s[it][1] = w ? s[it][1] + u1 : u1;
      }
    }
#pragma unroll
    for (int it = 0; it < 2; ++it) {
      u32x4 o = {cvtpk(s[it][0][0], s[it][0][1]), cvtpk(s[it][0][2], s[it][0][3]), cvtpk(s[it][1][0], s[it][1][1]), cvtpk(s[it][1][2], s[it][1][3])};
      *(u32x4*)((char*)out + (size_t)(rt * 80 + wave * 16 + it * 8 + vl) * XROW + ct * 128 + (lane & 7) * 16) = o;
    }
  }
  STAMP(31);
  RSTAMP(63);
  STAMP_OUT();
}

extern "C" void kernel_launch(void* const* d_in, const int* in_sizes, int n_in, void* d_out, int out_size, void* d_ws, size_t ws_size,
                              hipStream_t stream) {
  fused_kernel<<<256, 512, 0, stream>>>((const u16*)d_in[0], (const u16*)d_in[1], (const u16*)d_in[4], (const u16*)d_in[2], (const u16*)d_in[5],
                                        (const u16*)d_in[6], (char*)d_ws, (u16*)d_out);
}
```

```cpp
#include <hip/hip_runtime.h>
#include <hip/hip_bf16.h>
#include <stdint.h>

typedef unsigned short u16;
typedef unsigned long long u64;
typedef short s16x4 __attribute__((ext_vector_type(4)));
typedef float f32x2 __attribute__((ext_vector_type(2)));
typedef float f32x4 __attribute__((ext_vector_type(4)));
typedef unsigned u32x2 __attribute__((ext_vector_type(2)));
typedef unsigned u32x4 __attribute__((ext_vector_type(4)));
typedef u32x4 frag;
typedef __bf16 bf16x2 __attribute__((ext_vector_type(2)));
typedef __bf16 bfx8 __attribute__((ext_vector_type(8)));
#define LDSP __attribute__((address_space(3)))
#define GAS __attribute__((address_space(1)))
#define DI __device__ __forceinline__

constexpr int WROW = 3456;
constexpr int XROW = 1152;
constexpr int PITCH = 432;
constexpr int KO = 32 * PITCH;
constexpr int STG_W = 64 * PITCH;
constexpr int STG = STG_W + 144 * 128;
constexpr int G_OFF = 3 * STG;
constexpr int SCR_OFF = G_OFF + 4608;
constexpr int EX_OFF = 143360;
constexpr int LDS_BYTES = 163840;
constexpr int O_BYTES = 128 * 36 * 1024;
constexpr int FLAG_OFF = 8 << 20;
constexpr int EPOCH_OFF = FLAG_OFF + (1 << 20);
#define ROT 11
#define PRE0 5
#define NBX 22
#define FS1 128
#define FS2 8
#define PSLEEP 1
#define CODEPF 17792
#define NB1 5

#define MFMA(a, b, c) __builtin_amdgcn_mfma_f32_16x16x32_bf16(__builtin_bit_cast(bfx8, (a)), __builtin_bit_cast(bfx8, (b)), (c), 0, 0, 0)
#define RLX_AGENT __ATOMIC_RELAXED, __HIP_MEMORY_SCOPE_AGENT
#define HOFF(h) (36 * (h) + ((h) & 1) * 4)

DI void glds16(unsigned voff, const void* sbase, unsigned lds_addr) {
  asm volatile("s_mov_b32 m0, %2\n\ts_nop 0\n\tglobal_load_lds_dwordx4 %0, %1" ::"v"(voff), "s"(sbase), "s"(lds_addr) : "memory");
}
DI u32x2 trd(unsigned a) { return __builtin_bit_cast(u32x2, __builtin_amdgcn_ds_read_tr16_b64_v4i16((LDSP s16x4*)(size_t)a)); }
DI frag ldW(unsigned a0, unsigned a1) {
  u32x2 lo = trd(a0), hi = trd(a1);
  return __builtin_shufflevector(lo, hi, 0, 1, 2, 3);
}
DI frag ld128(unsigned a) { return *(LDSP frag*)(size_t)a; }
DI u32x2 ld64(unsigned a) { return *(LDSP u32x2*)(size_t)a; }
DI void st64(unsigned a, u32x2 v) { *(LDSP u32x2*)(size_t)a = v; }
DI unsigned cvtpk(float a, float b) {
  f32x2 f = {a, b};
  return __builtin_bit_cast(unsigned, __builtin_convertvector(f, bf16x2));
}
DI u32x2 pack4(f32x4 a) { return u32x2{cvtpk(a[0], a[1]), cvtpk(a[2], a[3])}; }
DI frag cat(u32x2 a, u32x2 b) { return __builtin_shufflevector(a, b, 0, 1, 2, 3); }
DI void swap32(float& a, float& b) {
  auto r = __builtin_amdgcn_permlane32_swap(__float_as_uint(a), __float_as_uint(b), false, false);
  a = __uint_as_float(r[0]); b = __uint_as_float(r[1]);
}
DI void swap16(float& a, float& b) {
  auto r = __builtin_amdgcn_permlane16_swap(__float_as_uint(a), __float_as_uint(b), false, false);
  a = __uint_as_float(r[0]); b = __uint_as_float(r[1]);
}
DI float fmx(float a, float b) { return __builtin_elementwise_maximum(a, b); }
DI float fmx3(float a, float b, float c) { return fmx(fmx(a, b), c); }
#define STAMP(i)
#define RSTAMP(i)
#define STAMP_OUT()
#define STAMP_ARG
#define STAMP_PASS
#define STAMP_DECL
#define SGB(m, n) __builtin_amdgcn_sched_group_barrier(m, n, 0)
#define SCHED0() __builtin_amdgcn_sched_barrier(0)
#define WAIT_LGKM0() do { __builtin_amdgcn_s_waitcnt(0xC07F); asm volatile("" ::: "memory"); } while (0)
#define WAIT_VM0() asm volatile("s_waitcnt vmcnt(0)" ::: "memory")
#define BARRIER() __builtin_amdgcn_s_barrier()
#define PIN(x) asm volatile("" : "+v"(x))
#define TAG_SCALAR() do { unsigned lo_ = __builtin_amdgcn_readfirstlane((unsigned)tag), hi_ = __builtin_amdgcn_readfirstlane((unsigned)(tag >> 32)); \
    tag = (((u64)hi_ << 32) | lo_) + 1; asm volatile("" : "+s"(tag)); } while (0)
#define PINS(x) asm volatile("" : "+s"(x))
#define WR2(addr, a, b, o0, o1) asm volatile("ds_write2st64_b64 %0, %1, %2 offset0:%3 offset1:%4" ::"v"(addr), "v"(a), "v"(b), "i"(o0), "i"(o1) : "memory")

DI void pv_store(u32x4 pk, const frag (&V)[5], u32x4 sel, unsigned vmain, unsigned vleft, __amdgpu_buffer_rsrc_t ors, unsigned so, unsigned sl, bool g0) {
  const f32x4 z = {0.f, 0.f, 0.f, 0.f};
  frag P[4];
  f32x4 o[4], os[4];
  auto mkP = [&](int h) {
    unsigned u = pk[h];
    P[h] = frag{__builtin_amdgcn_perm(u, u, sel[0]), __builtin_amdgcn_perm(u, u, sel[1]), __builtin_amdgcn_perm(u, u, sel[2]), __builtin_amdgcn_perm(u, u, sel[3])};
    PIN(P[h]);
  };
  mkP(0); mkP(1);
  SCHED0();
  u32x4 f0, f1;
  os[0] = MFMA(V[4], P[0], z); PIN(os[0]); o[0] = MFMA(V[0], P[0], z); PIN(o[0]); mkP(2); SCHED0();
  os[1] = MFMA(V[4], P[1], z); PIN(os[1]); o[1] = MFMA(V[1], P[1], z); PIN(o[1]); mkP(3); SCHED0();
  os[2] = MFMA(V[4], P[2], z); PIN(os[2]); os[3] = MFMA(V[4], P[3], z); PIN(os[3]);
  o[2] = MFMA(V[2], P[2], z); PIN(o[2]); o[3] = MFMA(V[3], P[3], z); PIN(o[3]); SCHED0();
  f0 = u32x4{cvtpk(o[0][0], o[0][1]), cvtpk(o[0][2], o[0][3]), cvtpk(o[1][0], o[1][1]), cvtpk(o[1][2], o[1][3])};
  PIN(f0); SCHED0();
  __builtin_amdgcn_raw_buffer_store_b128(f0, ors, vmain, so, 16);
  SCHED0();
  u32x2 f2 = {cvtpk(g0 ? os[0][0] : os[2][0], g0 ? os[0][1] : os[2][1]), cvtpk(g0 ? os[1][2] : os[3][2], g0 ? os[1][3] : os[3][3])};
  PIN(f2); SCHED0();
  f1 = u32x4{cvtpk(o[2][0], o[2][1]), cvtpk(o[2][2], o[2][3]), cvtpk(o[3][0], o[3][1]), cvtpk(o[3][2], o[3][3])};
  PIN(f1); PIN(f0); SCHED0();
  __builtin_amdgcn_raw_buffer_store_b128(f1, ors, vmain + 1024, so, 16);
  __builtin_amdgcn_raw_buffer_store_b64(f2, ors, vleft, sl, 16);
  SCHED0();
}

template <int MT>
DI void phase2(unsigned L, char* ws, __amdgpu_buffer_rsrc_t ors, u64 tag, int tt0, int mrow, int kq, int rot2, int lane STAMP_ARG) {
  const int g = lane >> 4, q = (lane >> 2) & 3, p = lane & 3, c = lane & 15;
  const unsigned sw = (2 * g + (q >> 1)) & 3;
  unsigned bw[4];
#pragma unroll
  for (int n = 0; n < 4; ++n) bw[n] = L + kq * 36864 + (4 * g + q) * 128 + 8 * p + ((n ^ sw) * 32);
  frag wf[9][4];
  auto rdw = [&](int i) {
#pragma unroll
    for (int n = 0; n < 4; ++n) wf[i][n] = ldW(bw[n] + i * 4096, bw[n] + i * 4096 + 2048);
  };
  rdw(0); rdw(1); rdw(2); rdw(3); rdw(4);
  SCHED0();
  unsigned so[9], vo[MT];
#pragma unroll
  for (int i = 0; i < 9; ++i) so[i] = (i < 8 ? 8 * kq + ((i + rot2) & 7) : 32 + kq) * 1024;
#pragma unroll
  for (int m = 0; m < MT; ++m) { int tt = tt0 + m; tt = tt > 127 ? 127 : tt; vo[m] = lane * 16 + tt * 36864; }
  int tf = (lane & 4) ? tt0 + MT - 1 : tt0; tf = tf > 127 ? 127 : tf;
  GAS u64* fp = (GAS u64*)(ws + FLAG_OFF + (kq * 64 + (tf >> 1)) * FS1 + (lane & 3) * FS2);
  unsigned rb[4];
#pragma unroll
  for (int n = 0; n < 4; ++n) rb[n] = L + kq * 36864 + (mrow + c) * 256 + (((4 * n + g) ^ c) * 16);
#pragma unroll
  for (int i = 0; i < 9; ++i) PINS(so[i]);
#pragma unroll
  for (int m = 0; m < MT; ++m) PIN(vo[m]);
#pragma unroll
  for (int n = 0; n < 4; ++n) PIN(rb[n]);
  PIN(fp);
  WAIT_LGKM0();
  BARRIER();
  STAMP(27);
#pragma clang loop unroll(disable)
  for (unsigned spins = 0; spins < (1u << 17); ++spins) {
    u64 v = __hip_atomic_load(fp, RLX_AGENT);
    if (__all(v == tag)) break;
    __builtin_amdgcn_s_sleep(PSLEEP);
  }
  __builtin_amdgcn_fence(__ATOMIC_ACQUIRE, "wavefront");
  asm volatile("" ::: "memory");
  STAMP(28);
  RSTAMP(62);
  frag of[9][MT];
  constexpr int WIN = 2;
#pragma unroll
  for (int i = 0; i < WIN; ++i)
#pragma unroll
    for (int m = 0; m < MT; ++m) { of[i][m] = __builtin_amdgcn_raw_buffer_load_b128(ors, vo[m], so[i], 16); SCHED0(); }
  f32x4 acc[MT][4];
#pragma unroll
  for (int i = 0; i < 9; ++i) {
    const bool rd = i >= 2 && i + 3 < 9;
    if (rd) rdw(i + 3);
#pragma unroll
    for (int m = 0; m < MT; ++m) {
#pragma unroll
      for (int n = 0; n < 4; ++n) acc[m][n] = MFMA(wf[i][n], of[i][m], i == 0 ? (f32x4{0, 0, 0, 0}) : acc[m][n]);
      if (i + WIN < 9) of[i + WIN][m] = __builtin_amdgcn_raw_buffer_load_b128(ors, vo[m], so[i + WIN], 16);
    }
#pragma unroll
    for (int m = 0; m < MT; ++m) {
#pragma unroll
      for (int n = 0; n < 4; ++n) { SGB(0x8, 1); if (m < 2 && rd) SGB(0x100, 1); }
      if (i + WIN < 9) SGB(0x20, 1);
    }
    SCHED0();
  }
  STAMP(29);
#pragma unroll
  for (int m = 0; m < MT; ++m)
#pragma unroll
    for (int n = 0; n < 4; ++n) *(LDSP f32x4*)(size_t)(rb[n] + m * 4096) = acc[m][n];
  WAIT_LGKM0();
  BARRIER();
}

__global__ __launch_bounds__(512, 2) void fused_kernel(const u16* __restrict__ x, const u16* __restrict__ wA, const u16* __restrict__ wB,
                                                        const u16* __restrict__ woA, const u16* __restrict__ woB, const u16* __restrict__ gw,
                                                        char* __restrict__ ws, u16* __restrict__ out) {
  __shared__ __attribute__((aligned(1024))) char lds[LDS_BYTES];
  const unsigned L = (unsigned)(size_t)(LDSP char*)lds;
  const int bid = blockIdx.x;
  const int xcd = bid & 7, idx = bid >> 3;
  const int tl = idx & 7, gl = idx >> 3;
  const int tile = (xcd & 1) * 8 + tl;
  const int grp = (xcd >> 1) * 4 + gl;
  int rot = ROT == 1 ? tl + 2 * gl : ROT == 2 ? (tl >> 1) * 2 + 2 * gl : ROT == 6 ? (tl % 3) * 3 + (gl % 3) * 3 : ROT == 7 ? (((tl >> 1) + gl) & 3) * 2 + (tl & 1 & (gl >> 1))
          : ROT == 8 ? (((tl + gl) & 3) * 9) >> 2 : ROT == 9 ? ((tl + gl + (xcd >> 1)) % 3) * 3 : ROT == 10 ? (((tl + gl) & 3) * 9 >> 2) + (xcd & 1)
          : ROT == 11 ? ((tl + gl) & 3) * 2 + ((xcd ^ (xcd >> 1)) & 1) : ROT == 12 ? ((tl + gl) % 3) * 3 + ((xcd & 1) + (xcd >> 1)) % 3
          : ROT == 13 ? ((tl + gl) & 1) * 4 + (((xcd & 1) * 2 + (xcd >> 1)) & 3) : ROT == 14 ? (tl + 2 * gl + ((xcd >> 1) & 1)) % 9
          : ROT == 15 ? (((tl + gl) & 3) * 2 + ((xcd ^ (xcd >> 1)) & 1) + (xcd >> 2)) : 0;
  if (rot >= 9) rot -= 9;
  const int tid = threadIdx.x, lane = tid & 63;
  const int wave = __builtin_amdgcn_readfirstlane(tid >> 6);
  const int vl = lane >> 3;
  STAMP_DECL;
  STAMP(0);
  RSTAMP(60);
  if (tid < 6) {
    u64 pc0;
    asm volatile("s_getpc_b64 %0" : "=s"(pc0));
    unsigned ln = idx * 6 + tid;
    ln = ln < CODEPF / 128 ? ln : 0;
    asm volatile("s_mov_b32 m0, %2\n\ts_nop 0\n\tglobal_load_lds_dword %0, %1" ::"v"(ln * 128), "s"(pc0 & ~127ull), "s"(L + SCR_OFF) : "memory");
  }
  asm volatile("" ::"s"(x), "s"(wA), "s"(wB), "s"(woA), "s"(woB), "s"(gw), "s"(ws), "s"(out));
  u64 tag;
  u32x2 gv = {0u, 0u};
  if (tid < 144) gv = *(const u32x2*)(gw + tid * 4);
  const int lin0 = xcd * 30 + idx;
  const bool act2 = idx < 30 && lin0 < 234;
  const int lin = act2 ? lin0 : 0;
  const int rt = (lin * 57) >> 9, ct = lin - rt * 9;
  const int rot2 = 3 * (rt - ((rt * 171) >> 9) * 3);
  const float sg = (grp >= 8) ? 1.4426950408889634f : -1.4426950408889634f;
  const unsigned wsch = ((((lane & 7) >> 1) ^ ((vl >> 1) & 3)) << 5) | ((lane & 1) << 4);
  const unsigned offn = vl * XROW + wsch, offl = ((vl >> 2) * 72 + (vl & 3)) * XROW + wsch;
  auto wo_piece = [&](int kq, int i, int j) {
    const char* b = (const char*)(kq >= 2 ? woB : woA) + ct * 128 + (kq & 1) * (288 * XROW);
    if (i == 8) b += ((j & 1) * 144 + ((j >> 1) ? 52 : 16)) * XROW;
    else b += (((i + rot2) & 7) * 36 + (j >> 1) * 20 + 8 * (j & 1)) * XROW;
    glds16(i == 8 ? offl : offn, b, L + (kq * 9 + i) * 4096 + j * 1024);
  };
  const __amdgpu_buffer_rsrc_t ors = __builtin_amdgcn_make_buffer_rsrc(ws, 0, O_BYTES, 0x00020000);
  const int g = lane >> 4, q = (lane >> 2) & 3, p = lane & 3, c = lane & 15;
  const int cw = wave & 3;

  if (__builtin_expect(wave >= 4, 1)) {
    __builtin_amdgcn_s_setprio(2);
    const int lw = cw, tl256 = tid - 256;
    const unsigned offx = (tile * 128 + 8 * lw + vl) * XROW + ((lane & 7) ^ ((4 * lw + (vl >> 1)) & 7)) * 16;
    const char* w = (const char*)((grp >= 8) ? wB : wA) + (grp & 7) * 144;
    unsigned offw[7], offh;
    auto kchunk = [&](int st) { int kc = st + rot; return kc >= 9 ? kc - 9 : kc; };
    auto issueX = [&](int kc, unsigned sb, int jj) { glds16(offx, (const char*)x + kc * 128 + jj * (32 * XROW), sb + STG_W + (2 + lw + 4 * jj) * 1024); };
    auto issueW = [&](int kc, unsigned sb, int j) { glds16(offw[j], w + kc * (64 * WROW), sb + (lw + 4 * j) * 1024); };
    auto issue6 = [&](int kc, unsigned sb) {
      if (lw < 3) issueW(kc, sb, 6);
      else { glds16(offw[6], (const char*)x + kc * 128, sb + STG_W); glds16(offh, (const char*)x + kc * 128, sb + STG_W + 1024); }
    };
    const int kc0 = rot, kc1 = kchunk(1);
    const unsigned sb0 = L, sb1 = L + STG;
    auto calcw = [&](int j) {
      unsigned qq = 256 * j + tl256;
      unsigned u = (qq * 2428u) >> 16, cc = qq - u * 27;
      unsigned row = (u & 0x38) | ((u & 1) << 2) | ((u >> 1) & 3);
      offw[j] = row * WROW + cc * 16 + ((cc * 57u) >> 9) * 1008;
    };
    issueX(kc0, sb0, 0);
#pragma unroll
    for (int j = 0; j < 6; ++j) {
      SCHED0();
      calcw(j);
      SCHED0();
      issueW(kc0, sb0, j);
      if (j < 3) issueX(kc0, sb0, j + 1);
    }
    calcw(6);
    {
      int t0 = tile * 128 - 16 + vl, t1 = t0 + 8;
      t0 = t0 < 0 ? 0 : t0; t1 = t1 < 0 ? 0 : t1;
      unsigned h0 = t0 * XROW + ((lane & 7) ^ (vl >> 1)) * 16;
      offh = t1 * XROW + ((lane & 7) ^ (4 + (vl >> 1))) * 16;
      offw[6] = lw == 3 ? h0 : offw[6];
    }
    issue6(kc0, sb0);
    STAMP(1);
#pragma unroll
    for (int j = 0; j < PRE0; ++j) issueW(kc1, sb1, j);
    STAMP(2);
    f32x4 he = {0, 0, 0, 0};
    const unsigned aE = L + (8 * g + 2 * q) * PITCH + (lw == 0 ? 144 : 288) + ((p & 1) ? 104 : 32);
    const unsigned aX = L + STG_W + c * 128 + ((g ^ (c >> 1)) * 16);
    frag fe[2], fx[2];
    auto halo_rd = [&](unsigned so) {
      if (lw < 2) {
#pragma unroll
        for (int ks = 0; ks < 2; ++ks) {
          fx[ks] = ld128((aX ^ (ks * 64)) + so);
          fe[ks] = ldW(aE + so + ks * KO, aE + so + ks * KO + PITCH);
        }
      }
    };
    auto halo_mma = [&]() {
      if (lw == 0) { he = MFMA(fe[0], fx[0], he); he = MFMA(fe[1], fx[1], he); }
      if (lw == 1) { he = MFMA(fx[0], fe[0], he); he = MFMA(fx[1], fe[1], he); }
    };
    int slot = 0, kn = kc1;
    asm volatile("s_waitcnt vmcnt(%0)" ::"i"(PRE0) : "memory");
#pragma clang loop unroll(disable)
    for (int st = 0; st < 8; ++st) {
      BARRIER();
      STAMP(3 + st);
      if (st == 0) {
#pragma unroll
        for (int j = PRE0; j < 6; ++j) issueW(kc1, sb1, j);
        tag = __hip_atomic_load((GAS u64*)(ws + EPOCH_OFF), RLX_AGENT);
      }
      const unsigned b1 = L + (slot == 2 ? 0 : slot + 1) * STG, b2 = L + (slot == 0 ? 2 : slot - 1) * STG;
      issue6(kn, b1);
      issueX(kn, b1, 0);
      halo_rd(slot * STG);
      issueX(kn, b1, 1); issueX(kn, b1, 2); issueX(kn, b1, 3);
      SCHED0();
      halo_mma();
      SCHED0();
      kn = kn == 8 ? 0 : kn + 1;
      if (st < 7) {
#pragma unroll
        for (int j = 0; j < 6; ++j) issueW(kn, b2, j);
      } else {
#pragma unroll
        for (int I = 0; I < 6; ++I) wo_piece(0, I, lw);
      }
      slot = slot == 2 ? 0 : slot + 1;
      WAIT_LGKM0(); asm volatile("s_waitcnt vmcnt(6)" ::: "memory");
    }
    BARRIER();
    STAMP(11);
    halo_rd(2 * STG);
    wo_piece(0, 6, lw); wo_piece(0, 7, lw);
    SCHED0();
    halo_mma();
    SCHED0();
    if (lw < 2) {
      u32x2 v = pack4(he);
      if (lw == 0 && tile == 0) v = u32x2{0u, 0u};
      st64(L + EX_OFF + lane * 8 + (lw == 0 ? 4 : 9) * 512, v);
    }
    __builtin_amdgcn_s_setprio(0);
#pragma unroll
    for (int I = 8; I < NBX; ++I) wo_piece(I / 9, I % 9, lw);
    WAIT_LGKM0();
    STAMP(12);
    BARRIER();
    STAMP(13);
#pragma unroll
    for (int I = NBX; I < 34; ++I) wo_piece(I / 9, I % 9, lw);
    WAIT_VM0();
    TAG_SCALAR();
    STAMP(14);
  } else {
    const unsigned wrow = L + (8 * g + 2 * q) * PITCH;
    tag = __hip_atomic_load((GAS u64*)(ws + EPOCH_OFF), RLX_AGENT);
    f32x4 maskP, maskC, maskP0;
    {
      const float ninf = -__builtin_inff();
#pragma unroll
      for (int r = 0; r < 4; ++r) {
        bool pm = (r & 1) == (c & 1);
        maskC[r] = (pm && 4 * g + r <= c) ? 0.f : ninf;
        maskP[r] = (pm && 4 * g + r > c) ? 0.f : ninf;
      }
      maskP0 = maskP + ((tile == 0 && wave == 0) ? -998244352.f * 4.242640687119285f : 0.f);
      asm volatile("" : "+v"(maskP), "+v"(maskC), "+v"(maskP0));
    }
    u32x4 sel;
    {
      const int par = c & 1;
      const bool cur1 = 4 * g + par <= c, cur2 = 4 * g + 2 + par <= c;
      const unsigned s1 = par ? 0x01000c0cu : 0x0c0c0100u;
      const unsigned s2 = par ? 0x03020c0cu : 0x0c0c0302u;
      sel[0] = cur1 ? 0x0c0c0c0cu : s1;
      sel[1] = cur2 ? 0x0c0c0c0cu : s2;
      sel[2] = cur1 ? s1 : 0x0c0c0c0cu;
      sel[3] = cur2 ? s2 : 0x0c0c0c0cu;
    }
    unsigned vmain = lane * 16, vleft = lane < 32 ? c * 16 + g * 8 : 0x40000000u;
    unsigned osoff = (tile * 8 + 2 * wave) * 36864 + grp * 2048, oloff = (tile * 8 + 2 * wave) * 36864 + 32768 + grp * 256;
    GAS u64* myflag = (GAS u64*)(ws + FLAG_OFF + ((grp >> 2) * 64 + tile * 4 + cw) * FS1 + (grp & 3) * FS2);
    PINS(myflag);
    unsigned osoff1 = osoff + 36864, oloff1 = oloff + 36864;
    PINS(osoff); PINS(oloff); PINS(osoff1); PINS(oloff1);
    asm volatile("" : "+v"(sel), "+v"(vmain), "+v"(vleft));
    f32x4 aK[2][4], aQ[2][4], aKQ[2], aV[2][5], hk = {0, 0, 0, 0}, hv = {0, 0, 0, 0};
#pragma unroll
    for (int t = 0; t < 2; ++t) {
#pragma unroll
      for (int f = 0; f < 4; ++f) { aK[t][f] = f32x4{0, 0, 0, 0}; aQ[t][f] = f32x4{0, 0, 0, 0}; }
      aKQ[t] = f32x4{0, 0, 0, 0};
#pragma unroll
      for (int f = 0; f < 5; ++f) aV[t][f] = f32x4{0, 0, 0, 0};
    }
    unsigned cW = wrow + 8 * p;
    unsigned cKQ = wrow + (p < 2 ? 144 : 0) + ((p & 1) ? 104 : 32);
    unsigned cXh0 = L + STG_W + c * 128 + ((g ^ (c >> 1)) * 16), cXh1 = cXh0 ^ 64;
    unsigned cX0 = cXh0 + (1 + 2 * wave) * 2048, cX1 = cX0 ^ 64;
    unsigned cH = cW + 144 + HOFF(wave);
    unsigned cV[4];
#pragma unroll
    for (int i = 0; i < 4; ++i)
      cV[i] = p < 2 ? wrow + 288 + (p ? 104 : 32) + (i >> 1) * KO + (i & 1) * PITCH : L + G_OFF + rot * 512 + (8 * g + q) * 8 + (i >> 1) * 256 + (i & 1) * 32;
    frag cv[5], xb[2];
#pragma unroll
    for (int f = 0; f < 5; ++f) cv[f] = frag{0, 0, 0, 0};
    xb[0] = frag{0, 0, 0, 0}; xb[1] = frag{0, 0, 0, 0};
    int slot = 0, kc = rot;
    unsigned exr = L + EX_OFF + wave * 5120 + lane * 8, exh = L + EX_OFF + wave * 512 + lane * 8;
    unsigned exgw = L + SCR_OFF + wave * 128 + g * 16, exgr = L + SCR_OFF + wave * 128 + c * 4;
    asm volatile("" : "+v"(aK[0][0]), "+v"(aK[0][1]), "+v"(aK[0][2]), "+v"(aK[0][3]), "+v"(aK[1][0]), "+v"(aK[1][1]), "+v"(aK[1][2]), "+v"(aK[1][3]),
                      "+v"(aQ[0][0]), "+v"(aQ[0][1]), "+v"(aQ[0][2]), "+v"(aQ[0][3]), "+v"(aQ[1][0]), "+v"(aQ[1][1]), "+v"(aQ[1][2]), "+v"(aQ[1][3]),
                      "+v"(aKQ[0]), "+v"(aKQ[1]), "+v"(hk), "+v"(hv), "+v"(xb[0]), "+v"(xb[1]), "+v"(exr), "+v"(exh), "+v"(exgw), "+v"(exgr));
    asm volatile("" : "+v"(aV[0][0]), "+v"(aV[0][1]), "+v"(aV[0][2]), "+v"(aV[0][3]), "+v"(aV[0][4]), "+v"(aV[1][0]), "+v"(aV[1][1]), "+v"(aV[1][2]),
                      "+v"(aV[1][3]), "+v"(aV[1][4]), "+v"(cv[0]), "+v"(cv[1]), "+v"(cv[2]), "+v"(cv[3]), "+v"(cv[4]),
                      "+v"(cW), "+v"(cKQ), "+v"(cX0), "+v"(cX1), "+v"(cH), "+v"(cXh0), "+v"(cXh1), "+v"(cV[0]), "+v"(cV[1]), "+v"(cV[2]), "+v"(cV[3]));
    SCHED0();
    STAMP(1);
    if (tid < 144) {
      unsigned a0 = __builtin_amdgcn_perm(gv[0], gv[0], 0x01000100), a1 = __builtin_amdgcn_perm(gv[0], gv[0], 0x03020302);
      unsigned a2 = __builtin_amdgcn_perm(gv[1], gv[1], 0x01000100), a3 = __builtin_amdgcn_perm(gv[1], gv[1], 0x03020302);
      *(LDSP u32x4*)(size_t)(L + G_OFF + tid * 32) = u32x4{a0, a0, a1, a1};
      *(LDSP u32x4*)(size_t)(L + G_OFF + tid * 32 + 16) = u32x4{a2, a2, a3, a3};
    }
    STAMP(2);
    WAIT_LGKM0();
#pragma clang loop unroll(disable)
    for (int st = 0; st < 9; ++st) {
      BARRIER();
      SCHED0();
      frag xa[2], W0[14], W1[9], xh[2], Hk[2], Hv[2];
      xa[0] = ld128(cX0); xa[1] = ld128(cX0 + 2048);
#pragma unroll
      for (int f = 0; f < 4; ++f) W0[f] = ldW(cW + 144 + HOFF(f), cW + 144 + HOFF(f) + PITCH);
      W0[4] = ldW(cKQ, cKQ + PITCH);
#pragma unroll
      for (int f = 0; f < 5; ++f) { aV[0][f] = MFMA(xb[0], cv[f], aV[0][f]); aV[1][f] = MFMA(xb[1], cv[f], aV[1][f]); }
#pragma unroll
      for (int f = 0; f < 4; ++f) W0[5 + f] = ldW(cW + HOFF(f), cW + HOFF(f) + PITCH);
      xh[0] = ld128(cXh0);
      Hk[0] = ldW(cH, cH + PITCH); Hv[0] = ldW(cH + 144, cH + 144 + PITCH);
#pragma unroll
      for (int f = 0; f < 4; ++f) W0[9 + f] = ldW(cW + 288 + HOFF(f), cW + 288 + HOFF(f) + PITCH);
      W0[13] = ldW(cV[0], cV[1]);
      xb[0] = ld128(cX1); xb[1] = ld128(cX1 + 2048);
#pragma unroll
      for (int f = 0; f < 4; ++f) W1[f] = ldW(cW + KO + 144 + HOFF(f), cW + KO + 144 + HOFF(f) + PITCH);
      W1[4] = ldW(cKQ + KO, cKQ + KO + PITCH);
      xh[1] = ld128(cXh1);
      Hk[1] = ldW(cH + KO, cH + KO + PITCH); Hv[1] = ldW(cH + KO + 144, cH + KO + 144 + PITCH);
#pragma unroll
      for (int f = 0; f < 4; ++f) W1[5 + f] = ldW(cW + KO + HOFF(f), cW + KO + HOFF(f) + PITCH);
#pragma unroll
      for (int f = 0; f < 4; ++f) cv[f] = ldW(cW + KO + 288 + HOFF(f), cW + KO + 288 + HOFF(f) + PITCH);
      cv[4] = ldW(cV[2], cV[3]);
#pragma unroll
      for (int f = 0; f < 4; ++f) { aK[0][f] = MFMA(W0[f], xa[0], aK[0][f]); aK[1][f] = MFMA(W0[f], xa[1], aK[1][f]); }
      aKQ[0] = MFMA(W0[4], xa[0], aKQ[0]); aKQ[1] = MFMA(W0[4], xa[1], aKQ[1]);
#pragma unroll
      for (int f = 0; f < 4; ++f) { aQ[0][f] = MFMA(W0[5 + f], xa[0], aQ[0][f]); aQ[1][f] = MFMA(W0[5 + f], xa[1], aQ[1][f]); }
      hk = MFMA(Hk[0], xh[0], hk); hv = MFMA(xh[0], Hv[0], hv);
#pragma unroll
      for (int f = 0; f < 5; ++f) { aV[0][f] = MFMA(xa[0], W0[9 + f], aV[0][f]); aV[1][f] = MFMA(xa[1], W0[9 + f], aV[1][f]); }
#pragma unroll
      for (int f = 0; f < 4; ++f) { aK[0][f] = MFMA(W1[f], xb[0], aK[0][f]); aK[1][f] = MFMA(W1[f], xb[1], aK[1][f]); }
      aKQ[0] = MFMA(W1[4], xb[0], aKQ[0]); aKQ[1] = MFMA(W1[4], xb[1], aKQ[1]);
      hk = MFMA(Hk[1], xh[1], hk); hv = MFMA(xh[1], Hv[1], hv);
#pragma unroll
      for (int f = 0; f < 4; ++f) { aQ[0][f] = MFMA(W1[5 + f], xb[0], aQ[0][f]); aQ[1][f] = MFMA(W1[5 + f], xb[1], aQ[1][f]); }
      const int ds = slot == 2 ? -2 * STG : STG, dg = kc == 8 ? -8 * 512 : 512;
      slot = slot == 2 ? 0 : slot + 1;
      kc = kc == 8 ? 0 : kc + 1;
      const int dv = p < 2 ? ds : dg;
      cW += ds; cKQ += ds; cX0 += ds; cX1 += ds; cH += ds; cXh0 += ds; cXh1 += ds;
#pragma unroll
      for (int i = 0; i < 4; ++i) cV[i] += dv;
      SGB(0x100, 2);
#pragma unroll
      for (int i = 0; i < 54; ++i) {
        SGB(0x8, 1);
        if (i < 2 || (i >= 4 && i <= 48 && (i % 4) == 0)) SGB(0x100, 2); else SGB(0x100, 1);
        if (i >= 41 && i < 53 && (i % 4) != 0) SGB(0x4, 1);
      }
#pragma unroll
      for (int i = 0; i < 5; ++i) { SGB(0x8, 1); SGB(0x2, 2); }
      SGB(0x2, 4);
      SGB(0x8, 1);
      SCHED0();
      WAIT_LGKM0();
    }
    STAMP(12);
    const bool g0 = g == 0;
    frag K[3][4], Q[2][4], Vb[5], Va[5];
    u32x2 ks[2], qs[2];
    aV[1][4] = MFMA(xb[1], cv[4], aV[1][4]); PIN(aV[1][4]);
    aV[0][4] = MFMA(xb[0], cv[4], aV[0][4]); PIN(aV[0][4]);
#pragma unroll
    for (int t = 1; t >= 0; --t) {
      u32x2 kq = pack4(aKQ[t]);
      auto r0 = __builtin_amdgcn_permlane32_swap(kq[0], 0u, false, false);
      auto r1 = __builtin_amdgcn_permlane32_swap(kq[1], 0u, false, false);
      ks[t] = u32x2{r0[0], r1[0]}; qs[t] = u32x2{r0[1], r1[1]};
      PIN(ks[t]); PIN(qs[t]);
    }
    SCHED0();
#pragma unroll
    for (int f = 0; f < 4; ++f) { aV[1][f] = MFMA(xb[1], cv[f], aV[1][f]); PIN(aV[1][f]); K[2][f] = cat(pack4(aK[1][f]), ks[1]); PIN(K[2][f]); SCHED0(); }
    {
      u32x2 h = pack4(hk), hh = pack4(hv);
      if (tile == 0) h = u32x2{0u, 0u};
      WR2(exh, h, hh, 0, 5);
    }
    if (c >= 8) { *(LDSP f32x4*)(size_t)(exgw) = aV[0][4]; *(LDSP f32x4*)(size_t)(exgw + 64) = aV[1][4]; }
    SCHED0();
#pragma unroll
    for (int f = 0; f < 4; ++f) {
      aV[0][f] = MFMA(xb[0], cv[f], aV[0][f]); PIN(aV[0][f]);
      u32x2 v = pack4(aV[1][f]); Vb[f][2] = v[0]; Vb[f][3] = v[1]; PIN(Vb[f]); SCHED0();
    }
    { u32x2 v = pack4(aV[1][4]); Vb[4][2] = v[0]; Vb[4][3] = v[1]; PIN(Vb[4]); }
    if (wave < 3) {
#pragma unroll
      for (int f = 0; f < 4; ++f) WR2(exr, (u32x2{K[2][f][0], K[2][f][1]}), (u32x2{Vb[f][2], Vb[f][3]}), 10 + f, 15 + f);
      WR2(exr, ks[1], (u32x2{Vb[4][2], Vb[4][3]}), 14, 19);
    }
    SCHED0();
#pragma unroll
    for (int h = 0; h < 4; ++h) {
      K[1][h] = cat(pack4(aK[0][h]), ks[0]); PIN(K[1][h]);
      u32x2 qt = pack4(aQ[1][h]);
      unsigned e = ((h < 2) == g0) ? qs[1][h & 1] : 0u;
      Q[1][h] = (h & 1) ? frag{qt[0], qt[1], 0u, e} : frag{qt[0], qt[1], e, 0u}; PIN(Q[1][h]);
    }
    SCHED0();
    TAG_SCALAR();
    WAIT_VM0(); WAIT_LGKM0();
    STAMP(23);
    BARRIER();
    STAMP(24);
    SCHED0();
    u32x2 gz;
    asm volatile("ds_read2_b32 %0, %1 offset0:0 offset1:16" : "=v"(gz) : "v"(exgr) : "memory");
#pragma unroll
    for (int h = 0; h < 4; ++h) asm volatile("ds_read2st64_b64 %0, %1 offset0:%2 offset1:4" : "=v"(K[0][h]) : "v"(exr), "i"(h) : "memory");
    f32x4 sc[2][4], sp[2][4];
#pragma unroll
    for (int h = 0; h < 4; ++h) {
      sc[1][h] = MFMA(K[2][h], Q[1][h], maskC); PIN(sc[1][h]);
      sp[1][h] = MFMA(K[1][h], Q[1][h], maskP); PIN(sp[1][h]);
      u32x2 qt = pack4(aQ[0][h]);
      unsigned e = ((h < 2) == g0) ? qs[0][h & 1] : 0u;
      Q[0][h] = (h & 1) ? frag{qt[0], qt[1], 0u, e} : frag{qt[0], qt[1], e, 0u}; PIN(Q[0][h]);
      SCHED0();
    }
    asm volatile("s_waitcnt lgkmcnt(0)" : "+v"(gz), "+v"(K[0][0]), "+v"(K[0][1]), "+v"(K[0][2]), "+v"(K[0][3])::"memory");
    float v[2][4][2];
#pragma unroll
    for (int h = 0; h < 4; ++h) {
      sc[0][h] = MFMA(K[1][h], Q[0][h], maskC); PIN(sc[0][h]);
      sp[0][h] = MFMA(K[0][h], Q[0][h], maskP0); PIN(sp[0][h]);
      v[1][h][0] = fmx(fmx3(sp[1][h][0], sp[1][h][1], sc[1][h][0]), sc[1][h][1]); PIN(v[1][h][0]);
      v[1][h][1] = fmx(fmx3(sp[1][h][2], sp[1][h][3], sc[1][h][2]), sc[1][h][3]); PIN(v[1][h][1]);
      SCHED0();
    }
#pragma unroll
    for (int f = 0; f < 5; ++f) {
      u32x2 a = pack4(aV[0][f]); Vb[f][0] = a[0]; Vb[f][1] = a[1]; PIN(Vb[f]);
      Va[f] = __builtin_shufflevector(Vb[f], Vb[f], -1, -1, 0, 1); PIN(Va[f]);
    }
#pragma unroll
    for (int f = 0; f < 5; ++f) { u32x2 n = *(volatile LDSP u32x2*)(size_t)(exr + (5 + f) * 512); Va[f][0] = n[0]; Va[f][1] = n[1]; }
    u32x4 pk[2];
    auto softmax = [&](float (&w)[4][2], float gate_logit) {
      swap32(w[0][0], w[2][0]); swap32(w[1][0], w[3][0]); swap32(w[0][1], w[2][1]); swap32(w[1][1], w[3][1]);
      swap16(w[0][0], w[1][0]); swap16(w[2][0], w[3][0]); swap16(w[0][1], w[1][1]); swap16(w[2][1], w[3][1]);
      const float cs = 0.23570226039551584f * 1.4426950408889634f;
      float m = fmx(fmx3(fmx3(w[0][0], w[1][0], w[2][0]), w[3][0], w[0][1]), fmx3(w[1][1], w[2][1], w[3][1]));
      float mc = -m * cs;
      f32x2 e[4];
#pragma unroll
      for (int i = 0; i < 4; ++i) {
        f32x2 a = __builtin_elementwise_fma(f32x2{w[i][0], w[i][1]}, f32x2{cs, cs}, f32x2{mc, mc});
        e[i] = f32x2{__builtin_amdgcn_exp2f(a[0]), __builtin_amdgcn_exp2f(a[1])};
      }
      f32x2 s2 = (e[0] + e[1]) + (e[2] + e[3]);
      float s = s2[0] + s2[1];
      float r = __builtin_amdgcn_rcpf(__builtin_fmaf(s, __builtin_amdgcn_exp2f(gate_logit * sg), s));
      float pf[4];
#pragma unroll
      for (int i = 0; i < 4; ++i) { f32x2 pp = e[i] * r; pf[i] = __uint_as_float(cvtpk(pp[0], pp[1])); }
      swap32(pf[0], pf[2]); swap32(pf[1], pf[3]);
      swap16(pf[0], pf[1]); swap16(pf[2], pf[3]);
      return u32x4{__float_as_uint(pf[0]), __float_as_uint(pf[1]), __float_as_uint(pf[2]), __float_as_uint(pf[3])};
    };
    SCHED0();
#pragma unroll
    for (int h = 0; h < 4; ++h) {
      v[0][h][0] = fmx(fmx3(sp[0][h][0], sp[0][h][1], sc[0][h][0]), sc[0][h][1]);
      v[0][h][1] = fmx(fmx3(sp[0][h][2], sp[0][h][3], sc[0][h][2]), sc[0][h][3]);
    }
    pk[1] = softmax(v[1], __uint_as_float(gz[1]));
#pragma unroll
    for (int h = 0; h < 4; ++h) { PIN(v[0][h][0]); PIN(v[0][h][1]); }
    SCHED0();
    pv_store(pk[1], Vb, sel, vmain, vleft, ors, osoff1, oloff1, g0);
    pk[0] = softmax(v[0], __uint_as_float(gz[0]));
    SCHED0();
    pv_store(pk[0], Va, sel, vmain, vleft, ors, osoff, oloff, g0);
    STAMP(25);
    WAIT_VM0();
    if (lane == 0) __hip_atomic_store(myflag, tag, RLX_AGENT);
    STAMP(26);
    RSTAMP(61);
  }
  BARRIER();
  if (!act2) {
    if (bid == 255) {
      if (wave < 4) {
        const int b = wave * 64 + lane;
        GAS u64* fp = (GAS u64*)(ws + FLAG_OFF + (((b & 15) >> 2) * 64 + (b >> 4) * 4) * FS1 + (b & 3) * FS2);
#pragma clang loop unroll(disable)
        for (unsigned spins = 0; spins < (1u << 17); ++spins) {
          u64 v = __hip_atomic_load(fp, RLX_AGENT);
          if (__all(v == tag)) break;
          __builtin_amdgcn_s_sleep(8);
        }
      }
      BARRIER();
      if (tid == 0) __hip_atomic_store((GAS u64*)(ws + EPOCH_OFF), tag, RLX_AGENT);
    }
    return;
  }
  if (cw == 3) {
#pragma unroll
    for (int j = 0; j < 4; ++j) { wo_piece(3, 7, j); wo_piece(3, 8, j); }
  }
  if (wave < 4) phase2<2>(L, ws, ors, tag, rt * 5 + 3, 48, cw, rot2, lane STAMP_PASS);
  else phase2<3>(L, ws, ors, tag, rt * 5, 0, cw, rot2, lane STAMP_PASS);
  STAMP(30);
  if (wave < 5 && rt * 5 + wave < 128) {
    f32x4 s[2][2];
#pragma unroll
    for (int it = 0; it < 2; ++it) {
      const int r = it * 8 + vl;
      const unsigned a0 = L + (wave * 16 + r) * 256 + ((((lane & 7) * 2) ^ r) * 16);
#pragma unroll
      for (int w = 0; w < 4; ++w) {
        f32x4 u0 = *(LDSP f32x4*)(size_t)(a0 + w * 36864), u1 = *(LDSP f32x4*)(size_t)((a0 ^ 16) + w * 36864);
        s[it][0] = w ? s[it][0] + u0 : u0; s[it][1] = w ? s[it][1] + u1 : u1;
      }
    }
#pragma unroll
    for (int it = 0; it < 2; ++it) {
      u32x4 o = {cvtpk(s[it][0][0], s[it][0][1]), cvtpk(s[it][0][2], s[it][0][3]), cvtpk(s[it][1][0], s[it][1][1]), cvtpk(s[it][1][2], s[it][1][3])};
      *(u32x4*)((char*)out + (size_t)(rt * 80 + wave * 16 + it * 8 + vl) * XROW + ct * 128 + (lane & 7) * 16) = o;
    }
  }
  STAMP(31);
  RSTAMP(63);
  STAMP_OUT();
}

extern "C" void kernel_launch(void* const* d_in, const int* in_sizes, int n_in, void* d_out, int out_size, void* d_ws, size_t ws_size,
                              hipStream_t stream) {
  fused_kernel<<<256, 512, 0, stream>>>((const u16*)d_in[0], (const u16*)d_in[1], (const u16*)d_in[4], (const u16*)d_in[2], (const u16*)d_in[5],
                                        (const u16*)d_in[6], (char*)d_ws, (u16*)d_out);
}
```

```cpp
#include <hip/hip_runtime.h>
#include <hip/hip_bf16.h>
#include <stdint.h>
#include <type_traits>

typedef unsigned short u16;
typedef unsigned long long u64;
typedef short s16x4 __attribute__((ext_vector_type(4)));
typedef float f32x2 __attribute__((ext_vector_type(2)));
typedef float f32x4 __attribute__((ext_vector_type(4)));
typedef unsigned u32x2 __attribute__((ext_vector_type(2)));
typedef unsigned u32x4 __attribute__((ext_vector_type(4)));
typedef u32x4 frag;
typedef __bf16 bf16x2 __attribute__((ext_vector_type(2)));
typedef __bf16 bfx8 __attribute__((ext_vector_type(8)));
#define LDSP __attribute__((address_space(3)))
#define GAS __attribute__((address_space(1)))
#define DI __device__ __forceinline__

constexpr int WROW = 3456;
constexpr int XROW = 1152;
constexpr int PITCH = 432;
constexpr int KO = 32 * PITCH;
constexpr int STG_W = 64 * PITCH;
constexpr int STG = STG_W + 144 * 128;
constexpr int G_OFF = 3 * STG;
constexpr int SCR_OFF = G_OFF + 4608;
constexpr int EX_OFF = 143360;
constexpr int LDS_BYTES = 163840;
constexpr int O_BYTES = 128 * 36 * 1024;
constexpr int FLAG_OFF = 8 << 20;
constexpr int EPOCH_OFF = FLAG_OFF + (1 << 20);
#define ROT 11
#define PRE0 5
#define NBX 22
#define FS1 128
#define FS2 8
#define PSLEEP 1
#define CODEPF 22656
#define NB1 5

#define MFMA(a, b, c) __builtin_amdgcn_mfma_f32_16x16x32_bf16(__builtin_bit_cast(bfx8, (a)), __builtin_bit_cast(bfx8, (b)), (c), 0, 0, 0)
#define RLX_AGENT __ATOMIC_RELAXED, __HIP_MEMORY_SCOPE_AGENT
#define HOFF(h) (36 * (h) + ((h) & 1) * 4)

DI void glds16(unsigned voff, const void* sbase, unsigned lds_addr) {
  asm volatile("s_mov_b32 m0, %2\n\ts_nop 0\n\tglobal_load_lds_dwordx4 %0, %1" ::"v"(voff), "s"(sbase), "s"(lds_addr) : "memory");
}
DI u32x2 trd(unsigned a) { return __builtin_bit_cast(u32x2, __builtin_amdgcn_ds_read_tr16_b64_v4i16((LDSP s16x4*)(size_t)a)); }
DI frag ldW(unsigned a0, unsigned a1) {
  u32x2 lo = trd(a0), hi = trd(a1);
  return __builtin_shufflevector(lo, hi, 0, 1, 2, 3);
}
DI frag ld128(unsigned a) { return *(LDSP frag*)(size_t)a; }
DI u32x2 ld64(unsigned a) { return *(LDSP u32x2*)(size_t)a; }
DI void st64(unsigned a, u32x2 v) { *(LDSP u32x2*)(size_t)a = v; }
DI unsigned cvtpk(float a, float b) {
  f32x2 f = {a, b};
  return __builtin_bit_cast(unsigned, __builtin_convertvector(f, bf16x2));
}
DI u32x2 pack4(f32x4 a) { return u32x2{cvtpk(a[0], a[1]), cvtpk(a[2], a[3])}; }
DI frag cat(u32x2 a, u32x2 b) { return __builtin_shufflevector(a, b, 0, 1, 2, 3); }
DI void swap32(float& a, float& b) {
  auto r = __builtin_amdgcn_permlane32_swap(__float_as_uint(a), __float_as_uint(b), false, false);
  a = __uint_as_float(r[0]); b = __uint_as_float(r[1]);
}
DI void swap16(float& a, float& b) {
  auto r = __builtin_amdgcn_permlane16_swap(__float_as_uint(a), __float_as_uint(b), false, false);
  a = __uint_as_float(r[0]); b = __uint_as_float(r[1]);
}
DI float fmx(float a, float b) { return __builtin_elementwise_maximum(a, b); }
DI float fmx3(float a, float b, float c) { return fmx(fmx(a, b), c); }
#define STAMP(i)
#define RSTAMP(i)
#define STAMP_OUT()
#define STAMP_ARG
#define STAMP_PASS
#define STAMP_DECL
#define SGB(m, n) __builtin_amdgcn_sched_group_barrier(m, n, 0)
#define SCHED0() __builtin_amdgcn_sched_barrier(0)
#define WAIT_LGKM0() do { __builtin_amdgcn_s_waitcnt(0xC07F); asm volatile("" ::: "memory"); } while (0)
#define WAIT_VM0() asm volatile("s_waitcnt vmcnt(0)" ::: "memory")
#define BARRIER() __builtin_amdgcn_s_barrier()
#define PIN(x) asm volatile("" : "+v"(x))
#define TAG_SCALAR() do { unsigned lo_ = __builtin_amdgcn_readfirstlane((unsigned)tag), hi_ = __builtin_amdgcn_readfirstlane((unsigned)(tag >> 32)); \
    tag = (((u64)hi_ << 32) | lo_) + 1; asm volatile("" : "+s"(tag)); } while (0)
#define PINS(x) asm volatile("" : "+s"(x))
#define WR2(addr, a, b, o0, o1) asm volatile("ds_write2st64_b64 %0, %1, %2 offset0:%3 offset1:%4" ::"v"(addr), "v"(a), "v"(b), "i"(o0), "i"(o1) : "memory")

DI void pv_store(u32x4 pk, const frag (&V)[5], u32x4 sel, unsigned vmain, unsigned vleft, __amdgpu_buffer_rsrc_t ors, unsigned so, unsigned sl, bool g0) {
  const f32x4 z = {0.f, 0.f, 0.f, 0.f};
  frag P[4];
  f32x4 o[4], os[4];
  auto mkP = [&](int h) {
    unsigned u = pk[h];
    P[h] = frag{__builtin_amdgcn_perm(u, u, sel[0]), __builtin_amdgcn_perm(u, u, sel[1]), __builtin_amdgcn_perm(u, u, sel[2]), __builtin_amdgcn_perm(u, u, sel[3])};
    PIN(P[h]);
  };
  mkP(0); mkP(1);
  SCHED0();
  u32x4 f0, f1;
  os[0] = MFMA(V[4], P[0], z); PIN(os[0]); o[0] = MFMA(V[0], P[0], z); PIN(o[0]); mkP(2); SCHED0();
  os[1] = MFMA(V[4], P[1], z); PIN(os[1]); o[1] = MFMA(V[1], P[1], z); PIN(o[1]); mkP(3); SCHED0();
  os[2] = MFMA(V[4], P[2], z); PIN(os[2]); os[3] = MFMA(V[4], P[3], z); PIN(os[3]);
  o[2] = MFMA(V[2], P[2], z); PIN(o[2]); o[3] = MFMA(V[3], P[3], z); PIN(o[3]); SCHED0();
  f0 = u32x4{cvtpk(o[0][0], o[0][1]), cvtpk(o[0][2], o[0][3]), cvtpk(o[1][0], o[1][1]), cvtpk(o[1][2], o[1][3])};
  PIN(f0); SCHED0();
  __builtin_amdgcn_raw_buffer_store_b128(f0, ors, vmain, so, 16);
  SCHED0();
  u32x2 f2 = {cvtpk(g0 ? os[0][0] : os[2][0], g0 ? os[0][1] : os[2][1]), cvtpk(g0 ? os[1][2] : os[3][2], g0 ? os[1][3] : os[3][3])};
  PIN(f2); SCHED0();
  f1 = u32x4{cvtpk(o[2][0], o[2][1]), cvtpk(o[2][2], o[2][3]), cvtpk(o[3][0], o[3][1]), cvtpk(o[3][2], o[3][3])};
  PIN(f1); PIN(f0); SCHED0();
  __builtin_amdgcn_raw_buffer_store_b128(f1, ors, vmain + 1024, so, 16);
  __builtin_amdgcn_raw_buffer_store_b64(f2, ors, vleft, sl, 16);
  SCHED0();
}

template <int MT>
DI void phase2(unsigned L, char* ws, __amdgpu_buffer_rsrc_t ors, u64 tag, int tt0, int mrow, int kq, int rot2, int lane STAMP_ARG) {
  const int g = lane >> 4, q = (lane >> 2) & 3, p = lane & 3, c = lane & 15;
  const unsigned sw = (2 * g + (q >> 1)) & 3;
  unsigned bw[4];
#pragma unroll
  for (int n = 0; n < 4; ++n) bw[n] = L + kq * 36864 + (4 * g + q) * 128 + 8 * p + ((n ^ sw) * 32);
  frag wf[9][4];
  auto rdw = [&](int i) {
#pragma unroll
    for (int n = 0; n < 4; ++n) wf[i][n] = ldW(bw[n] + i * 4096, bw[n] + i * 4096 + 2048);
  };
  rdw(0); rdw(1); rdw(2); rdw(3); rdw(4);
  SCHED0();
  unsigned so[9], vo[MT];
#pragma unroll
  for (int i = 0; i < 9; ++i) so[i] = (i < 8 ? 8 * kq + ((i + rot2) & 7) : 32 + kq) * 1024;
#pragma unroll
  for (int m = 0; m < MT; ++m) { int tt = tt0 + m; tt = tt > 127 ? 127 : tt; vo[m] = lane * 16 + tt * 36864; }
  int tf = (lane & 4) ? tt0 + MT - 1 : tt0; tf = tf > 127 ? 127 : tf;
  GAS u64* fp = (GAS u64*)(ws + FLAG_OFF + (kq * 64 + (tf >> 1)) * FS1 + (lane & 3) * FS2);
  unsigned rb[4];
#pragma unroll
  for (int n = 0; n < 4; ++n) rb[n] = L + kq * 36864 + (mrow + c) * 256 + (((4 * n + g) ^ c) * 16);
#pragma unroll
  for (int i = 0; i < 9; ++i) PINS(so[i]);
#pragma unroll
  for (int m = 0; m < MT; ++m) PIN(vo[m]);
#pragma unroll
  for (int n = 0; n < 4; ++n) PIN(rb[n]);
  PIN(fp);
  WAIT_LGKM0();
  BARRIER();
  STAMP(27);
#pragma clang loop unroll(disable)
  for (unsigned spins = 0; spins < (1u << 17); ++spins) {
    u64 v = __hip_atomic_load(fp, RLX_AGENT);
    if (__all(v == tag)) break;
    __builtin_amdgcn_s_sleep(PSLEEP);
  }
  __builtin_amdgcn_fence(__ATOMIC_ACQUIRE, "wavefront");
  asm volatile("" ::: "memory");
  STAMP(28);
  RSTAMP(62);
  frag of[9][MT];
  constexpr int WIN = 2;
#pragma unroll
  for (int i = 0; i < WIN; ++i)
#pragma unroll
    for (int m = 0; m < MT; ++m) { of[i][m] = __builtin_amdgcn_raw_buffer_load_b128(ors, vo[m], so[i], 16); SCHED0(); }
  f32x4 acc[MT][4];
#pragma unroll
  for (int i = 0; i < 9; ++i) {
    const bool rd = i >= 2 && i + 3 < 9;
    if (rd) rdw(i + 3);
#pragma unroll
    for (int m = 0; m < MT; ++m) {
#pragma unroll
      for (int n = 0; n < 4; ++n) acc[m][n] = MFMA(wf[i][n], of[i][m], i == 0 ? (f32x4{0, 0, 0, 0}) : acc[m][n]);
      if (i + WIN < 9) of[i + WIN][m] = __builtin_amdgcn_raw_buffer_load_b128(ors, vo[m], so[i + WIN], 16);
    }
#pragma unroll
    for (int m = 0; m < MT; ++m) {
#pragma unroll
      for (int n = 0; n < 4; ++n) { SGB(0x8, 1); if (m < 2 && rd) SGB(0x100, 1); }
      if (i + WIN < 9) SGB(0x20, 1);
    }
    SCHED0();
  }
  STAMP(29);
#pragma unroll
  for (int m = 0; m < MT; ++m)
#pragma unroll
    for (int n = 0; n < 4; ++n) *(LDSP f32x4*)(size_t)(rb[n] + m * 4096) = acc[m][n];
  WAIT_LGKM0();
  BARRIER();
}

__global__ __launch_bounds__(512, 2) void fused_kernel(const u16* __restrict__ x, const u16* __restrict__ wA, const u16* __restrict__ wB,
                                                        const u16* __restrict__ woA, const u16* __restrict__ woB, const u16* __restrict__ gw,
                                                        char* __restrict__ ws, u16* __restrict__ out) {
  __shared__ __attribute__((aligned(1024))) char lds[LDS_BYTES];
  const unsigned L = (unsigned)(size_t)(LDSP char*)lds;
  const int bid = blockIdx.x;
  const int xcd = bid & 7, idx = bid >> 3;
  const int tl = idx & 7, gl = idx >> 3;
  const int tile = (xcd & 1) * 8 + tl;
  const int grp = (xcd >> 1) * 4 + gl;
  int rot = ROT == 1 ? tl + 2 * gl : ROT == 2 ? (tl >> 1) * 2 + 2 * gl : ROT == 6 ? (tl % 3) * 3 + (gl % 3) * 3 : ROT == 7 ? (((tl >> 1) + gl) & 3) * 2 + (tl & 1 & (gl >> 1))
          : ROT == 8 ? (((tl + gl) & 3) * 9) >> 2 : ROT == 9 ? ((tl + gl + (xcd >> 1)) % 3) * 3 : ROT == 10 ? (((tl + gl) & 3) * 9 >> 2) + (xcd & 1)
          : ROT == 11 ? ((tl + gl) & 3) * 2 + ((xcd ^ (xcd >> 1)) & 1) : ROT == 12 ? ((tl + gl) % 3) * 3 + ((xcd & 1) + (xcd >> 1)) % 3
          : ROT == 13 ? ((tl + gl) & 1) * 4 + (((xcd & 1) * 2 + (xcd >> 1)) & 3) : ROT == 14 ? (tl + 2 * gl + ((xcd >> 1) & 1)) % 9
          : ROT == 15 ? (((tl + gl) & 3) * 2 + ((xcd ^ (xcd >> 1)) & 1) + (xcd >> 2)) : 0;
  if (rot >= 9) rot -= 9;
  const int tid = threadIdx.x, lane = tid & 63;
  const int wave = __builtin_amdgcn_readfirstlane(tid >> 6);
  const int vl = lane >> 3;
  STAMP_DECL;
  STAMP(0);
  RSTAMP(60);
  if (tid < 7) {
    u64 pc0;
    asm volatile("s_getpc_b64 %0" : "=s"(pc0));
    unsigned ln = idx * 7 + tid;
    ln = ln < CODEPF / 128 ? ln : 0;
    asm volatile("s_mov_b32 m0, %2\n\ts_nop 0\n\tglobal_load_lds_dword %0, %1" ::"v"(ln * 128), "s"(pc0 & ~127ull), "s"(L + SCR_OFF) : "memory");
  }
  asm volatile("" ::"s"(x), "s"(wA), "s"(wB), "s"(woA), "s"(woB), "s"(gw), "s"(ws), "s"(out));
  u64 tag;
  u32x2 gv = {0u, 0u};
  if (tid < 144) gv = *(const u32x2*)(gw + tid * 4);
  const int lin0 = xcd * 30 + idx;
  const bool act2 = idx < 30 && lin0 < 234;
  const int lin = act2 ? lin0 : 0;
  const int rt = (lin * 57) >> 9, ct = lin - rt * 9;
  const int rot2 = 3 * (rt - ((rt * 171) >> 9) * 3);
  const float sg = (grp >= 8) ? 1.4426950408889634f : -1.4426950408889634f;
  const unsigned wsch = ((((lane & 7) >> 1) ^ ((vl >> 1) & 3)) << 5) | ((lane & 1) << 4);
  const unsigned offn = vl * XROW + wsch, offl = ((vl >> 2) * 72 + (vl & 3)) * XROW + wsch;
  auto wo_piece = [&](int kq, int i, int j) {
    const char* b = (const char*)(kq >= 2 ? woB : woA) + ct * 128 + (kq & 1) * (288 * XROW);
    if (i == 8) b += ((j & 1) * 144 + ((j >> 1) ? 52 : 16)) * XROW;
    else b += (((i + rot2) & 7) * 36 + (j >> 1) * 20 + 8 * (j & 1)) * XROW;
    glds16(i == 8 ? offl : offn, b, L + (kq * 9 + i) * 4096 + j * 1024);
  };
  const __amdgpu_buffer_rsrc_t ors = __builtin_amdgcn_make_buffer_rsrc(ws, 0, O_BYTES, 0x00020000);
  const int g = lane >> 4, q = (lane >> 2) & 3, p = lane & 3, c = lane & 15;
  const int cw = wave & 3;

  if (__builtin_expect(wave >= 4, 1)) {
    __builtin_amdgcn_s_setprio(2);
    const int lw = cw, tl256 = tid - 256;
    const unsigned offx = (tile * 128 + 8 * lw + vl) * XROW + ((lane & 7) ^ ((4 * lw + (vl >> 1)) & 7)) * 16;
    const char* w = (const char*)((grp >= 8) ? wB : wA) + (grp & 7) * 144;
    unsigned offw[7], offh;
    auto kchunk = [&](int st) { int kc = st + rot; return kc >= 9 ? kc - 9 : kc; };
    auto issueX = [&](int kc, unsigned sb, int jj) { glds16(offx, (const char*)x + kc * 128 + jj * (32 * XROW), sb + STG_W + (2 + lw + 4 * jj) * 1024); };
    auto issueW = [&](int kc, unsigned sb, int j) { glds16(offw[j], w + kc * (64 * WROW), sb + (lw + 4 * j) * 1024); };
    auto issue6 = [&](int kc, unsigned sb) {
      if (lw < 3) issueW(kc, sb, 6);
      else { glds16(offw[6], (const char*)x + kc * 128, sb + STG_W); glds16(offh, (const char*)x + kc * 128, sb + STG_W + 1024); }
    };
    const int kc0 = rot, kc1 = kchunk(1);
    const unsigned sb0 = L, sb1 = L + STG;
    auto calcw = [&](int j) {
      unsigned qq = 256 * j + tl256;
      unsigned u = (qq * 2428u) >> 16, cc = qq - u * 27;
      unsigned row = (u & 0x38) | ((u & 1) << 2) | ((u >> 1) & 3);
      offw[j] = row * WROW + cc * 16 + ((cc * 57u) >> 9) * 1008;
    };
    issueX(kc0, sb0, 0);
#pragma unroll
    for (int j = 0; j < 6; ++j) {
      SCHED0();
      calcw(j);
      SCHED0();
      issueW(kc0, sb0, j);
      if (j < 3) issueX(kc0, sb0, j + 1);
    }
    calcw(6);
    {
      int t0 = tile * 128 - 16 + vl, t1 = t0 + 8;
      t0 = t0 < 0 ? 0 : t0; t1 = t1 < 0 ? 0 : t1;
      unsigned h0 = t0 * XROW + ((lane & 7) ^ (vl >> 1)) * 16;
      offh = t1 * XROW + ((lane & 7) ^ (4 + (vl >> 1))) * 16;
      offw[6] = lw == 3 ? h0 : offw[6];
    }
    issue6(kc0, sb0);
    STAMP(1);
#pragma unroll
    for (int j = 0; j < PRE0; ++j) issueW(kc1, sb1, j);
    STAMP(2);
    f32x4 he = {0, 0, 0, 0};
    const unsigned aE = L + (8 * g + 2 * q) * PITCH + (lw == 0 ? 144 : 288) + ((p & 1) ? 104 : 32);
    const unsigned aX = L + STG_W + c * 128 + ((g ^ (c >> 1)) * 16);
    frag fe[2], fx[2];
    auto halo_rd = [&](unsigned so) {
      if (lw < 2) {
#pragma unroll
        for (int ks = 0; ks < 2; ++ks) {
          fx[ks] = ld128((aX ^ (ks * 64)) + so);
          fe[ks] = ldW(aE + so + ks * KO, aE + so + ks * KO + PITCH);
        }
      }
    };
    auto halo_mma = [&]() {
      if (lw == 0) { he = MFMA(fe[0], fx[0], he); he = MFMA(fe[1], fx[1], he); }
      if (lw == 1) { he = MFMA(fx[0], fe[0], he); he = MFMA(fx[1], fe[1], he); }
    };
    int slot = 0, kn = kc1;
    asm volatile("s_waitcnt vmcnt(%0)" ::"i"(PRE0) : "memory");
#pragma clang loop unroll(disable)
    for (int st = 0; st < 8; ++st) {
      BARRIER();
      STAMP(3 + st);
      if (st == 0) {
#pragma unroll
        for (int j = PRE0; j < 6; ++j) issueW(kc1, sb1, j);
        tag = __hip_atomic_load((GAS u64*)(ws + EPOCH_OFF), RLX_AGENT);
      }
      const unsigned b1 = L + (slot == 2 ? 0 : slot + 1) * STG, b2 = L + (slot == 0 ? 2 : slot - 1) * STG;
      issue6(kn, b1);
      issueX(kn, b1, 0);
      halo_rd(slot * STG);
      issueX(kn, b1, 1); issueX(kn, b1, 2); issueX(kn, b1, 3);
      SCHED0();
      halo_mma();
      SCHED0();
      kn = kn == 8 ? 0 : kn + 1;
      if (st < 7) {
#pragma unroll
        for (int j = 0; j < 6; ++j) issueW(kn, b2, j);
      } else {
#pragma unroll
        for (int I = 0; I < 6; ++I) wo_piece(0, I, lw);
      }
      slot = slot == 2 ? 0 : slot + 1;
      WAIT_LGKM0(); asm volatile("s_waitcnt vmcnt(6)" ::: "memory");
    }
    BARRIER();
    STAMP(11);
    halo_rd(2 * STG);
    wo_piece(0, 6, lw); wo_piece(0, 7, lw);
    SCHED0();
    halo_mma();
    SCHED0();
    if (lw < 2) {
      u32x2 v = pack4(he);
      if (lw == 0 && tile == 0) v = u32x2{0u, 0u};
      st64(L + EX_OFF + lane * 8 + (lw == 0 ? 4 : 9) * 512, v);
    }
    __builtin_amdgcn_s_setprio(0);
#pragma unroll
    for (int I = 8; I < NBX; ++I) wo_piece(I / 9, I % 9, lw);
    WAIT_LGKM0();
    STAMP(12);
    BARRIER();
    STAMP(13);
#pragma unroll
    for (int I = NBX; I < 34; ++I) wo_piece(I / 9, I % 9, lw);
    WAIT_VM0();
    TAG_SCALAR();
    STAMP(14);
  } else {
    const unsigned wrow = L + (8 * g + 2 * q) * PITCH;
    tag = __hip_atomic_load((GAS u64*)(ws + EPOCH_OFF), RLX_AGENT);
    f32x4 maskP, maskC, maskP0;
    {
      const float ninf = -__builtin_inff();
#pragma unroll
      for (int r = 0; r < 4; ++r) {
        bool pm = (r & 1) == (c & 1);
        maskC[r] = (pm && 4 * g + r <= c) ? 0.f : ninf;
        maskP[r] = (pm && 4 * g + r > c) ? 0.f : ninf;
      }
      maskP0 = maskP + ((tile == 0 && wave == 0) ? -998244352.f * 4.242640687119285f : 0.f);
      asm volatile("" : "+v"(maskP), "+v"(maskC), "+v"(maskP0));
    }
    u32x4 sel;
    {
      const int par = c & 1;
      const bool cur1 = 4 * g + par <= c, cur2 = 4 * g + 2 + par <= c;
      const unsigned s1 = par ? 0x01000c0cu : 0x0c0c0100u;
      const unsigned s2 = par ? 0x03020c0cu : 0x0c0c0302u;
      sel[0] = cur1 ? 0x0c0c0c0cu : s1;
      sel[1] = cur2 ? 0x0c0c0c0cu : s2;
      sel[2] = cur1 ? s1 : 0x0c0c0c0cu;
      sel[3] = cur2 ? s2 : 0x0c0c0c0cu;
    }
    unsigned vmain = lane * 16, vleft = lane < 32 ? c * 16 + g * 8 : 0x40000000u;
    unsigned osoff = (tile * 8 + 2 * wave) * 36864 + grp * 2048, oloff = (tile * 8 + 2 * wave) * 36864 + 32768 + grp * 256;
    GAS u64* myflag = (GAS u64*)(ws + FLAG_OFF + ((grp >> 2) * 64 + tile * 4 + cw) * FS1 + (grp & 3) * FS2);
    PINS(myflag);
    unsigned osoff1 = osoff + 36864, oloff1 = oloff + 36864;
    PINS(osoff); PINS(oloff); PINS(osoff1); PINS(oloff1);
    asm volatile("" : "+v"(sel), "+v"(vmain), "+v"(vleft));
    f32x4 aK[2][4], aQ[2][4], aKQ[2], aV[2][5], hk, hv;
    frag cv[5], xb[3];
    unsigned exr = L + EX_OFF + wave * 5120 + lane * 8, exh = L + EX_OFF + wave * 512 + lane * 8;
    unsigned exgw = L + SCR_OFF + wave * 128 + g * 16, exgr = L + SCR_OFF + wave * 128 + c * 4;
    PIN(exr); PIN(exh); PIN(exgw); PIN(exgr);
    auto mainloop = [&](auto WC) {
      constexpr int W = decltype(WC)::value;
      constexpr int XO = (1 + 2 * W) * 2048;
    hk = f32x4{0, 0, 0, 0}; hv = f32x4{0, 0, 0, 0};
#pragma unroll
    for (int t = 0; t < 2; ++t) {
#pragma unroll
      for (int f = 0; f < 4; ++f) { aK[t][f] = f32x4{0, 0, 0, 0}; aQ[t][f] = f32x4{0, 0, 0, 0}; }
      aKQ[t] = f32x4{0, 0, 0, 0};
#pragma unroll
      for (int f = 0; f < 5; ++f) aV[t][f] = f32x4{0, 0, 0, 0};
    }
    unsigned cW = wrow + 8 * p;
    unsigned cKQ = wrow + (p < 2 ? 144 : 0) + ((p & 1) ? 104 : 32);
    unsigned cX0 = L + STG_W + c * 128 + ((g ^ (c >> 1)) * 16), cX1 = cX0 ^ 64;
    unsigned cV[4];
#pragma unroll
    for (int i = 0; i < 4; ++i)
      cV[i] = p < 2 ? wrow + 288 + (p ? 104 : 32) + (i >> 1) * KO + (i & 1) * PITCH : L + G_OFF + rot * 512 + (8 * g + q) * 8 + (i >> 1) * 256 + (i & 1) * 32;
#pragma unroll
    for (int f = 0; f < 5; ++f) cv[f] = frag{0, 0, 0, 0};
    xb[0] = frag{0, 0, 0, 0}; xb[1] = frag{0, 0, 0, 0}; xb[2] = frag{0, 0, 0, 0};
    asm volatile("" : "+v"(aK[0][0]), "+v"(aK[0][1]), "+v"(aK[0][2]), "+v"(aK[0][3]), "+v"(aK[1][0]), "+v"(aK[1][1]), "+v"(aK[1][2]), "+v"(aK[1][3]),
                      "+v"(aQ[0][0]), "+v"(aQ[0][1]), "+v"(aQ[0][2]), "+v"(aQ[0][3]), "+v"(aQ[1][0]), "+v"(aQ[1][1]), "+v"(aQ[1][2]), "+v"(aQ[1][3]),
                      "+v"(aKQ[0]), "+v"(aKQ[1]), "+v"(hk), "+v"(hv), "+v"(xb[0]), "+v"(xb[1]), "+v"(xb[2]));
    asm volatile("" : "+v"(aV[0][0]), "+v"(aV[0][1]), "+v"(aV[0][2]), "+v"(aV[0][3]), "+v"(aV[0][4]), "+v"(aV[1][0]), "+v"(aV[1][1]), "+v"(aV[1][2]),
                      "+v"(aV[1][3]), "+v"(aV[1][4]), "+v"(cv[0]), "+v"(cv[1]), "+v"(cv[2]), "+v"(cv[3]), "+v"(cv[4]),
                      "+v"(cW), "+v"(cKQ), "+v"(cX0), "+v"(cX1), "+v"(cV[0]), "+v"(cV[1]), "+v"(cV[2]), "+v"(cV[3]));
    SCHED0();
    STAMP(1);
    if (W < 2 || (W == 2 && lane < 16)) {
      unsigned a0 = __builtin_amdgcn_perm(gv[0], gv[0], 0x01000100), a1 = __builtin_amdgcn_perm(gv[0], gv[0], 0x03020302);
      unsigned a2 = __builtin_amdgcn_perm(gv[1], gv[1], 0x01000100), a3 = __builtin_amdgcn_perm(gv[1], gv[1], 0x03020302);
      *(LDSP u32x4*)(size_t)(L + G_OFF + tid * 32) = u32x4{a0, a0, a1, a1};
      *(LDSP u32x4*)(size_t)(L + G_OFF + tid * 32 + 16) = u32x4{a2, a2, a3, a3};
    }
    STAMP(2);
    WAIT_LGKM0();
      int slot = 0, kc = rot;
#pragma clang loop unroll(disable)
      for (int st = 0; st < 9; ++st) {
        BARRIER();
        SCHED0();
        frag xa[3], W0[14], W1[9];
        xa[0] = ld128(cX0 + XO); xa[1] = ld128(cX0 + XO + 2048);
#pragma unroll
        for (int f = 0; f < 4; ++f) W0[f ^ 1] = ldW(cW + 144 + HOFF(f ^ 1), cW + 144 + HOFF(f ^ 1) + PITCH);
        xa[2] = ld128(cX0);
#pragma unroll
        for (int f = 0; f < 5; ++f) { aV[0][f] = MFMA(xb[0], cv[f], aV[0][f]); aV[1][f] = MFMA(xb[1], cv[f], aV[1][f]); }
        hv = MFMA(xb[2], cv[W], hv);
        W0[5] = ldW(cW + HOFF(0), cW + HOFF(0) + PITCH);
        W0[4] = ldW(cKQ, cKQ + PITCH);
        W0[7] = ldW(cW + HOFF(2), cW + HOFF(2) + PITCH);
        W0[6] = ldW(cW + HOFF(1), cW + HOFF(1) + PITCH);
        W0[9] = ldW(cW + 288 + HOFF(0), cW + 288 + HOFF(0) + PITCH);
        W0[8] = ldW(cW + HOFF(3), cW + HOFF(3) + PITCH);
        W0[11] = ldW(cW + 288 + HOFF(2), cW + 288 + HOFF(2) + PITCH);
        W0[10] = ldW(cW + 288 + HOFF(1), cW + 288 + HOFF(1) + PITCH);
        W0[13] = ldW(cV[0], cV[1]);
        W0[12] = ldW(cW + 288 + HOFF(3), cW + 288 + HOFF(3) + PITCH);
        xb[0] = ld128(cX1 + XO); xb[1] = ld128(cX1 + XO + 2048); xb[2] = ld128(cX1);
#pragma unroll
        for (int f = 0; f < 4; ++f) W1[f ^ 1] = ldW(cW + KO + 144 + HOFF(f ^ 1), cW + KO + 144 + HOFF(f ^ 1) + PITCH);
        W1[5] = ldW(cW + KO + HOFF(0), cW + KO + HOFF(0) + PITCH);
        W1[4] = ldW(cKQ + KO, cKQ + KO + PITCH);
        W1[8] = ldW(cW + KO + HOFF(3), cW + KO + HOFF(3) + PITCH);
        W1[7] = ldW(cW + KO + HOFF(2), cW + KO + HOFF(2) + PITCH);
        W1[6] = ldW(cW + KO + HOFF(1), cW + KO + HOFF(1) + PITCH);
#pragma unroll
        for (int f = 0; f < 4; ++f) cv[f] = ldW(cW + KO + 288 + HOFF(f), cW + KO + 288 + HOFF(f) + PITCH);
        cv[4] = ldW(cV[2], cV[3]);
#pragma unroll
        for (int f = 0; f < 4; ++f) { aK[0][f] = MFMA(W0[f], xa[0], aK[0][f]); aK[1][f] = MFMA(W0[f], xa[1], aK[1][f]); }
        hk = MFMA(W0[W], xa[2], hk);
        aKQ[0] = MFMA(W0[4], xa[0], aKQ[0]); aKQ[1] = MFMA(W0[4], xa[1], aKQ[1]);
#pragma unroll
        for (int f = 0; f < 4; ++f) { aQ[0][f] = MFMA(W0[5 + f], xa[0], aQ[0][f]); aQ[1][f] = MFMA(W0[5 + f], xa[1], aQ[1][f]); }
#pragma unroll
        for (int f = 0; f < 5; ++f) { aV[0][f] = MFMA(xa[0], W0[9 + f], aV[0][f]); aV[1][f] = MFMA(xa[1], W0[9 + f], aV[1][f]); }
        hv = MFMA(xa[2], W0[9 + W], hv);
#pragma unroll
        for (int f = 0; f < 4; ++f) { aK[0][f] = MFMA(W1[f], xb[0], aK[0][f]); aK[1][f] = MFMA(W1[f], xb[1], aK[1][f]); }
        hk = MFMA(W1[W], xb[2], hk);
        aKQ[0] = MFMA(W1[4], xb[0], aKQ[0]); aKQ[1] = MFMA(W1[4], xb[1], aKQ[1]);
#pragma unroll
        for (int f = 0; f < 4; ++f) { aQ[0][f] = MFMA(W1[5 + f], xb[0], aQ[0][f]); aQ[1][f] = MFMA(W1[5 + f], xb[1], aQ[1][f]); }
        const int ds = slot == 2 ? -2 * STG : STG, dg = kc == 8 ? -8 * 512 : 512;
        slot = slot == 2 ? 0 : slot + 1;
        kc = kc == 8 ? 0 : kc + 1;
        const int dv = p < 2 ? ds : dg;
        cW += ds; cKQ += ds; cX0 += ds; cX1 += ds;
#pragma unroll
        for (int i = 0; i < 4; ++i) cV[i] += dv;
        SGB(0x100, 2);
#pragma unroll
        for (int i = 0; i < 54; ++i) {
          SGB(0x8, 1);
          if (i < 3 || i == 16 || i == 28 || i == 40) SGB(0x100, 2); else SGB(0x100, 1);
          if (i >= 3 && i < 11) SGB(0x4, 1);
          if (i >= 51) SGB(0x2, 1);
        }
#pragma unroll
        for (int i = 0; i < 4; ++i) { SGB(0x8, 1); SGB(0x2, 2); }
        SGB(0x8, 2);
        SCHED0();
        WAIT_LGKM0();
      }
      hv = MFMA(xb[2], cv[W], hv);
    };
    if (wave == 0) mainloop(std::integral_constant<int, 0>{});
    else if (wave == 1) mainloop(std::integral_constant<int, 1>{});
    else if (wave == 2) mainloop(std::integral_constant<int, 2>{});
    else mainloop(std::integral_constant<int, 3>{});
    STAMP(12);
    const bool g0 = g == 0;
    frag K[3][4], Q[2][4], Vb[5], Va[5];
    u32x2 ks[2], qs[2];
    aV[1][4] = MFMA(xb[1], cv[4], aV[1][4]); PIN(aV[1][4]);
    aV[0][4] = MFMA(xb[0], cv[4], aV[0][4]); PIN(aV[0][4]);
#pragma unroll
    for (int t = 1; t >= 0; --t) {
      u32x2 kq = pack4(aKQ[t]);
      auto r0 = __builtin_amdgcn_permlane32_swap(kq[0], 0u, false, false);
      auto r1 = __builtin_amdgcn_permlane32_swap(kq[1], 0u, false, false);
      ks[t] = u32x2{r0[0], r1[0]}; qs[t] = u32x2{r0[1], r1[1]};
      PIN(ks[t]); PIN(qs[t]);
    }
    SCHED0();
#pragma unroll
    for (int f = 0; f < 4; ++f) { aV[1][f] = MFMA(xb[1], cv[f], aV[1][f]); PIN(aV[1][f]); K[2][f] = cat(pack4(aK[1][f]), ks[1]); PIN(K[2][f]); SCHED0(); }
    {
      u32x2 h = pack4(hk), hh = pack4(hv);
      if (tile == 0) h = u32x2{0u, 0u};
      WR2(exh, h, hh, 0, 5);
    }
    if (c >= 8) { *(LDSP f32x4*)(size_t)(exgw) = aV[0][4]; *(LDSP f32x4*)(size_t)(exgw + 64) = aV[1][4]; }
    SCHED0();
#pragma unroll
    for (int f = 0; f < 4; ++f) {
      aV[0][f] = MFMA(xb[0], cv[f], aV[0][f]); PIN(aV[0][f]);
      u32x2 v = pack4(aV[1][f]); Vb[f][2] = v[0]; Vb[f][3] = v[1]; PIN(Vb[f]); SCHED0();
    }
    { u32x2 v = pack4(aV[1][4]); Vb[4][2] = v[0]; Vb[4][3] = v[1]; PIN(Vb[4]); }
    if (wave < 3) {
#pragma unroll
      for (int f = 0; f < 4; ++f) WR2(exr, (u32x2{K[2][f][0], K[2][f][1]}), (u32x2{Vb[f][2], Vb[f][3]}), 10 + f, 15 + f);
      WR2(exr, ks[1], (u32x2{Vb[4][2], Vb[4][3]}), 14, 19);
    }
    SCHED0();
#pragma unroll
    for (int h = 0; h < 4; ++h) {
      K[1][h] = cat(pack4(aK[0][h]), ks[0]); PIN(K[1][h]);
      u32x2 qt = pack4(aQ[1][h]);
      unsigned e = ((h < 2) == g0) ? qs[1][h & 1] : 0u;
      Q[1][h] = (h & 1) ? frag{qt[0], qt[1], 0u, e} : frag{qt[0], qt[1], e, 0u}; PIN(Q[1][h]);
    }
    SCHED0();
    TAG_SCALAR();
    WAIT_VM0(); WAIT_LGKM0();
    STAMP(23);
    BARRIER();
    STAMP(24);
    SCHED0();
    u32x2 gz;
    asm volatile("ds_read2_b32 %0, %1 offset0:0 offset1:16" : "=v"(gz) : "v"(exgr) : "memory");
#pragma unroll
    for (int h = 0; h < 4; ++h) asm volatile("ds_read2st64_b64 %0, %1 offset0:%2 offset1:4" : "=v"(K[0][h]) : "v"(exr), "i"(h) : "memory");
    f32x4 sc[2][4], sp[2][4];
#pragma unroll
    for (int h = 0; h < 4; ++h) {
      sc[1][h] = MFMA(K[2][h], Q[1][h], maskC); PIN(sc[1][h]);
      sp[1][h] = MFMA(K[1][h], Q[1][h], maskP); PIN(sp[1][h]);
      u32x2 qt = pack4(aQ[0][h]);
      unsigned e = ((h < 2) == g0) ? qs[0][h & 1] : 0u;
      Q[0][h] = (h & 1) ? frag{qt[0], qt[1], 0u, e} : frag{qt[0], qt[1], e, 0u}; PIN(Q[0][h]);
      SCHED0();
    }
    asm volatile("s_waitcnt lgkmcnt(0)" : "+v"(gz), "+v"(K[0][0]), "+v"(K[0][1]), "+v"(K[0][2]), "+v"(K[0][3])::"memory");
    float v[2][4][2];
#pragma unroll
    for (int h = 0; h < 4; ++h) {
      sc[0][h] = MFMA(K[1][h], Q[0][h], maskC); PIN(sc[0][h]);
      sp[0][h] = MFMA(K[0][h], Q[0][h], maskP0); PIN(sp[0][h]);
      v[1][h][0] = fmx(fmx3(sp[1][h][0], sp[1][h][1], sc[1][h][0]), sc[1][h][1]); PIN(v[1][h][0]);
      v[1][h][1] = fmx(fmx3(sp[1][h][2], sp[1][h][3], sc[1][h][2]), sc[1][h][3]); PIN(v[1][h][1]);
      SCHED0();
    }
#pragma unroll
    for (int f = 0; f < 5; ++f) {
      u32x2 a = pack4(aV[0][f]); Vb[f][0] = a[0]; Vb[f][1] = a[1]; PIN(Vb[f]);
      Va[f] = __builtin_shufflevector(Vb[f], Vb[f], -1, -1, 0, 1); PIN(Va[f]);
    }
#pragma unroll
    for (int f = 0; f < 5; ++f) { u32x2 n = *(volatile LDSP u32x2*)(size_t)(exr + (5 + f) * 512); Va[f][0] = n[0]; Va[f][1] = n[1]; }
    u32x4 pk[2];
    auto softmax = [&](float (&w)[4][2], float gate_logit) {
      swap32(w[0][0], w[2][0]); swap32(w[1][0], w[3][0]); swap32(w[0][1], w[2][1]); swap32(w[1][1], w[3][1]);
      swap16(w[0][0], w[1][0]); swap16(w[2][0], w[3][0]); swap16(w[0][1], w[1][1]); swap16(w[2][1], w[3][1]);
      const float cs = 0.23570226039551584f * 1.4426950408889634f;
      float m = fmx(fmx3(fmx3(w[0][0], w[1][0], w[2][0]), w[3][0], w[0][1]), fmx3(w[1][1], w[2][1], w[3][1]));
      float mc = -m * cs;
      f32x2 e[4];
#pragma unroll
      for (int i = 0; i < 4; ++i) {
        f32x2 a = __builtin_elementwise_fma(f32x2{w[i][0], w[i][1]}, f32x2{cs, cs}, f32x2{mc, mc});
        e[i] = f32x2{__builtin_amdgcn_exp2f(a[0]), __builtin_amdgcn_exp2f(a[1])};
      }
      f32x2 s2 = (e[0] + e[1]) + (e[2] + e[3]);
      float s = s2[0] + s2[1];
      float r = __builtin_amdgcn_rcpf(__builtin_fmaf(s, __builtin_amdgcn_exp2f(gate_logit * sg), s));
      float pf[4];
#pragma unroll
      for (int i = 0; i < 4; ++i) { f32x2 pp = e[i] * r; pf[i] = __uint_as_float(cvtpk(pp[0], pp[1])); }
      swap32(pf[0], pf[2]); swap32(pf[1], pf[3]);
      swap16(pf[0], pf[1]); swap16(pf[2], pf[3]);
      return u32x4{__float_as_uint(pf[0]), __float_as_uint(pf[1]), __float_as_uint(pf[2]), __float_as_uint(pf[3])};
    };
    SCHED0();
#pragma unroll
    for (int h = 0; h < 4; ++h) {
      v[0][h][0] = fmx(fmx3(sp[0][h][0], sp[0][h][1], sc[0][h][0]), sc[0][h][1]);
      v[0][h][1] = fmx(fmx3(sp[0][h][2], sp[0][h][3], sc[0][h][2]), sc[0][h][3]);
    }
    pk[1] = softmax(v[1], __uint_as_float(gz[1]));
#pragma unroll
    for (int h = 0; h < 4; ++h) { PIN(v[0][h][0]); PIN(v[0][h][1]); }
    SCHED0();
    pv_store(pk[1], Vb, sel, vmain, vleft, ors, osoff1, oloff1, g0);
    pk[0] = softmax(v[0], __uint_as_float(gz[0]));
    SCHED0();
    pv_store(pk[0], Va, sel, vmain, vleft, ors, osoff, oloff, g0);
    STAMP(25);
    WAIT_VM0();
    if (lane == 0) __hip_atomic_store(myflag, tag, RLX_AGENT);
    STAMP(26);
    RSTAMP(61);
  }
  BARRIER();
  if (!act2) {
    if (bid == 255) {
      if (wave < 4) {
        const int b = wave * 64 + lane;
        GAS u64* fp = (GAS u64*)(ws + FLAG_OFF + (((b & 15) >> 2) * 64 + (b >> 4) * 4) * FS1 + (b & 3) * FS2);
#pragma clang loop unroll(disable)
        for (unsigned spins = 0; spins < (1u << 17); ++spins) {
          u64 v = __hip_atomic_load(fp, RLX_AGENT);
          if (__all(v == tag)) break;
          __builtin_amdgcn_s_sleep(8);
        }
      }
      BARRIER();
      if (tid == 0) __hip_atomic_store((GAS u64*)(ws + EPOCH_OFF), tag, RLX_AGENT);
    }
    return;
  }
  if (cw == 3) {
#pragma unroll
    for (int j = 0; j < 4; ++j) { wo_piece(3, 7, j); wo_piece(3, 8, j); }
  }
  if (wave < 4) phase2<2>(L, ws, ors, tag, rt * 5 + 3, 48, cw, rot2, lane STAMP_PASS);
  else phase2<3>(L, ws, ors, tag, rt * 5, 0, cw, rot2, lane STAMP_PASS);
  STAMP(30);
  if (wave < 5 && rt * 5 + wave < 128) {
    f32x4 s[2][2];
#pragma unroll
    for (int it = 0; it < 2; ++it) {
      const int r = it * 8 + vl;
      const unsigned a0 = L + (wave * 16 + r) * 256 + ((((lane & 7) * 2) ^ r) * 16);
#pragma unroll
      for (int w = 0; w < 4; ++w) {
        f32x4 u0 = *(LDSP f32x4*)(size_t)(a0 + w * 36864), u1 = *(LDSP f32x4*)(size_t)((a0 ^ 16) + w * 36864);
        s[it][0] = w ? s[it][0] + u0 : u0; s[it][1] = w ? s[it][1] + u1 : u1;
      }
    }
#pragma unroll
    for (int it = 0; it < 2; ++it) {
      u32x4 o = {cvtpk(s[it][0][0], s[it][0][1]), cvtpk(s[it][0][2], s[it][0][3]), cvtpk(s[it][1][0], s[it][1][1]), cvtpk(s[it][1][2], s[it][1][3])};
      *(u32x4*)((char*)out + (size_t)(rt * 80 + wave * 16 + it * 8 + vl) * XROW + ct * 128 + (lane & 7) * 16) = o;
    }
  }
  STAMP(31);
  RSTAMP(63);
  STAMP_OUT();
}

extern "C" void kernel_launch(void* const* d_in, const int* in_sizes, int n_in, void* d_out, int out_size, void* d_ws, size_t ws_size,
                              hipStream_t stream) {
  fused_kernel<<<256, 512, 0, stream>>>((const u16*)d_in[0], (const u16*)d_in[1], (const u16*)d_in[4], (const u16*)d_in[2], (const u16*)d_in[5],
                                        (const u16*)d_in[6], (char*)d_ws, (u16*)d_out);
}
```

```cpp
#include <hip/hip_runtime.h>
#include <hip/hip_bf16.h>
#include <stdint.h>
#include <type_traits>

typedef unsigned short u16;
typedef unsigned long long u64;
typedef short s16x4 __attribute__((ext_vector_type(4)));
typedef float f32x2 __attribute__((ext_vector_type(2)));
typedef float f32x4 __attribute__((ext_vector_type(4)));
typedef unsigned u32x2 __attribute__((ext_vector_type(2)));
typedef unsigned u32x4 __attribute__((ext_vector_type(4)));
typedef u32x4 frag;
typedef __bf16 bf16x2 __attribute__((ext_vector_type(2)));
typedef __bf16 bfx8 __attribute__((ext_vector_type(8)));
#define LDSP __attribute__((address_space(3)))
#define GAS __attribute__((address_space(1)))
#define DI __device__ __forceinline__

constexpr int WROW = 3456;
constexpr int XROW = 1152;
constexpr int PITCH = 432;
constexpr int KO = 32 * PITCH;
constexpr int STG_W = 64 * PITCH;
constexpr int STG = STG_W + 144 * 128;
constexpr int G_OFF = 3 * STG;
constexpr int SCR_OFF = G_OFF + 4608;
constexpr int EX_OFF = 143360;
constexpr int LDS_BYTES = 163840;
constexpr int O_BYTES = 128 * 36 * 1024;
constexpr int FLAG_OFF = 8 << 20;
constexpr int EPOCH_OFF = FLAG_OFF + (1 << 20);
#define ROT 11
#define PRE0 5
#define NBX 22
#define FS1 128
#define FS2 8
#define PSLEEP 1
#define CODEPF 22656
#define NB1 5

#define MFMA(a, b, c) __builtin_amdgcn_mfma_f32_16x16x32_bf16(__builtin_bit_cast(bfx8, (a)), __builtin_bit_cast(bfx8, (b)), (c), 0, 0, 0)
#define RLX_AGENT __ATOMIC_RELAXED, __HIP_MEMORY_SCOPE_AGENT
#define HOFF(h) (36 * (h) + ((h) & 1) * 4)

DI void glds16(unsigned voff, const void* sbase, unsigned lds_addr) {
  asm volatile("s_mov_b32 m0, %2\n\ts_nop 0\n\tglobal_load_lds_dwordx4 %0, %1" ::"v"(voff), "s"(sbase), "s"(lds_addr) : "memory");
}
DI u32x2 trd(unsigned a) { return __builtin_bit_cast(u32x2, __builtin_amdgcn_ds_read_tr16_b64_v4i16((LDSP s16x4*)(size_t)a)); }
DI frag ldW(unsigned a0, unsigned a1) {
  u32x2 lo = trd(a0), hi = trd(a1);
  return __builtin_shufflevector(lo, hi, 0, 1, 2, 3);
}
DI frag ld128(unsigned a) { return *(LDSP frag*)(size_t)a; }
DI u32x2 ld64(unsigned a) { return *(LDSP u32x2*)(size_t)a; }
DI void st64(unsigned a, u32x2 v) { *(LDSP u32x2*)(size_t)a = v; }
DI unsigned cvtpk(float a, float b) {
  f32x2 f = {a, b};
  return __builtin_bit_cast(unsigned, __builtin_convertvector(f, bf16x2));
}
DI u32x2 pack4(f32x4 a) { return u32x2{cvtpk(a[0], a[1]), cvtpk(a[2], a[3])}; }
DI frag cat(u32x2 a, u32x2 b) { return __builtin_shufflevector(a, b, 0, 1, 2, 3); }
DI void swap32(float& a, float& b) {
  auto r = __builtin_amdgcn_permlane32_swap(__float_as_uint(a), __float_as_uint(b), false, false);
  a = __uint_as_float(r[0]); b = __uint_as_float(r[1]);
}
DI void swap16(float& a, float& b) {
  auto r = __builtin_amdgcn_permlane16_swap(__float_as_uint(a), __float_as_uint(b), false, false);
  a = __uint_as_float(r[0]); b = __uint_as_float(r[1]);
}
DI float fmx(float a, float b) { return __builtin_elementwise_maximum(a, b); }
DI float fmx3(float a, float b, float c) { return fmx(fmx(a, b), c); }
#define STAMP(i)
#define RSTAMP(i)
#define STAMP_OUT()
#define STAMP_ARG
#define STAMP_PASS
#define STAMP_DECL
#define SGB(m, n) __builtin_amdgcn_sched_group_barrier(m, n, 0)
#define SCHED0() __builtin_amdgcn_sched_barrier(0)
#define WAIT_LGKM0() do { __builtin_amdgcn_s_waitcnt(0xC07F); asm volatile("" ::: "memory"); } while (0)
#define WAIT_VM0() asm volatile("s_waitcnt vmcnt(0)" ::: "memory")
#define BARRIER() __builtin_amdgcn_s_barrier()
#define PIN(x) asm volatile("" : "+v"(x))
#define TAG_SCALAR() do { unsigned lo_ = __builtin_amdgcn_readfirstlane((unsigned)tag), hi_ = __builtin_amdgcn_readfirstlane((unsigned)(tag >> 32)); \
    tag = (((u64)hi_ << 32) | lo_) + 1; asm volatile("" : "+s"(tag)); } while (0)
#define PINS(x) asm volatile("" : "+s"(x))
#define WR2(addr, a, b, o0, o1) asm volatile("ds_write2st64_b64 %0, %1, %2 offset0:%3 offset1:%4" ::"v"(addr), "v"(a), "v"(b), "i"(o0), "i"(o1) : "memory")

DI void pv_store(u32x4 pk, const frag (&V)[5], u32x4 sel, unsigned vmain, unsigned vleft, __amdgpu_buffer_rsrc_t ors, unsigned so, unsigned sl, bool g0) {
  const f32x4 z = {0.f, 0.f, 0.f, 0.f};
  frag P[4];
  f32x4 o[4], os[4];
  auto mkP = [&](int h) {
    unsigned u = pk[h];
    P[h] = frag{__builtin_amdgcn_perm(u, u, sel[0]), __builtin_amdgcn_perm(u, u, sel[1]), __builtin_amdgcn_perm(u, u, sel[2]), __builtin_amdgcn_perm(u, u, sel[3])};
    PIN(P[h]);
  };
  mkP(0); mkP(1);
  SCHED0();
  u32x4 f0, f1;
  os[0] = MFMA(V[4], P[0], z); PIN(os[0]); o[0] = MFMA(V[0], P[0], z); PIN(o[0]); mkP(2); SCHED0();
  os[1] = MFMA(V[4], P[1], z); PIN(os[1]); o[1] = MFMA(V[1], P[1], z); PIN(o[1]); mkP(3); SCHED0();
  os[2] = MFMA(V[4], P[2], z); PIN(os[2]); os[3] = MFMA(V[4], P[3], z); PIN(os[3]);
  o[2] = MFMA(V[2], P[2], z); PIN(o[2]); o[3] = MFMA(V[3], P[3], z); PIN(o[3]); SCHED0();
  f0 = u32x4{cvtpk(o[0][0], o[0][1]), cvtpk(o[0][2], o[0][3]), cvtpk(o[1][0], o[1][1]), cvtpk(o[1][2], o[1][3])};
  PIN(f0); SCHED0();
  __builtin_amdgcn_raw_buffer_store_b128(f0, ors, vmain, so, 16);
  SCHED0();
  u32x2 f2 = {cvtpk(g0 ? os[0][0] : os[2][0], g0 ? os[0][1] : os[2][1]), cvtpk(g0 ? os[1][2] : os[3][2], g0 ? os[1][3] : os[3][3])};
  PIN(f2); SCHED0();
  f1 = u32x4{cvtpk(o[2][0], o[2][1]), cvtpk(o[2][2], o[2][3]), cvtpk(o[3][0], o[3][1]), cvtpk(o[3][2], o[3][3])};
  PIN(f1); PIN(f0); SCHED0();
  __builtin_amdgcn_raw_buffer_store_b128(f1, ors, vmain + 1024, so, 16);
  __builtin_amdgcn_raw_buffer_store_b64(f2, ors, vleft, sl, 16);
  SCHED0();
}

template <int MT>
DI void phase2(unsigned L, char* ws, __amdgpu_buffer_rsrc_t ors, u64 tag, int tt0, int mrow, int kq, int rot2, int lane STAMP_ARG) {
  const int g = lane >> 4, q = (lane >> 2) & 3, p = lane & 3, c = lane & 15;
  const unsigned sw = (2 * g + (q >> 1)) & 3;
  unsigned bw[4];
#pragma unroll
  for (int n = 0; n < 4; ++n) bw[n] = L + kq * 36864 + (4 * g + q) * 128 + 8 * p + ((n ^ sw) * 32);
  frag wf[9][4];
  auto rdw = [&](int i) {
#pragma unroll
    for (int n = 0; n < 4; ++n) wf[i][n] = ldW(bw[n] + i * 4096, bw[n] + i * 4096 + 2048);
  };
  rdw(0); rdw(1); rdw(2); rdw(3); rdw(4);
  SCHED0();
  unsigned so[9], vo[MT];
#pragma unroll
  for (int i = 0; i < 9; ++i) so[i] = (i < 8 ? 8 * kq + ((i + rot2) & 7) : 32 + kq) * 1024;
#pragma unroll
  for (int m = 0; m < MT; ++m) { int tt = tt0 + m; tt = tt > 127 ? 127 : tt; vo[m] = lane * 16 + tt * 36864; }
  int tf = (lane & 4) ? tt0 + MT - 1 : tt0; tf = tf > 127 ? 127 : tf;
  GAS u64* fp = (GAS u64*)(ws + FLAG_OFF + (kq * 64 + (tf >> 1)) * FS1 + (lane & 3) * FS2);
  unsigned rb[4];
#pragma unroll
  for (int n = 0; n < 4; ++n) rb[n] = L + kq * 36864 + (mrow + c) * 256 + (((4 * n + g) ^ c) * 16);
#pragma unroll
  for (int i = 0; i < 9; ++i) PINS(so[i]);
#pragma unroll
  for (int m = 0; m < MT; ++m) PIN(vo[m]);
#pragma unroll
  for (int n = 0; n < 4; ++n) PIN(rb[n]);
  PIN(fp);
  WAIT_LGKM0();
  BARRIER();
  STAMP(27);
#pragma clang loop unroll(disable)
  for (unsigned spins = 0; spins < (1u << 17); ++spins) {
    u64 v = __hip_atomic_load(fp, RLX_AGENT);
    if (__all(v == tag)) break;
    __builtin_amdgcn_s_sleep(PSLEEP);
  }
  __builtin_amdgcn_fence(__ATOMIC_ACQUIRE, "wavefront");
  asm volatile("" ::: "memory");
  STAMP(28);
  RSTAMP(62);
  frag of[9][MT];
  constexpr int WIN = 2;
#pragma unroll
  for (int i = 0; i < WIN; ++i)
#pragma unroll
    for (int m = 0; m < MT; ++m) { of[i][m] = __builtin_amdgcn_raw_buffer_load_b128(ors, vo[m], so[i], 16); SCHED0(); }
  f32x4 acc[MT][4];
#pragma unroll
  for (int i = 0; i < 9; ++i) {
    const bool rd = i >= 2 && i + 3 < 9;
    if (rd) rdw(i + 3);
#pragma unroll
    for (int m = 0; m < MT; ++m) {
#pragma unroll
      for (int n = 0; n < 4; ++n) acc[m][n] = MFMA(wf[i][n], of[i][m], i == 0 ? (f32x4{0, 0, 0, 0}) : acc[m][n]);
      if (i + WIN < 9) of[i + WIN][m] = __builtin_amdgcn_raw_buffer_load_b128(ors, vo[m], so[i + WIN], 16);
    }
#pragma unroll
    for (int m = 0; m < MT; ++m) {
#pragma unroll
      for (int n = 0; n < 4; ++n) { SGB(0x8, 1); if (m < 2 && rd) SGB(0x100, 1); }
      if (i + WIN < 9) SGB(0x20, 1);
    }
    SCHED0();
  }
  STAMP(29);
#pragma unroll
  for (int m = 0; m < MT; ++m)
#pragma unroll
    for (int n = 0; n < 4; ++n) *(LDSP f32x4*)(size_t)(rb[n] + m * 4096) = acc[m][n];
  WAIT_LGKM0();
  BARRIER();
}

extern "C" __global__ __launch_bounds__(512, 2) void fused_kernel(const u16* __restrict__ x, const u16* __restrict__ wA, const u16* __restrict__ wB,
                                                        const u16* __restrict__ woA, const u16* __restrict__ woB, const u16* __restrict__ gw,
                                                        char* __restrict__ ws, u16* __restrict__ out) {
  __shared__ __attribute__((aligned(1024))) char lds[LDS_BYTES];
  const unsigned L = (unsigned)(size_t)(LDSP char*)lds;
  const int bid = blockIdx.x;
  const int xcd = bid & 7, idx = bid >> 3;
  const int tl = idx & 7, gl = idx >> 3;
  const int tile = (xcd & 1) * 8 + tl;
  const int grp = (xcd >> 1) * 4 + gl;
  int rot = ROT == 1 ? tl + 2 * gl : ROT == 2 ? (tl >> 1) * 2 + 2 * gl : ROT == 6 ? (tl % 3) * 3 + (gl % 3) * 3 : ROT == 7 ? (((tl >> 1) + gl) & 3) * 2 + (tl & 1 & (gl >> 1))
          : ROT == 8 ? (((tl + gl) & 3) * 9) >> 2 : ROT == 9 ? ((tl + gl + (xcd >> 1)) % 3) * 3 : ROT == 10 ? (((tl + gl) & 3) * 9 >> 2) + (xcd & 1)
          : ROT == 11 ? ((tl + gl) & 3) * 2 + ((xcd ^ (xcd >> 1)) & 1) : ROT == 12 ? ((tl + gl) % 3) * 3 + ((xcd & 1) + (xcd >> 1)) % 3
          : ROT == 13 ? ((tl + gl) & 1) * 4 + (((xcd & 1) * 2 + (xcd >> 1)) & 3) : ROT == 14 ? (tl + 2 * gl + ((xcd >> 1) & 1)) % 9
          : ROT == 15 ? (((tl + gl) & 3) * 2 + ((xcd ^ (xcd >> 1)) & 1) + (xcd >> 2)) : 0;
  if (rot >= 9) rot -= 9;
  const int tid = threadIdx.x, lane = tid & 63;
  const int wave = __builtin_amdgcn_readfirstlane(tid >> 6);
  const int vl = lane >> 3;
  STAMP_DECL;
  STAMP(0);
  RSTAMP(60);
  if (tid < 7) {
    u64 pc0;
    unsigned len;
    asm volatile("s_getpc_b64 %0\n\ts_mov_b32 %1, .Lfk_tail-fused_kernel+128" : "=s"(pc0), "=s"(len));
    unsigned off = (idx * 7 + tid) * 128;
    off = off < len ? off : 0;
    asm volatile("s_mov_b32 m0, %2\n\ts_nop 0\n\tglobal_load_lds_dword %0, %1" ::"v"(off), "s"(pc0 & ~255ull), "s"(L + SCR_OFF) : "memory");
  }
  asm volatile("" ::"s"(x), "s"(wA), "s"(wB), "s"(woA), "s"(woB), "s"(gw), "s"(ws), "s"(out));
  u64 tag;
  u32x2 gv = {0u, 0u};
  if (tid < 144) gv = *(const u32x2*)(gw + tid * 4);
  const int lin0 = xcd * 30 + idx;
  const bool act2 = idx < 30 && lin0 < 234;
  const int lin = act2 ? lin0 : 0;
  const int rt = (lin * 57) >> 9, ct = lin - rt * 9;
  const int rot2 = 3 * (rt - ((rt * 171) >> 9) * 3);
  const float sg = (grp >= 8) ? 1.4426950408889634f : -1.4426950408889634f;
  const unsigned wsch = ((((lane & 7) >> 1) ^ ((vl >> 1) & 3)) << 5) | ((lane & 1) << 4);
  const unsigned offn = vl * XROW + wsch, offl = ((vl >> 2) * 72 + (vl & 3)) * XROW + wsch;
  auto wo_piece = [&](int kq, int i, int j) {
    const char* b = (const char*)(kq >= 2 ? woB : woA) + ct * 128 + (kq & 1) * (288 * XROW);
    if (i == 8) b += ((j & 1) * 144 + ((j >> 1) ? 52 : 16)) * XROW;
    else b += (((i + rot2) & 7) * 36 + (j >> 1) * 20 + 8 * (j & 1)) * XROW;
    glds16(i == 8 ? offl : offn, b, L + (kq * 9 + i) * 4096 + j * 1024);
  };
  const __amdgpu_buffer_rsrc_t ors = __builtin_amdgcn_make_buffer_rsrc(ws, 0, O_BYTES, 0x00020000);
  const int g = lane >> 4, q = (lane >> 2) & 3, p = lane & 3, c = lane & 15;
  const int cw = wave & 3;

  if (__builtin_expect(wave >= 4, 1)) {
    __builtin_amdgcn_s_setprio(2);
    const int lw = cw, tl256 = tid - 256;
    const unsigned offx = (tile * 128 + 8 * lw + vl) * XROW + ((lane & 7) ^ ((4 * lw + (vl >> 1)) & 7)) * 16;
    const char* w = (const char*)((grp >= 8) ? wB : wA) + (grp & 7) * 144;
    unsigned offw[7], offh;
    auto kchunk = [&](int st) { int kc = st + rot; return kc >= 9 ? kc - 9 : kc; };
    auto issueX = [&](int kc, unsigned sb, int jj) { glds16(offx, (const char*)x + kc * 128 + jj * (32 * XROW), sb + STG_W + (2 + lw + 4 * jj) * 1024); };
    auto issueW = [&](int kc, unsigned sb, int j) { glds16(offw[j], w + kc * (64 * WROW), sb + (lw + 4 * j) * 1024); };
    auto issue6 = [&](int kc, unsigned sb) {
      if (lw < 3) issueW(kc, sb, 6);
      else { glds16(offw[6], (const char*)x + kc * 128, sb + STG_W); glds16(offh, (const char*)x + kc * 128, sb + STG_W + 1024); }
    };
    const int kc0 = rot, kc1 = kchunk(1);
    const unsigned sb0 = L, sb1 = L + STG;
    auto calcw = [&](int j) {
      unsigned qq = 256 * j + tl256;
      unsigned u = (qq * 2428u) >> 16, cc = qq - u * 27;
      unsigned row = (u & 0x38) | ((u & 1) << 2) | ((u >> 1) & 3);
      offw[j] = row * WROW + cc * 16 + ((cc * 57u) >> 9) * 1008;
    };
    issueX(kc0, sb0, 0);
#pragma unroll
    for (int j = 0; j < 6; ++j) {
      SCHED0();
      calcw(j);
      SCHED0();
      issueW(kc0, sb0, j);
      if (j < 3) issueX(kc0, sb0, j + 1);
    }
    calcw(6);
    {
      int t0 = tile * 128 - 16 + vl, t1 = t0 + 8;
      t0 = t0 < 0 ? 0 : t0; t1 = t1 < 0 ? 0 : t1;
      unsigned h0 = t0 * XROW + ((lane & 7) ^ (vl >> 1)) * 16;
      offh = t1 * XROW + ((lane & 7) ^ (4 + (vl >> 1))) * 16;
      offw[6] = lw == 3 ? h0 : offw[6];
    }
    issue6(kc0, sb0);
    STAMP(1);
#pragma unroll
    for (int j = 0; j < PRE0; ++j) issueW(kc1, sb1, j);
    STAMP(2);
    f32x4 he = {0, 0, 0, 0};
    const unsigned aE = L + (8 * g + 2 * q) * PITCH + (lw == 0 ? 144 : 288) + ((p & 1) ? 104 : 32);
    const unsigned aX = L + STG_W + c * 128 + ((g ^ (c >> 1)) * 16);
    frag fe[2], fx[2];
    auto halo_rd = [&](unsigned so) {
      if (lw < 2) {
#pragma unroll
        for (int ks = 0; ks < 2; ++ks) {
          fx[ks] = ld128((aX ^ (ks * 64)) + so);
          fe[ks] = ldW(aE + so + ks * KO, aE + so + ks * KO + PITCH);
        }
      }
    };
    auto halo_mma = [&]() {
      if (lw == 0) { he = MFMA(fe[0], fx[0], he); he = MFMA(fe[1], fx[1], he); }
      if (lw == 1) { he = MFMA(fx[0], fe[0], he); he = MFMA(fx[1], fe[1], he); }
    };
    int slot = 0, kn = kc1;
    asm volatile("s_waitcnt vmcnt(%0)" ::"i"(PRE0) : "memory");
#pragma clang loop unroll(disable)
    for (int st = 0; st < 8; ++st) {
      BARRIER();
      STAMP(3 + st);
      if (st == 0) {
#pragma unroll
        for (int j = PRE0; j < 6; ++j) issueW(kc1, sb1, j);
        tag = __hip_atomic_load((GAS u64*)(ws + EPOCH_OFF), RLX_AGENT);
      }
      const unsigned b1 = L + (slot == 2 ? 0 : slot + 1) * STG, b2 = L + (slot == 0 ? 2 : slot - 1) * STG;
      issue6(kn, b1);
      issueX(kn, b1, 0);
      halo_rd(slot * STG);
      issueX(kn, b1, 1); issueX(kn, b1, 2); issueX(kn, b1, 3);
      SCHED0();
      halo_mma();
      SCHED0();
      kn = kn == 8 ? 0 : kn + 1;
      if (st < 7) {
#pragma unroll
        for (int j = 0; j < 6; ++j) issueW(kn, b2, j);
      } else {
#pragma unroll
        for (int I = 0; I < 6; ++I) wo_piece(0, I, lw);
      }
      slot = slot == 2 ? 0 : slot + 1;
      WAIT_LGKM0(); asm volatile("s_waitcnt vmcnt(6)" ::: "memory");
    }
    BARRIER();
    STAMP(11);
    halo_rd(2 * STG);
    wo_piece(0, 6, lw); wo_piece(0, 7, lw);
    SCHED0();
    halo_mma();
    SCHED0();
    if (lw < 2) {
      u32x2 v = pack4(he);
      if (lw == 0 && tile == 0) v = u32x2{0u, 0u};
      st64(L + EX_OFF + lane * 8 + (lw == 0 ? 4 : 9) * 512, v);
    }
    __builtin_amdgcn_s_setprio(0);
#pragma unroll
    for (int I = 8; I < NBX; ++I) wo_piece(I / 9, I % 9, lw);
    WAIT_LGKM0();
    STAMP(12);
    BARRIER();
    STAMP(13);
#pragma unroll
    for (int I = NBX; I < 34; ++I) wo_piece(I / 9, I % 9, lw);
    WAIT_VM0();
    TAG_SCALAR();
    STAMP(14);
  } else {
    const unsigned wrow = L + (8 * g + 2 * q) * PITCH;
    tag = __hip_atomic_load((GAS u64*)(ws + EPOCH_OFF), RLX_AGENT);
    f32x4 maskP, maskC, maskP0;
    {
      const float ninf = -__builtin_inff();
#pragma unroll
      for (int r = 0; r < 4; ++r) {
        bool pm = (r & 1) == (c & 1);
        maskC[r] = (pm && 4 * g + r <= c) ? 0.f : ninf;
        maskP[r] = (pm && 4 * g + r > c) ? 0.f : ninf;
      }
      maskP0 = maskP + ((tile == 0 && wave == 0) ? -998244352.f * 4.242640687119285f : 0.f);
      asm volatile("" : "+v"(maskP), "+v"(maskC), "+v"(maskP0));
    }
    u32x4 sel;
    {
      const int par = c & 1;
      const bool cur1 = 4 * g + par <= c, cur2 = 4 * g + 2 + par <= c;
      const unsigned s1 = par ? 0x01000c0cu : 0x0c0c0100u;
      const unsigned s2 = par ? 0x03020c0cu : 0x0c0c0302u;
      sel[0] = cur1 ? 0x0c0c0c0cu : s1;
      sel[1] = cur2 ? 0x0c0c0c0cu : s2;
      sel[2] = cur1 ? s1 : 0x0c0c0c0cu;
      sel[3] = cur2 ? s2 : 0x0c0c0c0cu;
    }
    unsigned vmain = lane * 16, vleft = lane < 32 ? c * 16 + g * 8 : 0x40000000u;
    unsigned osoff = (tile * 8 + 2 * wave) * 36864 + grp * 2048, oloff = (tile * 8 + 2 * wave) * 36864 + 32768 + grp * 256;
    GAS u64* myflag = (GAS u64*)(ws + FLAG_OFF + ((grp >> 2) * 64 + tile * 4 + cw) * FS1 + (grp & 3) * FS2);
    PINS(myflag);
    unsigned osoff1 = osoff + 36864, oloff1 = oloff + 36864;
    PINS(osoff); PINS(oloff); PINS(osoff1); PINS(oloff1);
    asm volatile("" : "+v"(sel), "+v"(vmain), "+v"(vleft));
    f32x4 aK[2][4], aQ[2][4], aKQ[2], aV[2][5], hk, hv;
    frag cv[5], xb[3];
    unsigned exr = L + EX_OFF + wave * 5120 + lane * 8, exh = L + EX_OFF + wave * 512 + lane * 8;
    unsigned exgw = L + SCR_OFF + wave * 128 + g * 16, exgr = L + SCR_OFF + wave * 128 + c * 4;
    PIN(exr); PIN(exh); PIN(exgw); PIN(exgr);
    auto mainloop = [&](auto WC) {
      constexpr int W = decltype(WC)::value;
      constexpr int XO = (1 + 2 * W) * 2048;
    hk = f32x4{0, 0, 0, 0}; hv = f32x4{0, 0, 0, 0};
#pragma unroll
    for (int t = 0; t < 2; ++t) {
#pragma unroll
      for (int f = 0; f < 4; ++f) { aK[t][f] = f32x4{0, 0, 0, 0}; aQ[t][f] = f32x4{0, 0, 0, 0}; }
      aKQ[t] = f32x4{0, 0, 0, 0};
#pragma unroll
      for (int f = 0; f < 5; ++f) aV[t][f] = f32x4{0, 0, 0, 0};
    }
    unsigned cW = wrow + 8 * p;
    unsigned cKQ = wrow + (p < 2 ? 144 : 0) + ((p & 1) ? 104 : 32);
    unsigned cX0 = L + STG_W + c * 128 + ((g ^ (c >> 1)) * 16), cX1 = cX0 ^ 64;
    unsigned cV[4];
#pragma unroll
    for (int i = 0; i < 4; ++i)
      cV[i] = p < 2 ? wrow + 288 + (p ? 104 : 32) + (i >> 1) * KO + (i & 1) * PITCH : L + G_OFF + rot * 512 + (8 * g + q) * 8 + (i >> 1) * 256 + (i & 1) * 32;
#pragma unroll
    for (int f = 0; f < 5; ++f) cv[f] = frag{0, 0, 0, 0};
    xb[0] = frag{0, 0, 0, 0}; xb[1] = frag{0, 0, 0, 0}; xb[2] = frag{0, 0, 0, 0};
    asm volatile("" : "+v"(aK[0][0]), "+v"(aK[0][1]), "+v"(aK[0][2]), "+v"(aK[0][3]), "+v"(aK[1][0]), "+v"(aK[1][1]), "+v"(aK[1][2]), "+v"(aK[1][3]),
                      "+v"(aQ[0][0]), "+v"(aQ[0][1]), "+v"(aQ[0][2]), "+v"(aQ[0][3]), "+v"(aQ[1][0]), "+v"(aQ[1][1]), "+v"(aQ[1][2]), "+v"(aQ[1][3]),
                      "+v"(aKQ[0]), "+v"(aKQ[1]), "+v"(hk), "+v"(hv), "+v"(xb[0]), "+v"(xb[1]), "+v"(xb[2]));
    asm volatile("" : "+v"(aV[0][0]), "+v"(aV[0][1]), "+v"(aV[0][2]), "+v"(aV[0][3]), "+v"(aV[0][4]), "+v"(aV[1][0]), "+v"(aV[1][1]), "+v"(aV[1][2]),
                      "+v"(aV[1][3]), "+v"(aV[1][4]), "+v"(cv[0]), "+v"(cv[1]), "+v"(cv[2]), "+v"(cv[3]), "+v"(cv[4]),
                      "+v"(cW), "+v"(cKQ), "+v"(cX0), "+v"(cX1), "+v"(cV[0]), "+v"(cV[1]), "+v"(cV[2]), "+v"(cV[3]));
    SCHED0();
    STAMP(1);
    if (W < 2 || (W == 2 && lane < 16)) {
      unsigned a0 = __builtin_amdgcn_perm(gv[0], gv[0], 0x01000100), a1 = __builtin_amdgcn_perm(gv[0], gv[0], 0x03020302);
      unsigned a2 = __builtin_amdgcn_perm(gv[1], gv[1], 0x01000100), a3 = __builtin_amdgcn_perm(gv[1], gv[1], 0x03020302);
      *(LDSP u32x4*)(size_t)(L + G_OFF + tid * 32) = u32x4{a0, a0, a1, a1};
      *(LDSP u32x4*)(size_t)(L + G_OFF + tid * 32 + 16) = u32x4{a2, a2, a3, a3};
    }
    STAMP(2);
    WAIT_LGKM0();
      int slot = 0, kc = rot;
#pragma clang loop unroll(disable)
      for (int st = 0; st < 9; ++st) {
        BARRIER();
        SCHED0();
        frag xa[3], W0[14], W1[9];
        xa[0] = ld128(cX0 + XO); xa[1] = ld128(cX0 + XO + 2048);
#pragma unroll
        for (int f = 0; f < 4; ++f) W0[f ^ 1] = ldW(cW + 144 + HOFF(f ^ 1), cW + 144 + HOFF(f ^ 1) + PITCH);
        xa[2] = ld128(cX0);
#pragma unroll
        for (int f = 0; f < 5; ++f) { aV[0][f] = MFMA(xb[0], cv[f], aV[0][f]); aV[1][f] = MFMA(xb[1], cv[f], aV[1][f]); }
        hv = MFMA(xb[2], cv[W], hv);
        W0[5] = ldW(cW + HOFF(0), cW + HOFF(0) + PITCH);
        W0[4] = ldW(cKQ, cKQ + PITCH);
        W0[7] = ldW(cW + HOFF(2), cW + HOFF(2) + PITCH);
        W0[6] = ldW(cW + HOFF(1), cW + HOFF(1) + PITCH);
        W0[9] = ldW(cW + 288 + HOFF(0), cW + 288 + HOFF(0) + PITCH);
        W0[8] = ldW(cW + HOFF(3), cW + HOFF(3) + PITCH);
        W0[11] = ldW(cW + 288 + HOFF(2), cW + 288 + HOFF(2) + PITCH);
        W0[10] = ldW(cW + 288 + HOFF(1), cW + 288 + HOFF(1) + PITCH);
        W0[13] = ldW(cV[0], cV[1]);
        W0[12] = ldW(cW + 288 + HOFF(3), cW + 288 + HOFF(3) + PITCH);
        xb[0] = ld128(cX1 + XO); xb[1] = ld128(cX1 + XO + 2048); xb[2] = ld128(cX1);
#pragma unroll
        for (int f = 0; f < 4; ++f) W1[f ^ 1] = ldW(cW + KO + 144 + HOFF(f ^ 1), cW + KO + 144 + HOFF(f ^ 1) + PITCH);
        W1[5] = ldW(cW + KO + HOFF(0), cW + KO + HOFF(0) + PITCH);
        W1[4] = ldW(cKQ + KO, cKQ + KO + PITCH);
        W1[8] = ldW(cW + KO + HOFF(3), cW + KO + HOFF(3) + PITCH);
        W1[7] = ldW(cW + KO + HOFF(2), cW + KO + HOFF(2) + PITCH);
        W1[6] = ldW(cW + KO + HOFF(1), cW + KO + HOFF(1) + PITCH);
#pragma unroll
        for (int f = 0; f < 4; ++f) cv[f] = ldW(cW + KO + 288 + HOFF(f), cW + KO + 288 + HOFF(f) + PITCH);
        cv[4] = ldW(cV[2], cV[3]);
#pragma unroll
        for (int f = 0; f < 4; ++f) { aK[0][f] = MFMA(W0[f], xa[0], aK[0][f]); aK[1][f] = MFMA(W0[f], xa[1], aK[1][f]); }
        hk = MFMA(W0[W], xa[2], hk);
        aKQ[0] = MFMA(W0[4], xa[0], aKQ[0]); aKQ[1] = MFMA(W0[4], xa[1], aKQ[1]);
#pragma unroll
        for (int f = 0; f < 4; ++f) { aQ[0][f] = MFMA(W0[5 + f], xa[0], aQ[0][f]); aQ[1][f] = MFMA(W0[5 + f], xa[1], aQ[1][f]); }
#pragma unroll
        for (int f = 0; f < 5; ++f) { aV[0][f] = MFMA(xa[0], W0[9 + f], aV[0][f]); aV[1][f] = MFMA(xa[1], W0[9 + f], aV[1][f]); }
        hv = MFMA(xa[2], W0[9 + W], hv);
#pragma unroll
        for (int f = 0; f < 4; ++f) { aK[0][f] = MFMA(W1[f], xb[0], aK[0][f]); aK[1][f] = MFMA(W1[f], xb[1], aK[1][f]); }
        hk = MFMA(W1[W], xb[2], hk);
        aKQ[0] = MFMA(W1[4], xb[0], aKQ[0]); aKQ[1] = MFMA(W1[4], xb[1], aKQ[1]);
#pragma unroll
        for (int f = 0; f < 4; ++f) { aQ[0][f] = MFMA(W1[5 + f], xb[0], aQ[0][f]); aQ[1][f] = MFMA(W1[5 + f], xb[1], aQ[1][f]); }
        const int ds = slot == 2 ? -2 * STG : STG, dg = kc == 8 ? -8 * 512 : 512;
        slot = slot == 2 ? 0 : slot + 1;
        kc = kc == 8 ? 0 : kc + 1;
        const int dv = p < 2 ? ds : dg;
        cW += ds; cKQ += ds; cX0 += ds; cX1 += ds;
#pragma unroll
        for (int i = 0; i < 4; ++i) cV[i] += dv;
        SGB(0x100, 2);
#pragma unroll
        for (int i = 0; i < 54; ++i) {
          SGB(0x8, 1);
          if (i < 3 || i == 16 || i == 28 || i == 40) SGB(0x100, 2); else SGB(0x100, 1);
          if (i >= 3 && i < 11) SGB(0x4, 1);
          if (i >= 51) SGB(0x2, 1);
        }
#pragma unroll
        for (int i = 0; i < 4; ++i) { SGB(0x8, 1); SGB(0x2, 2); }
        SGB(0x8, 2);
        SCHED0();
        WAIT_LGKM0();
      }
      hv = MFMA(xb[2], cv[W], hv);
    };
    if (wave == 0) mainloop(std::integral_constant<int, 0>{});
    else if (wave == 1) mainloop(std::integral_constant<int, 1>{});
    else if (wave == 2) mainloop(std::integral_constant<int, 2>{});
    else mainloop(std::integral_constant<int, 3>{});
    STAMP(12);
    const bool g0 = g == 0;
    frag K[3][4], Q[2][4], Vb[5], Va[5];
    u32x2 ks[2], qs[2];
    aV[1][4] = MFMA(xb[1], cv[4], aV[1][4]); PIN(aV[1][4]);
    aV[0][4] = MFMA(xb[0], cv[4], aV[0][4]); PIN(aV[0][4]);
#pragma unroll
    for (int t = 1; t >= 0; --t) {
      u32x2 kq = pack4(aKQ[t]);
      auto r0 = __builtin_amdgcn_permlane32_swap(kq[0], 0u, false, false);
      auto r1 = __builtin_amdgcn_permlane32_swap(kq[1], 0u, false, false);
      ks[t] = u32x2{r0[0], r1[0]}; qs[t] = u32x2{r0[1], r1[1]};
      PIN(ks[t]); PIN(qs[t]);
    }
    SCHED0();
#pragma unroll
    for (int f = 0; f < 4; ++f) { aV[1][f] = MFMA(xb[1], cv[f], aV[1][f]); PIN(aV[1][f]); K[2][f] = cat(pack4(aK[1][f]), ks[1]); PIN(K[2][f]); SCHED0(); }
    {
      u32x2 h = pack4(hk), hh = pack4(hv);
      if (tile == 0) h = u32x2{0u, 0u};
      WR2(exh, h, hh, 0, 5);
    }
    if (c >= 8) { *(LDSP f32x4*)(size_t)(exgw) = aV[0][4]; *(LDSP f32x4*)(size_t)(exgw + 64) = aV[1][4]; }
    SCHED0();
#pragma unroll
    for (int f = 0; f < 4; ++f) {
      aV[0][f] = MFMA(xb[0], cv[f], aV[0][f]); PIN(aV[0][f]);
      u32x2 v = pack4(aV[1][f]); Vb[f][2] = v[0]; Vb[f][3] = v[1]; PIN(Vb[f]); SCHED0();
    }
    { u32x2 v = pack4(aV[1][4]); Vb[4][2] = v[0]; Vb[4][3] = v[1]; PIN(Vb[4]); }
    if (wave < 3) {
#pragma unroll
      for (int f = 0; f < 4; ++f) WR2(exr, (u32x2{K[2][f][0], K[2][f][1]}), (u32x2{Vb[f][2], Vb[f][3]}), 10 + f, 15 + f);
      WR2(exr, ks[1], (u32x2{Vb[4][2], Vb[4][3]}), 14, 19);
    }
    SCHED0();
#pragma unroll
    for (int h = 0; h < 4; ++h) {
      K[1][h] = cat(pack4(aK[0][h]), ks[0]); PIN(K[1][h]);
      u32x2 qt = pack4(aQ[1][h]);
      unsigned e = ((h < 2) == g0) ? qs[1][h & 1] : 0u;
      Q[1][h] = (h & 1) ? frag{qt[0], qt[1], 0u, e} : frag{qt[0], qt[1], e, 0u}; PIN(Q[1][h]);
    }
    SCHED0();
    TAG_SCALAR();
    WAIT_VM0(); WAIT_LGKM0();
    STAMP(23);
    BARRIER();
    STAMP(24);
    SCHED0();
    u32x2 gz;
    asm volatile("ds_read2_b32 %0, %1 offset0:0 offset1:16" : "=v"(gz) : "v"(exgr) : "memory");
#pragma unroll
    for (int h = 0; h < 4; ++h) asm volatile("ds_read2st64_b64 %0, %1 offset0:%2 offset1:4" : "=v"(K[0][h]) : "v"(exr), "i"(h) : "memory");
    f32x4 sc[2][4], sp[2][4];
#pragma unroll
    for (int h = 0; h < 4; ++h) {
      sc[1][h] = MFMA(K[2][h], Q[1][h], maskC); PIN(sc[1][h]);
      sp[1][h] = MFMA(K[1][h], Q[1][h], maskP); PIN(sp[1][h]);
      u32x2 qt = pack4(aQ[0][h]);
      unsigned e = ((h < 2) == g0) ? qs[0][h & 1] : 0u;
      Q[0][h] = (h & 1) ? frag{qt[0], qt[1], 0u, e} : frag{qt[0], qt[1], e, 0u}; PIN(Q[0][h]);
      SCHED0();
    }
    asm volatile("s_waitcnt lgkmcnt(0)" : "+v"(gz), "+v"(K[0][0]), "+v"(K[0][1]), "+v"(K[0][2]), "+v"(K[0][3])::"memory");
    float v[2][4][2];
#pragma unroll
    for (int h = 0; h < 4; ++h) {
      sc[0][h] = MFMA(K[1][h], Q[0][h], maskC); PIN(sc[0][h]);
      sp[0][h] = MFMA(K[0][h], Q[0][h], maskP0); PIN(sp[0][h]);
      v[1][h][0] = fmx(fmx3(sp[1][h][0], sp[1][h][1], sc[1][h][0]), sc[1][h][1]); PIN(v[1][h][0]);
      v[1][h][1] = fmx(fmx3(sp[1][h][2], sp[1][h][3], sc[1][h][2]), sc[1][h][3]); PIN(v[1][h][1]);
      SCHED0();
    }
#pragma unroll
    for (int f = 0; f < 5; ++f) {
      u32x2 a = pack4(aV[0][f]); Vb[f][0] = a[0]; Vb[f][1] = a[1]; PIN(Vb[f]);
      Va[f] = __builtin_shufflevector(Vb[f], Vb[f], -1, -1, 0, 1); PIN(Va[f]);
    }
#pragma unroll
    for (int f = 0; f < 5; ++f) { u32x2 n = *(volatile LDSP u32x2*)(size_t)(exr + (5 + f) * 512); Va[f][0] = n[0]; Va[f][1] = n[1]; }
    u32x4 pk[2];
    auto softmax = [&](float (&w)[4][2], float gate_logit) {
      swap32(w[0][0], w[2][0]); swap32(w[1][0], w[3][0]); swap32(w[0][1], w[2][1]); swap32(w[1][1], w[3][1]);
      swap16(w[0][0], w[1][0]); swap16(w[2][0], w[3][0]); swap16(w[0][1], w[1][1]); swap16(w[2][1], w[3][1]);
      const float cs = 0.23570226039551584f * 1.4426950408889634f;
      float m = fmx(fmx3(fmx3(w[0][0], w[1][0], w[2][0]), w[3][0], w[0][1]), fmx3(w[1][1], w[2][1], w[3][1]));
      float mc = -m * cs;
      f32x2 e[4];
#pragma unroll
      for (int i = 0; i < 4; ++i) {
        f32x2 a = __builtin_elementwise_fma(f32x2{w[i][0], w[i][1]}, f32x2{cs, cs}, f32x2{mc, mc});
        e[i] = f32x2{__builtin_amdgcn_exp2f(a[0]), __builtin_amdgcn_exp2f(a[1])};
      }
      f32x2 s2 = (e[0] + e[1]) + (e[2] + e[3]);
      float s = s2[0] + s2[1];
      float r = __builtin_amdgcn_rcpf(__builtin_fmaf(s, __builtin_amdgcn_exp2f(gate_logit * sg), s));
      float pf[4];
#pragma unroll
      for (int i = 0; i < 4; ++i) { f32x2 pp = e[i] * r; pf[i] = __uint_as_float(cvtpk(pp[0], pp[1])); }
      swap32(pf[0], pf[2]); swap32(pf[1], pf[3]);
      swap16(pf[0], pf[1]); swap16(pf[2], pf[3]);
      return u32x4{__float_as_uint(pf[0]), __float_as_uint(pf[1]), __float_as_uint(pf[2]), __float_as_uint(pf[3])};
    };
    SCHED0();
#pragma unroll
    for (int h = 0; h < 4; ++h) {
      v[0][h][0] = fmx(fmx3(sp[0][h][0], sp[0][h][1], sc[0][h][0]), sc[0][h][1]);
      v[0][h][1] = fmx(fmx3(sp[0][h][2], sp[0][h][3], sc[0][h][2]), sc[0][h][3]);
    }
    pk[1] = softmax(v[1], __uint_as_float(gz[1]));
#pragma unroll
    for (int h = 0; h < 4; ++h) { PIN(v[0][h][0]); PIN(v[0][h][1]); }
    SCHED0();
    pv_store(pk[1], Vb, sel, vmain, vleft, ors, osoff1, oloff1, g0);
    pk[0] = softmax(v[0], __uint_as_float(gz[0]));
    SCHED0();
    pv_store(pk[0], Va, sel, vmain, vleft, ors, osoff, oloff, g0);
    STAMP(25);
    WAIT_VM0();
    if (lane == 0) __hip_atomic_store(myflag, tag, RLX_AGENT);
    STAMP(26);
    RSTAMP(61);
    asm volatile(".Lfk_tail:");
  }
  BARRIER();
  if (!act2) {
    if (bid == 255) {
      if (wave < 4) {
        const int b = wave * 64 + lane;
        GAS u64* fp = (GAS u64*)(ws + FLAG_OFF + (((b & 15) >> 2) * 64 + (b >> 4) * 4) * FS1 + (b & 3) * FS2);
#pragma clang loop unroll(disable)
        for (unsigned spins = 0; spins < (1u << 17); ++spins) {
          u64 v = __hip_atomic_load(fp, RLX_AGENT);
          if (__all(v == tag)) break;
          __builtin_amdgcn_s_sleep(8);
        }
      }
      BARRIER();
      if (tid == 0) __hip_atomic_store((GAS u64*)(ws + EPOCH_OFF), tag, RLX_AGENT);
    }
    return;
  }
  if (cw == 3) {
#pragma unroll
    for (int j = 0; j < 4; ++j) { wo_piece(3, 7, j); wo_piece(3, 8, j); }
  }
  if (wave < 4) phase2<2>(L, ws, ors, tag, rt * 5 + 3, 48, cw, rot2, lane STAMP_PASS);
  else phase2<3>(L, ws, ors, tag, rt * 5, 0, cw, rot2, lane STAMP_PASS);
  STAMP(30);
  if (wave < 5 && rt * 5 + wave < 128) {
    f32x4 s[2][2];
#pragma unroll
    for (int it = 0; it < 2; ++it) {
      const int r = it * 8 + vl;
      const unsigned a0 = L + (wave * 16 + r) * 256 + ((((lane & 7) * 2) ^ r) * 16);
#pragma unroll
      for (int w = 0; w < 4; ++w) {
        f32x4 u0 = *(LDSP f32x4*)(size_t)(a0 + w * 36864), u1 = *(LDSP f32x4*)(size_t)((a0 ^ 16) + w * 36864);
        s[it][0] = w ? s[it][0] + u0 : u0; s[it][1] = w ? s[it][1] + u1 : u1;
      }
    }
#pragma unroll
    for (int it = 0; it < 2; ++it) {
      u32x4 o = {cvtpk(s[it][0][0], s[it][0][1]), cvtpk(s[it][0][2], s[it][0][3]), cvtpk(s[it][1][0], s[it][1][1]), cvtpk(s[it][1][2], s[it][1][3])};
      *(u32x4*)((char*)out + (size_t)(rt * 80 + wave * 16 + it * 8 + vl) * XROW + ct * 128 + (lane & 7) * 16) = o;
    }
  }
  STAMP(31);
  RSTAMP(63);
  STAMP_OUT();
}

extern "C" void kernel_launch(void* const* d_in, const int* in_sizes, int n_in, void* d_out, int out_size, void* d_ws, size_t ws_size,
                              hipStream_t stream) {
  fused_kernel<<<256, 512, 0, stream>>>((const u16*)d_in[0], (const u16*)d_in[1], (const u16*)d_in[4], (const u16*)d_in[2], (const u16*)d_in[5],
                                        (const u16*)d_in[6], (char*)d_ws, (u16*)d_out);
}
```

```cpp
#define WARMUP 64
#include <hip/hip_runtime.h>
#include <hip/hip_bf16.h>
#include <stdint.h>
#include <type_traits>

typedef unsigned short u16;
typedef unsigned long long u64;
typedef short s16x4 __attribute__((ext_vector_type(4)));
typedef float f32x2 __attribute__((ext_vector_type(2)));
typedef float f32x4 __attribute__((ext_vector_type(4)));
typedef unsigned u32x2 __attribute__((ext_vector_type(2)));
typedef unsigned u32x4 __attribute__((ext_vector_type(4)));
typedef u32x4 frag;
typedef __bf16 bf16x2 __attribute__((ext_vector_type(2)));
typedef __bf16 bfx8 __attribute__((ext_vector_type(8)));
#define LDSP __attribute__((address_space(3)))
#define GAS __attribute__((address_space(1)))
#define DI __device__ __forceinline__

constexpr int WROW = 3456;
constexpr int XROW = 1152;
constexpr int PITCH = 432;
constexpr int KO = 32 * PITCH;
constexpr int STG_W = 64 * PITCH;
constexpr int STG = STG_W + 144 * 128;
constexpr int G_OFF = 3 * STG;
constexpr int SCR_OFF = G_OFF + 4608;
constexpr int EX_OFF = 143360;
constexpr int LDS_BYTES = 163840;
constexpr int O_BYTES = 128 * 36 * 1024;
constexpr int FLAG_OFF = 8 << 20;
constexpr int EPOCH_OFF = FLAG_OFF + (1 << 20);
#define ROT 11
#define PRE0 5
#define NBX 22
#define FS1 128
#define FS2 8
#define PSLEEP 1
#define CODEPF 22656
#define NB1 5

#define MFMA(a, b, c) __builtin_amdgcn_mfma_f32_16x16x32_bf16(__builtin_bit_cast(bfx8, (a)), __builtin_bit_cast(bfx8, (b)), (c), 0, 0, 0)
#define RLX_AGENT __ATOMIC_RELAXED, __HIP_MEMORY_SCOPE_AGENT
#define HOFF(h) (36 * (h) + ((h) & 1) * 4)

DI void glds16(unsigned voff, const void* sbase, unsigned lds_addr) {
  asm volatile("s_mov_b32 m0, %2\n\ts_nop 0\n\tglobal_load_lds_dwordx4 %0, %1" ::"v"(voff), "s"(sbase), "s"(lds_addr) : "memory");
}
DI u32x2 trd(unsigned a) { return __builtin_bit_cast(u32x2, __builtin_amdgcn_ds_read_tr16_b64_v4i16((LDSP s16x4*)(size_t)a)); }
DI frag ldW(unsigned a0, unsigned a1) {
  u32x2 lo = trd(a0), hi = trd(a1);
  return __builtin_shufflevector(lo, hi, 0, 1, 2, 3);
}
DI frag ld128(unsigned a) { return *(LDSP frag*)(size_t)a; }
DI u32x2 ld64(unsigned a) { return *(LDSP u32x2*)(size_t)a; }
DI void st64(unsigned a, u32x2 v) { *(LDSP u32x2*)(size_t)a = v; }
DI unsigned cvtpk(float a, float b) {
  f32x2 f = {a, b};
  return __builtin_bit_cast(unsigned, __builtin_convertvector(f, bf16x2));
}
DI u32x2 pack4(f32x4 a) { return u32x2{cvtpk(a[0], a[1]), cvtpk(a[2], a[3])}; }
DI frag cat(u32x2 a, u32x2 b) { return __builtin_shufflevector(a, b, 0, 1, 2, 3); }
DI void swap32(float& a, float& b) {
  auto r = __builtin_amdgcn_permlane32_swap(__float_as_uint(a), __float_as_uint(b), false, false);
  a = __uint_as_float(r[0]); b = __uint_as_float(r[1]);
}
DI void swap16(float& a, float& b) {
  auto r = __builtin_amdgcn_permlane16_swap(__float_as_uint(a), __float_as_uint(b), false, false);
  a = __uint_as_float(r[0]); b = __uint_as_float(r[1]);
}
DI float fmx(float a, float b) { return __builtin_elementwise_maximum(a, b); }
DI float fmx3(float a, float b, float c) { return fmx(fmx(a, b), c); }
#define STAMP(i)
#define RSTAMP(i)
#define STAMP_OUT()
#define STAMP_ARG
#define STAMP_PASS
#define STAMP_DECL
#define SGB(m, n) __builtin_amdgcn_sched_group_barrier(m, n, 0)
#define SCHED0() __builtin_amdgcn_sched_barrier(0)
#define WAIT_LGKM0() do { __builtin_amdgcn_s_waitcnt(0xC07F); asm volatile("" ::: "memory"); } while (0)
#define WAIT_VM0() asm volatile("s_waitcnt vmcnt(0)" ::: "memory")
#define BARRIER() __builtin_amdgcn_s_barrier()
#define PIN(x) asm volatile("" : "+v"(x))
#define TAG_SCALAR() do { unsigned lo_ = __builtin_amdgcn_readfirstlane((unsigned)tag), hi_ = __builtin_amdgcn_readfirstlane((unsigned)(tag >> 32)); \
    tag = (((u64)hi_ << 32) | lo_) + 1; asm volatile("" : "+s"(tag)); } while (0)
#define PINS(x) asm volatile("" : "+s"(x))
#define WR2(addr, a, b, o0, o1) asm volatile("ds_write2st64_b64 %0, %1, %2 offset0:%3 offset1:%4" ::"v"(addr), "v"(a), "v"(b), "i"(o0), "i"(o1) : "memory")

DI void pv_store(u32x4 pk, const frag (&V)[5], u32x4 sel, unsigned vmain, unsigned vleft, __amdgpu_buffer_rsrc_t ors, unsigned so, unsigned sl, bool g0) {
  const f32x4 z = {0.f, 0.f, 0.f, 0.f};
  frag P[4];
  f32x4 o[4], os[4];
  auto mkP = [&](int h) {
    unsigned u = pk[h];
    P[h] = frag{__builtin_amdgcn_perm(u, u, sel[0]), __builtin_amdgcn_perm(u, u, sel[1]), __builtin_amdgcn_perm(u, u, sel[2]), __builtin_amdgcn_perm(u, u, sel[3])};
    PIN(P[h]);
  };
  mkP(0); mkP(1);
  SCHED0();
  u32x4 f0, f1;
  os[0] = MFMA(V[4], P[0], z); PIN(os[0]); o[0] = MFMA(V[0], P[0], z); PIN(o[0]); mkP(2); SCHED0();
  os[1] = MFMA(V[4], P[1], z); PIN(os[1]); o[1] = MFMA(V[1], P[1], z); PIN(o[1]); mkP(3); SCHED0();
  os[2] = MFMA(V[4], P[2], z); PIN(os[2]); os[3] = MFMA(V[4], P[3], z); PIN(os[3]);
  o[2] = MFMA(V[2], P[2], z); PIN(o[2]); o[3] = MFMA(V[3], P[3], z); PIN(o[3]); SCHED0();
  f0 = u32x4{cvtpk(o[0][0], o[0][1]), cvtpk(o[0][2], o[0][3]), cvtpk(o[1][0], o[1][1]), cvtpk(o[1][2], o[1][3])};
  PIN(f0); SCHED0();
  __builtin_amdgcn_raw_buffer_store_b128(f0, ors, vmain, so, 16);
  SCHED0();
  u32x2 f2 = {cvtpk(g0 ? os[0][0] : os[2][0], g0 ? os[0][1] : os[2][1]), cvtpk(g0 ? os[1][2] : os[3][2], g0 ? os[1][3] : os[3][3])};
  PIN(f2); SCHED0();
  f1 = u32x4{cvtpk(o[2][0], o[2][1]), cvtpk(o[2][2], o[2][3]), cvtpk(o[3][0], o[3][1]), cvtpk(o[3][2], o[3][3])};
  PIN(f1); PIN(f0); SCHED0();
  __builtin_amdgcn_raw_buffer_store_b128(f1, ors, vmain + 1024, so, 16);
  __builtin_amdgcn_raw_buffer_store_b64(f2, ors, vleft, sl, 16);
  SCHED0();
}

template <int MT>
DI void phase2(unsigned L, char* ws, __amdgpu_buffer_rsrc_t ors, u64 tag, int tt0, int mrow, int kq, int rot2, int lane STAMP_ARG) {
  const int g = lane >> 4, q = (lane >> 2) & 3, p = lane & 3, c = lane & 15;
  const unsigned sw = (2 * g + (q >> 1)) & 3;
  unsigned bw[4];
#pragma unroll
  for (int n = 0; n < 4; ++n) bw[n] = L + kq * 36864 + (4 * g + q) * 128 + 8 * p + ((n ^ sw) * 32);
  frag wf[9][4];
  auto rdw = [&](int i) {
#pragma unroll
    for (int n = 0; n < 4; ++n) wf[i][n] = ldW(bw[n] + i * 4096, bw[n] + i * 4096 + 2048);
  };
  rdw(0); rdw(1); rdw(2); rdw(3); rdw(4);
  SCHED0();
  unsigned so[9], vo[MT];
#pragma unroll
  for (int i = 0; i < 9; ++i) so[i] = (i < 8 ? 8 * kq + ((i + rot2) & 7) : 32 + kq) * 1024;
#pragma unroll
  for (int m = 0; m < MT; ++m) { int tt = tt0 + m; tt = tt > 127 ? 127 : tt; vo[m] = lane * 16 + tt * 36864; }
  int tf = (lane & 4) ? tt0 + MT - 1 : tt0; tf = tf > 127 ? 127 : tf;
  GAS u64* fp = (GAS u64*)(ws + FLAG_OFF + (kq * 64 + (tf >> 1)) * FS1 + (lane & 3) * FS2);
  unsigned rb[4];
#pragma unroll
  for (int n = 0; n < 4; ++n) rb[n] = L + kq * 36864 + (mrow + c) * 256 + (((4 * n + g) ^ c) * 16);
#pragma unroll
  for (int i = 0; i < 9; ++i) PINS(so[i]);
#pragma unroll
  for (int m = 0; m < MT; ++m) PIN(vo[m]);
#pragma unroll
  for (int n = 0; n < 4; ++n) PIN(rb[n]);
  PIN(fp);
  WAIT_LGKM0();
  BARRIER();
  STAMP(27);
#pragma clang loop unroll(disable)
  for (unsigned spins = 0; spins < (1u << 17); ++spins) {
    u64 v = __hip_atomic_load(fp, RLX_AGENT);
    if (__all(v == tag)) break;
    __builtin_amdgcn_s_sleep(PSLEEP);
  }
  __builtin_amdgcn_fence(__ATOMIC_ACQUIRE, "wavefront");
  asm volatile("" ::: "memory");
  STAMP(28);
  RSTAMP(62);
  frag of[9][MT];
  constexpr int WIN = 2;
#pragma unroll
  for (int i = 0; i < WIN; ++i)
#pragma unroll
    for (int m = 0; m < MT; ++m) { of[i][m] = __builtin_amdgcn_raw_buffer_load_b128(ors, vo[m], so[i], 16); SCHED0(); }
  f32x4 acc[MT][4];
#pragma unroll
  for (int i = 0; i < 9; ++i) {
    const bool rd = i >= 2 && i + 3 < 9;
    if (rd) rdw(i + 3);
#pragma unroll
    for (int m = 0; m < MT; ++m) {
#pragma unroll
      for (int n = 0; n < 4; ++n) acc[m][n] = MFMA(wf[i][n], of[i][m], i == 0 ? (f32x4{0, 0, 0, 0}) : acc[m][n]);
      if (i + WIN < 9) of[i + WIN][m] = __builtin_amdgcn_raw_buffer_load_b128(ors, vo[m], so[i + WIN], 16);
    }
#pragma unroll
    for (int m = 0; m < MT; ++m) {
#pragma unroll
      for (int n = 0; n < 4; ++n) { SGB(0x8, 1); if (m < 2 && rd) SGB(0x100, 1); }
      if (i + WIN < 9) SGB(0x20, 1);
    }
    SCHED0();
  }
  STAMP(29);
#pragma unroll
  for (int m = 0; m < MT; ++m)
#pragma unroll
    for (int n = 0; n < 4; ++n) *(LDSP f32x4*)(size_t)(rb[n] + m * 4096) = acc[m][n];
  WAIT_LGKM0();
  BARRIER();
}

extern "C" __global__ __launch_bounds__(512, 2) void fused_kernel(const u16* __restrict__ x, const u16* __restrict__ wA, const u16* __restrict__ wB,
                                                        const u16* __restrict__ woA, const u16* __restrict__ woB, const u16* __restrict__ gw,
                                                        char* __restrict__ ws, u16* __restrict__ out) {
  __shared__ __attribute__((aligned(1024))) char lds[LDS_BYTES];
  const unsigned L = (unsigned)(size_t)(LDSP char*)lds;
  const int bid = blockIdx.x;
  const int xcd = bid & 7, idx = bid >> 3;
  const int tl = idx & 7, gl = idx >> 3;
  const int tile = (xcd & 1) * 8 + tl;
  const int grp = (xcd >> 1) * 4 + gl;
  int rot = ROT == 1 ? tl + 2 * gl : ROT == 2 ? (tl >> 1) * 2 + 2 * gl : ROT == 6 ? (tl % 3) * 3 + (gl % 3) * 3 : ROT == 7 ? (((tl >> 1) + gl) & 3) * 2 + (tl & 1 & (gl >> 1))
          : ROT == 8 ? (((tl + gl) & 3) * 9) >> 2 : ROT == 9 ? ((tl + gl + (xcd >> 1)) % 3) * 3 : ROT == 10 ? (((tl + gl) & 3) * 9 >> 2) + (xcd & 1)
          : ROT == 11 ? ((tl + gl) & 3) * 2 + ((xcd ^ (xcd >> 1)) & 1) : ROT == 12 ? ((tl + gl) % 3) * 3 + ((xcd & 1) + (xcd >> 1)) % 3
          : ROT == 13 ? ((tl + gl) & 1) * 4 + (((xcd & 1) * 2 + (xcd >> 1)) & 3) : ROT == 14 ? (tl + 2 * gl + ((xcd >> 1) & 1)) % 9
          : ROT == 15 ? (((tl + gl) & 3) * 2 + ((xcd ^ (xcd >> 1)) & 1) + (xcd >> 2)) : 0;
  if (rot >= 9) rot -= 9;
  const int tid = threadIdx.x, lane = tid & 63;
  const int wave = __builtin_amdgcn_readfirstlane(tid >> 6);
  const int vl = lane >> 3;
  STAMP_DECL;
  STAMP(0);
  RSTAMP(60);
  if (tid < 7) {
    u64 pc0;
    unsigned len;
    asm volatile("s_getpc_b64 %0\n\ts_mov_b32 %1, .Lfk_tail-fused_kernel+128" : "=s"(pc0), "=s"(len));
    unsigned off = (idx * 7 + tid) * 128;
    off = off < len ? off : 0;
    asm volatile("s_mov_b32 m0, %2\n\ts_nop 0\n\tglobal_load_lds_dword %0, %1" ::"v"(off), "s"(pc0 & ~255ull), "s"(L + SCR_OFF) : "memory");
  }
  asm volatile("" ::"s"(x), "s"(wA), "s"(wB), "s"(woA), "s"(woB), "s"(gw), "s"(ws), "s"(out));
  u64 tag;
  u32x2 gv = {0u, 0u};
  if (tid < 144) gv = *(const u32x2*)(gw + tid * 4);
  const int lin0 = xcd * 30 + idx;
  const bool act2 = idx < 30 && lin0 < 234;
  const int lin = act2 ? lin0 : 0;
  const int rt = (lin * 57) >> 9, ct = lin - rt * 9;
  const int rot2 = 3 * (rt - ((rt * 171) >> 9) * 3);
  const float sg = (grp >= 8) ? 1.4426950408889634f : -1.4426950408889634f;
  const unsigned wsch = ((((lane & 7) >> 1) ^ ((vl >> 1) & 3)) << 5) | ((lane & 1) << 4);
  const unsigned offn = vl * XROW + wsch, offl = ((vl >> 2) * 72 + (vl & 3)) * XROW + wsch;
  auto wo_piece = [&](int kq, int i, int j) {
    const char* b = (const char*)(kq >= 2 ? woB : woA) + ct * 128 + (kq & 1) * (288 * XROW);
    if (i == 8) b += ((j & 1) * 144 + ((j >> 1) ? 52 : 16)) * XROW;
    else b += (((i + rot2) & 7) * 36 + (j >> 1) * 20 + 8 * (j & 1)) * XROW;
    glds16(i == 8 ? offl : offn, b, L + (kq * 9 + i) * 4096 + j * 1024);
  };
  const __amdgpu_buffer_rsrc_t ors = __builtin_amdgcn_make_buffer_rsrc(ws, 0, O_BYTES, 0x00020000);
  const int g = lane >> 4, q = (lane >> 2) & 3, p = lane & 3, c = lane & 15;
  const int cw = wave & 3;

  if (__builtin_expect(wave >= 4, 1)) {
    __builtin_amdgcn_s_setprio(2);
    const int lw = cw, tl256 = tid - 256;
    const unsigned offx = (tile * 128 + 8 * lw + vl) * XROW + ((lane & 7) ^ ((4 * lw + (vl >> 1)) & 7)) * 16;
    const char* w = (const char*)((grp >= 8) ? wB : wA) + (grp & 7) * 144;
    unsigned offw[7], offh;
    auto kchunk = [&](int st) { int kc = st + rot; return kc >= 9 ? kc - 9 : kc; };
    auto issueX = [&](int kc, unsigned sb, int jj) { glds16(offx, (const char*)x + kc * 128 + jj * (32 * XROW), sb + STG_W + (2 + lw + 4 * jj) * 1024); };
    auto issueW = [&](int kc, unsigned sb, int j) { glds16(offw[j], w + kc * (64 * WROW), sb + (lw + 4 * j) * 1024); };
    auto issue6 = [&](int kc, unsigned sb) {
      if (lw < 3) issueW(kc, sb, 6);
      else { glds16(offw[6], (const char*)x + kc * 128, sb + STG_W); glds16(offh, (const char*)x + kc * 128, sb + STG_W + 1024); }
    };
    const int kc0 = rot, kc1 = kchunk(1);
    const unsigned sb0 = L, sb1 = L + STG;
    auto calcw = [&](int j) {
      unsigned qq = 256 * j + tl256;
      unsigned u = (qq * 2428u) >> 16, cc = qq - u * 27;
      unsigned row = (u & 0x38) | ((u & 1) << 2) | ((u >> 1) & 3);
      offw[j] = row * WROW + cc * 16 + ((cc * 57u) >> 9) * 1008;
    };
    issueX(kc0, sb0, 0);
#pragma unroll
    for (int j = 0; j < 6; ++j) {
      SCHED0();
      calcw(j);
      SCHED0();
      issueW(kc0, sb0, j);
      if (j < 3) issueX(kc0, sb0, j + 1);
    }
    calcw(6);
    {
      int t0 = tile * 128 - 16 + vl, t1 = t0 + 8;
      t0 = t0 < 0 ? 0 : t0; t1 = t1 < 0 ? 0 : t1;
      unsigned h0 = t0 * XROW + ((lane & 7) ^ (vl >> 1)) * 16;
      offh = t1 * XROW + ((lane & 7) ^ (4 + (vl >> 1))) * 16;
      offw[6] = lw == 3 ? h0 : offw[6];
    }
    issue6(kc0, sb0);
    STAMP(1);
#pragma unroll
    for (int j = 0; j < PRE0; ++j) issueW(kc1, sb1, j);
    STAMP(2);
    f32x4 he = {0, 0, 0, 0};
    const unsigned aE = L + (8 * g + 2 * q) * PITCH + (lw == 0 ? 144 : 288) + ((p & 1) ? 104 : 32);
    const unsigned aX = L + STG_W + c * 128 + ((g ^ (c >> 1)) * 16);
    frag fe[2], fx[2];
    auto halo_rd = [&](unsigned so) {
      if (lw < 2) {
#pragma unroll
        for (int ks = 0; ks < 2; ++ks) {
          fx[ks] = ld128((aX ^ (ks * 64)) + so);
          fe[ks] = ldW(aE + so + ks * KO, aE + so + ks * KO + PITCH);
        }
      }
    };
    auto halo_mma = [&]() {
      if (lw == 0) { he = MFMA(fe[0], fx[0], he); he = MFMA(fe[1], fx[1], he); }
      if (lw == 1) { he = MFMA(fx[0], fe[0], he); he = MFMA(fx[1], fe[1], he); }
    };
    int slot = 0, kn = kc1;
    asm volatile("s_waitcnt vmcnt(%0)" ::"i"(PRE0) : "memory");
#pragma clang loop unroll(disable)
    for (int st = 0; st < 8; ++st) {
      BARRIER();
      STAMP(3 + st);
      if (st == 0) {
#pragma unroll
        for (int j = PRE0; j < 6; ++j) issueW(kc1, sb1, j);
        tag = __hip_atomic_load((GAS u64*)(ws + EPOCH_OFF), RLX_AGENT);
      }
      const unsigned b1 = L + (slot == 2 ? 0 : slot + 1) * STG, b2 = L + (slot == 0 ? 2 : slot - 1) * STG;
      issue6(kn, b1);
      issueX(kn, b1, 0);
      halo_rd(slot * STG);
      issueX(kn, b1, 1); issueX(kn, b1, 2); issueX(kn, b1, 3);
      SCHED0();
      halo_mma();
      SCHED0();
      kn = kn == 8 ? 0 : kn + 1;
      if (st < 7) {
#pragma unroll
        for (int j = 0; j < 6; ++j) issueW(kn, b2, j);
      } else {
#pragma unroll
        for (int I = 0; I < 6; ++I) wo_piece(0, I, lw);
      }
      slot = slot == 2 ? 0 : slot + 1;
      WAIT_LGKM0(); asm volatile("s_waitcnt vmcnt(6)" ::: "memory");
    }
    BARRIER();
    STAMP(11);
    halo_rd(2 * STG);
    wo_piece(0, 6, lw); wo_piece(0, 7, lw);
    SCHED0();
    halo_mma();
    SCHED0();
    if (lw < 2) {
      u32x2 v = pack4(he);
      if (lw == 0 && tile == 0) v = u32x2{0u, 0u};
      st64(L + EX_OFF + lane * 8 + (lw == 0 ? 4 : 9) * 512, v);
    }
    __builtin_amdgcn_s_setprio(0);
#pragma unroll
    for (int I = 8; I < NBX; ++I) wo_piece(I / 9, I % 9, lw);
    WAIT_LGKM0();
    STAMP(12);
    BARRIER();
    STAMP(13);
#pragma unroll
    for (int I = NBX; I < 34; ++I) wo_piece(I / 9, I % 9, lw);
    WAIT_VM0();
    TAG_SCALAR();
    STAMP(14);
  } else {
    const unsigned wrow = L + (8 * g + 2 * q) * PITCH;
    tag = __hip_atomic_load((GAS u64*)(ws + EPOCH_OFF), RLX_AGENT);
    f32x4 maskP, maskC, maskP0;
    {
      const float ninf = -__builtin_inff();
#pragma unroll
      for (int r = 0; r < 4; ++r) {
        bool pm = (r & 1) == (c & 1);
        maskC[r] = (pm && 4 * g + r <= c) ? 0.f : ninf;
        maskP[r] = (pm && 4 * g + r > c) ? 0.f : ninf;
      }
      maskP0 = maskP + ((tile == 0 && wave == 0) ? -998244352.f * 4.242640687119285f : 0.f);
      asm volatile("" : "+v"(maskP), "+v"(maskC), "+v"(maskP0));
    }
    u32x4 sel;
    {
      const int par = c & 1;
      const bool cur1 = 4 * g + par <= c, cur2 = 4 * g + 2 + par <= c;
      const unsigned s1 = par ? 0x01000c0cu : 0x0c0c0100u;
      const unsigned s2 = par ? 0x03020c0cu : 0x0c0c0302u;
      sel[0] = cur1 ? 0x0c0c0c0cu : s1;
      sel[1] = cur2 ? 0x0c0c0c0cu : s2;
      sel[2] = cur1 ? s1 : 0x0c0c0c0cu;
      sel[3] = cur2 ? s2 : 0x0c0c0c0cu;
    }
    unsigned vmain = lane * 16, vleft = lane < 32 ? c * 16 + g * 8 : 0x40000000u;
    unsigned osoff = (tile * 8 + 2 * wave) * 36864 + grp * 2048, oloff = (tile * 8 + 2 * wave) * 36864 + 32768 + grp * 256;
    GAS u64* myflag = (GAS u64*)(ws + FLAG_OFF + ((grp >> 2) * 64 + tile * 4 + cw) * FS1 + (grp & 3) * FS2);
    PINS(myflag);
    unsigned osoff1 = osoff + 36864, oloff1 = oloff + 36864;
    PINS(osoff); PINS(oloff); PINS(osoff1); PINS(oloff1);
    asm volatile("" : "+v"(sel), "+v"(vmain), "+v"(vleft));
    f32x4 aK[2][4], aQ[2][4], aKQ[2], aV[2][5], hk, hv;
    frag cv[5], xb[3];
    unsigned exr = L + EX_OFF + wave * 5120 + lane * 8, exh = L + EX_OFF + wave * 512 + lane * 8;
    unsigned exgw = L + SCR_OFF + wave * 128 + g * 16, exgr = L + SCR_OFF + wave * 128 + c * 4;
    PIN(exr); PIN(exh); PIN(exgw); PIN(exgr);
    {
      frag ra = {lane * 0x9E3779B1u, lane * 0x85EBCA77u + 77u, lane * 0xC2B2AE3Du + 5u, lane * 0x27D4EB2Fu + 9u};
      ra = (ra & 0x81ff81ffu) | 0x3c003c00u;
      frag rb = (ra >> 3 & 0x01ff01ffu) | 0x3c003c00u | (ra << 9 & 0x80008000u);
      f32x4 d[4] = {};
#pragma clang loop unroll(disable)
      for (int i = 0; i < WARMUP / 8; ++i) {
#pragma unroll
        for (int j = 0; j < 4; ++j) { d[j] = MFMA(ra, rb, d[j]); }
#pragma unroll
        for (int j = 0; j < 4; ++j) { d[j] = MFMA(rb, ra, d[j]); }
      }
      asm volatile("" ::"v"(d[0]), "v"(d[1]), "v"(d[2]), "v"(d[3]));
    }
    auto mainloop = [&](auto WC) {
      constexpr int W = decltype(WC)::value;
      constexpr int XO = (1 + 2 * W) * 2048;
    hk = f32x4{0, 0, 0, 0}; hv = f32x4{0, 0, 0, 0};
#pragma unroll
    for (int t = 0; t < 2; ++t) {
#pragma unroll
      for (int f = 0; f < 4; ++f) { aK[t][f] = f32x4{0, 0, 0, 0}; aQ[t][f] = f32x4{0, 0, 0, 0}; }
      aKQ[t] = f32x4{0, 0, 0, 0};
#pragma unroll
      for (int f = 0; f < 5; ++f) aV[t][f] = f32x4{0, 0, 0, 0};
    }
    unsigned cW = wrow + 8 * p;
    unsigned cKQ = wrow + (p < 2 ? 144 : 0) + ((p & 1) ? 104 : 32);
    unsigned cX0 = L + STG_W + c * 128 + ((g ^ (c >> 1)) * 16), cX1 = cX0 ^ 64;
    unsigned cV[4];
#pragma unroll
    for (int i = 0; i < 4; ++i)
      cV[i] = p < 2 ? wrow + 288 + (p ? 104 : 32) + (i >> 1) * KO + (i & 1) * PITCH : L + G_OFF + rot * 512 + (8 * g + q) * 8 + (i >> 1) * 256 + (i & 1) * 32;
#pragma unroll
    for (int f = 0; f < 5; ++f) cv[f] = frag{0, 0, 0, 0};
    xb[0] = frag{0, 0, 0, 0}; xb[1] = frag{0, 0, 0, 0}; xb[2] = frag{0, 0, 0, 0};
    asm volatile("" : "+v"(aK[0][0]), "+v"(aK[0][1]), "+v"(aK[0][2]), "+v"(aK[0][3]), "+v"(aK[1][0]), "+v"(aK[1][1]), "+v"(aK[1][2]), "+v"(aK[1][3]),
                      "+v"(aQ[0][0]), "+v"(aQ[0][1]), "+v"(aQ[0][2]), "+v"(aQ[0][3]), "+v"(aQ[1][0]), "+v"(aQ[1][1]), "+v"(aQ[1][2]), "+v"(aQ[1][3]),
                      "+v"(aKQ[0]), "+v"(aKQ[1]), "+v"(hk), "+v"(hv), "+v"(xb[0]), "+v"(xb[1]), "+v"(xb[2]));
    asm volatile("" : "+v"(aV[0][0]), "+v"(aV[0][1]), "+v"(aV[0][2]), "+v"(aV[0][3]), "+v"(aV[0][4]), "+v"(aV[1][0]), "+v"(aV[1][1]), "+v"(aV[1][2]),
                      "+v"(aV[1][3]), "+v"(aV[1][4]), "+v"(cv[0]), "+v"(cv[1]), "+v"(cv[2]), "+v"(cv[3]), "+v"(cv[4]),
                      "+v"(cW), "+v"(cKQ), "+v"(cX0), "+v"(cX1), "+v"(cV[0]), "+v"(cV[1]), "+v"(cV[2]), "+v"(cV[3]));
    SCHED0();
    STAMP(1);
    if (W < 2 || (W == 2 && lane < 16)) {
      unsigned a0 = __builtin_amdgcn_perm(gv[0], gv[0], 0x01000100), a1 = __builtin_amdgcn_perm(gv[0], gv[0], 0x03020302);
      unsigned a2 = __builtin_amdgcn_perm(gv[1], gv[1], 0x01000100), a3 = __builtin_amdgcn_perm(gv[1], gv[1], 0x03020302);
      *(LDSP u32x4*)(size_t)(L + G_OFF + tid * 32) = u32x4{a0, a0, a1, a1};
      *(LDSP u32x4*)(size_t)(L + G_OFF + tid * 32 + 16) = u32x4{a2, a2, a3, a3};
    }
    STAMP(2);
    WAIT_LGKM0();
      int slot = 0, kc = rot;
#pragma clang loop unroll(disable)
      for (int st = 0; st < 9; ++st) {
        BARRIER();
        SCHED0();
        frag xa[3], W0[14], W1[9];
        xa[0] = ld128(cX0 + XO); xa[1] = ld128(cX0 + XO + 2048);
#pragma unroll
        for (int f = 0; f < 4; ++f) W0[f ^ 1] = ldW(cW + 144 + HOFF(f ^ 1), cW + 144 + HOFF(f ^ 1) + PITCH);
        xa[2] = ld128(cX0);
#pragma unroll
        for (int f = 0; f < 5; ++f) { aV[0][f] = MFMA(xb[0], cv[f], aV[0][f]); aV[1][f] = MFMA(xb[1], cv[f], aV[1][f]); }
        hv = MFMA(xb[2], cv[W], hv);
        W0[5] = ldW(cW + HOFF(0), cW + HOFF(0) + PITCH);
        W0[4] = ldW(cKQ, cKQ + PITCH);
        W0[7] = ldW(cW + HOFF(2), cW + HOFF(2) + PITCH);
        W0[6] = ldW(cW + HOFF(1), cW + HOFF(1) + PITCH);
        W0[9] = ldW(cW + 288 + HOFF(0), cW + 288 + HOFF(0) + PITCH);
        W0[8] = ldW(cW + HOFF(3), cW + HOFF(3) + PITCH);
        W0[11] = ldW(cW + 288 + HOFF(2), cW + 288 + HOFF(2) + PITCH);
        W0[10] = ldW(cW + 288 + HOFF(1), cW + 288 + HOFF(1) + PITCH);
        W0[13] = ldW(cV[0], cV[1]);
        W0[12] = ldW(cW + 288 + HOFF(3), cW + 288 + HOFF(3) + PITCH);
        xb[0] = ld128(cX1 + XO); xb[1] = ld128(cX1 + XO + 2048); xb[2] = ld128(cX1);
#pragma unroll
        for (int f = 0; f < 4; ++f) W1[f ^ 1] = ldW(cW + KO + 144 + HOFF(f ^ 1), cW + KO + 144 + HOFF(f ^ 1) + PITCH);
        W1[5] = ldW(cW + KO + HOFF(0), cW + KO + HOFF(0) + PITCH);
        W1[4] = ldW(cKQ + KO, cKQ + KO + PITCH);
        W1[8] = ldW(cW + KO + HOFF(3), cW + KO + HOFF(3) + PITCH);
        W1[7] = ldW(cW + KO + HOFF(2), cW + KO + HOFF(2) + PITCH);
        W1[6] = ldW(cW + KO + HOFF(1), cW + KO + HOFF(1) + PITCH);
#pragma unroll
        for (int f = 0; f < 4; ++f) cv[f] = ldW(cW + KO + 288 + HOFF(f), cW + KO + 288 + HOFF(f) + PITCH);
        cv[4] = ldW(cV[2], cV[3]);
#pragma unroll
        for (int f = 0; f < 4; ++f) { aK[0][f] = MFMA(W0[f], xa[0], aK[0][f]); aK[1][f] = MFMA(W0[f], xa[1], aK[1][f]); }
        hk = MFMA(W0[W], xa[2], hk);
        aKQ[0] = MFMA(W0[4], xa[0], aKQ[0]); aKQ[1] = MFMA(W0[4], xa[1], aKQ[1]);
#pragma unroll
        for (int f = 0; f < 4; ++f) { aQ[0][f] = MFMA(W0[5 + f], xa[0], aQ[0][f]); aQ[1][f] = MFMA(W0[5 + f], xa[1], aQ[1][f]); }
#pragma unroll
        for (int f = 0; f < 5; ++f) { aV[0][f] = MFMA(xa[0], W0[9 + f], aV[0][f]); aV[1][f] = MFMA(xa[1], W0[9 + f], aV[1][f]); }
        hv = MFMA(xa[2], W0[9 + W], hv);
#pragma unroll
        for (int f = 0; f < 4; ++f) { aK[0][f] = MFMA(W1[f], xb[0], aK[0][f]); aK[1][f] = MFMA(W1[f], xb[1], aK[1][f]); }
        hk = MFMA(W1[W], xb[2], hk);
        aKQ[0] = MFMA(W1[4], xb[0], aKQ[0]); aKQ[1] = MFMA(W1[4], xb[1], aKQ[1]);
#pragma unroll
        for (int f = 0; f < 4; ++f) { aQ[0][f] = MFMA(W1[5 + f], xb[0], aQ[0][f]); aQ[1][f] = MFMA(W1[5 + f], xb[1], aQ[1][f]); }
        const int ds = slot == 2 ? -2 * STG : STG, dg = kc == 8 ? -8 * 512 : 512;
        slot = slot == 2 ? 0 : slot + 1;
        kc = kc == 8 ? 0 : kc + 1;
        const int dv = p < 2 ? ds : dg;
        cW += ds; cKQ += ds; cX0 += ds; cX1 += ds;
#pragma unroll
        for (int i = 0; i < 4; ++i) cV[i] += dv;
        SGB(0x100, 2);
#pragma unroll
        for (int i = 0; i < 54; ++i) {
          SGB(0x8, 1);
          if (i < 3 || i == 16 || i == 28 || i == 40) SGB(0x100, 2); else SGB(0x100, 1);
          if (i >= 3 && i < 11) SGB(0x4, 1);
          if (i >= 51) SGB(0x2, 1);
        }
#pragma unroll
        for (int i = 0; i < 4; ++i) { SGB(0x8, 1); SGB(0x2, 2); }
        SGB(0x8, 2);
        SCHED0();
        WAIT_LGKM0();
      }
      hv = MFMA(xb[2], cv[W], hv);
    };
    if (wave == 0) mainloop(std::integral_constant<int, 0>{});
    else if (wave == 1) mainloop(std::integral_constant<int, 1>{});
    else if (wave == 2) mainloop(std::integral_constant<int, 2>{});
    else mainloop(std::integral_constant<int, 3>{});
    STAMP(12);
    const bool g0 = g == 0;
    frag K[3][4], Q[2][4], Vb[5], Va[5];
    u32x2 ks[2], qs[2];
    aV[1][4] = MFMA(xb[1], cv[4], aV[1][4]); PIN(aV[1][4]);
    aV[0][4] = MFMA(xb[0], cv[4], aV[0][4]); PIN(aV[0][4]);
#pragma unroll
    for (int t = 1; t >= 0; --t) {
      u32x2 kq = pack4(aKQ[t]);
      auto r0 = __builtin_amdgcn_permlane32_swap(kq[0], 0u, false, false);
      auto r1 = __builtin_amdgcn_permlane32_swap(kq[1], 0u, false, false);
      ks[t] = u32x2{r0[0], r1[0]}; qs[t] = u32x2{r0[1], r1[1]};
      PIN(ks[t]); PIN(qs[t]);
    }
    SCHED0();
#pragma unroll
    for (int f = 0; f < 4; ++f) { aV[1][f] = MFMA(xb[1], cv[f], aV[1][f]); PIN(aV[1][f]); K[2][f] = cat(pack4(aK[1][f]), ks[1]); PIN(K[2][f]); SCHED0(); }
    {
      u32x2 h = pack4(hk), hh = pack4(hv);
      if (tile == 0) h = u32x2{0u, 0u};
      WR2(exh, h, hh, 0, 5);
    }
    if (c >= 8) { *(LDSP f32x4*)(size_t)(exgw) = aV[0][4]; *(LDSP f32x4*)(size_t)(exgw + 64) = aV[1][4]; }
    SCHED0();
#pragma unroll
    for (int f = 0; f < 4; ++f) {
      aV[0][f] = MFMA(xb[0], cv[f], aV[0][f]); PIN(aV[0][f]);
      u32x2 v = pack4(aV[1][f]); Vb[f][2] = v[0]; Vb[f][3] = v[1]; PIN(Vb[f]); SCHED0();
    }
    { u32x2 v = pack4(aV[1][4]); Vb[4][2] = v[0]; Vb[4][3] = v[1]; PIN(Vb[4]); }
    if (wave < 3) {
#pragma unroll
      for (int f = 0; f < 4; ++f) WR2(exr, (u32x2{K[2][f][0], K[2][f][1]}), (u32x2{Vb[f][2], Vb[f][3]}), 10 + f, 15 + f);
      WR2(exr, ks[1], (u32x2{Vb[4][2], Vb[4][3]}), 14, 19);
    }
    SCHED0();
#pragma unroll
    for (int h = 0; h < 4; ++h) {
      K[1][h] = cat(pack4(aK[0][h]), ks[0]); PIN(K[1][h]);
      u32x2 qt = pack4(aQ[1][h]);
      unsigned e = ((h < 2) == g0) ? qs[1][h & 1] : 0u;
      Q[1][h] = (h & 1) ? frag{qt[0], qt[1], 0u, e} : frag{qt[0], qt[1], e, 0u}; PIN(Q[1][h]);
    }
    SCHED0();
    TAG_SCALAR();
    WAIT_VM0(); WAIT_LGKM0();
    STAMP(23);
    BARRIER();
    STAMP(24);
    SCHED0();
    u32x2 gz;
    asm volatile("ds_read2_b32 %0, %1 offset0:0 offset1:16" : "=v"(gz) : "v"(exgr) : "memory");
#pragma unroll
    for (int h = 0; h < 4; ++h) asm volatile("ds_read2st64_b64 %0, %1 offset0:%2 offset1:4" : "=v"(K[0][h]) : "v"(exr), "i"(h) : "memory");
    f32x4 sc[2][4], sp[2][4];
#pragma unroll
    for (int h = 0; h < 4; ++h) {
      sc[1][h] = MFMA(K[2][h], Q[1][h], maskC); PIN(sc[1][h]);
      sp[1][h] = MFMA(K[1][h], Q[1][h], maskP); PIN(sp[1][h]);
      u32x2 qt = pack4(aQ[0][h]);
      unsigned e = ((h < 2) == g0) ? qs[0][h & 1] : 0u;
      Q[0][h] = (h & 1) ? frag{qt[0], qt[1], 0u, e} : frag{qt[0], qt[1], e, 0u}; PIN(Q[0][h]);
      SCHED0();
    }
    asm volatile("s_waitcnt lgkmcnt(0)" : "+v"(gz), "+v"(K[0][0]), "+v"(K[0][1]), "+v"(K[0][2]), "+v"(K[0][3])::"memory");
    float v[2][4][2];
#pragma unroll
    for (int h = 0; h < 4; ++h) {
      sc[0][h] = MFMA(K[1][h], Q[0][h], maskC); PIN(sc[0][h]);
      sp[0][h] = MFMA(K[0][h], Q[0][h], maskP0); PIN(sp[0][h]);
      v[1][h][0] = fmx(fmx3(sp[1][h][0], sp[1][h][1], sc[1][h][0]), sc[1][h][1]); PIN(v[1][h][0]);
      v[1][h][1] = fmx(fmx3(sp[1][h][2], sp[1][h][3], sc[1][h][2]), sc[1][h][3]); PIN(v[1][h][1]);
      SCHED0();
    }
#pragma unroll
    for (int f = 0; f < 5; ++f) {
      u32x2 a = pack4(aV[0][f]); Vb[f][0] = a[0]; Vb[f][1] = a[1]; PIN(Vb[f]);
      Va[f] = __builtin_shufflevector(Vb[f], Vb[f], -1, -1, 0, 1); PIN(Va[f]);
    }
#pragma unroll
    for (int f = 0; f < 5; ++f) { u32x2 n = *(volatile LDSP u32x2*)(size_t)(exr + (5 + f) * 512); Va[f][0] = n[0]; Va[f][1] = n[1]; }
    u32x4 pk[2];
    auto softmax = [&](float (&w)[4][2], float gate_logit) {
      swap32(w[0][0], w[2][0]); swap32(w[1][0], w[3][0]); swap32(w[0][1], w[2][1]); swap32(w[1][1], w[3][1]);
      swap16(w[0][0], w[1][0]); swap16(w[2][0], w[3][0]); swap16(w[0][1], w[1][1]); swap16(w[2][1], w[3][1]);
      const float cs = 0.23570226039551584f * 1.4426950408889634f;
      float m = fmx(fmx3(fmx3(w[0][0], w[1][0], w[2][0]), w[3][0], w[0][1]), fmx3(w[1][1], w[2][1], w[3][1]));
      float mc = -m * cs;
      f32x2 e[4];
#pragma unroll
      for (int i = 0; i < 4; ++i) {
        f32x2 a = __builtin_elementwise_fma(f32x2{w[i][0], w[i][1]}, f32x2{cs, cs}, f32x2{mc, mc});
        e[i] = f32x2{__builtin_amdgcn_exp2f(a[0]), __builtin_amdgcn_exp2f(a[1])};
      }
      f32x2 s2 = (e[0] + e[1]) + (e[2] + e[3]);
      float s = s2[0] + s2[1];
      float r = __builtin_amdgcn_rcpf(__builtin_fmaf(s, __builtin_amdgcn_exp2f(gate_logit * sg), s));
      float pf[4];
#pragma unroll
      for (int i = 0; i < 4; ++i) { f32x2 pp = e[i] * r; pf[i] = __uint_as_float(cvtpk(pp[0], pp[1])); }
      swap32(pf[0], pf[2]); swap32(pf[1], pf[3]);
      swap16(pf[0], pf[1]); swap16(pf[2], pf[3]);
      return u32x4{__float_as_uint(pf[0]), __float_as_uint(pf[1]), __float_as_uint(pf[2]), __float_as_uint(pf[3])};
    };
    SCHED0();
#pragma unroll
    for (int h = 0; h < 4; ++h) {
      v[0][h][0] = fmx(fmx3(sp[0][h][0], sp[0][h][1], sc[0][h][0]), sc[0][h][1]);
      v[0][h][1] = fmx(fmx3(sp[0][h][2], sp[0][h][3], sc[0][h][2]), sc[0][h][3]);
    }
    pk[1] = softmax(v[1], __uint_as_float(gz[1]));
#pragma unroll
    for (int h = 0; h < 4; ++h) { PIN(v[0][h][0]); PIN(v[0][h][1]); }
    SCHED0();
    pv_store(pk[1], Vb, sel, vmain, vleft, ors, osoff1, oloff1, g0);
    pk[0] = softmax(v[0], __uint_as_float(gz[0]));
    SCHED0();
    pv_store(pk[0], Va, sel, vmain, vleft, ors, osoff, oloff, g0);
    STAMP(25);
    WAIT_VM0();
    if (lane == 0) __hip_atomic_store(myflag, tag, RLX_AGENT);
    STAMP(26);
    RSTAMP(61);
    asm volatile(".Lfk_tail:");
  }
  BARRIER();
  if (!act2) {
    if (bid == 255) {
      if (wave < 4) {
        const int b = wave * 64 + lane;
        GAS u64* fp = (GAS u64*)(ws + FLAG_OFF + (((b & 15) >> 2) * 64 + (b >> 4) * 4) * FS1 + (b & 3) * FS2);
#pragma clang loop unroll(disable)
        for (unsigned spins = 0; spins < (1u << 17); ++spins) {
          u64 v = __hip_atomic_load(fp, RLX_AGENT);
          if (__all(v == tag)) break;
          __builtin_amdgcn_s_sleep(8);
        }
      }
      BARRIER();
      if (tid == 0) __hip_atomic_store((GAS u64*)(ws + EPOCH_OFF), tag, RLX_AGENT);
    }
    return;
  }
  if (cw == 3) {
#pragma unroll
    for (int j = 0; j < 4; ++j) { wo_piece(3, 7, j); wo_piece(3, 8, j); }
  }
  if (wave < 4) phase2<2>(L, ws, ors, tag, rt * 5 + 3, 48, cw, rot2, lane STAMP_PASS);
  else phase2<3>(L, ws, ors, tag, rt * 5, 0, cw, rot2, lane STAMP_PASS);
  STAMP(30);
  if (wave < 5 && rt * 5 + wave < 128) {
    f32x4 s[2][2];
#pragma unroll
    for (int it = 0; it < 2; ++it) {
      const int r = it * 8 + vl;
      const unsigned a0 = L + (wave * 16 + r) * 256 + ((((lane & 7) * 2) ^ r) * 16);
#pragma unroll
      for (int w = 0; w < 4; ++w) {
        f32x4 u0 = *(LDSP f32x4*)(size_t)(a0 + w * 36864), u1 = *(LDSP f32x4*)(size_t)((a0 ^ 16) + w * 36864);
        s[it][0] = w ? s[it][0] + u0 : u0; s[it][1] = w ? s[it][1] + u1 : u1;
      }
    }
#pragma unroll
    for (int it = 0; it < 2; ++it) {
      u32x4 o = {cvtpk(s[it][0][0], s[it][0][1]), cvtpk(s[it][0][2], s[it][0][3]), cvtpk(s[it][1][0], s[it][1][1]), cvtpk(s[it][1][2], s[it][1][3])};
      *(u32x4*)((char*)out + (size_t)(rt * 80 + wave * 16 + it * 8 + vl) * XROW + ct * 128 + (lane & 7) * 16) = o;
    }
  }
  STAMP(31);
  RSTAMP(63);
  STAMP_OUT();
}

extern "C" void kernel_launch(void* const* d_in, const int* in_sizes, int n_in, void* d_out, int out_size, void* d_ws, size_t ws_size,
                              hipStream_t stream) {
  fused_kernel<<<256, 512, 0, stream>>>((const u16*)d_in[0], (const u16*)d_in[1], (const u16*)d_in[4], (const u16*)d_in[2], (const u16*)d_in[5],
                                        (const u16*)d_in[6], (char*)d_ws, (u16*)d_out);
}
```

```cpp
#include <hip/hip_runtime.h>
#include <hip/hip_bf16.h>
#include <stdint.h>
#include <type_traits>

typedef unsigned short u16;
typedef unsigned long long u64;
typedef short s16x4 __attribute__((ext_vector_type(4)));
typedef float f32x2 __attribute__((ext_vector_type(2)));
typedef float f32x4 __attribute__((ext_vector_type(4)));
typedef unsigned u32x2 __attribute__((ext_vector_type(2)));
typedef unsigned u32x4 __attribute__((ext_vector_type(4)));
typedef u32x4 frag;
typedef __bf16 bf16x2 __attribute__((ext_vector_type(2)));
typedef __bf16 bfx8 __attribute__((ext_vector_type(8)));
#define LDSP __attribute__((address_space(3)))
#define GAS __attribute__((address_space(1)))
#define DI __device__ __forceinline__

constexpr int WROW = 3456;
constexpr int XROW = 1152;
constexpr int PITCH = 432;
constexpr int KO = 32 * PITCH;
constexpr int STG_W = 64 * PITCH;
constexpr int STG = STG_W + 144 * 128;
constexpr int G_OFF = 3 * STG;
constexpr int SCR_OFF = G_OFF + 4608;
constexpr int EX_OFF = 143360;
constexpr int LDS_BYTES = 163840;
constexpr int O_BYTES = 128 * 36 * 1024;
constexpr int FLAG_OFF = 8 << 20;
constexpr int EPOCH_OFF = FLAG_OFF + (1 << 20);
#define ROT 11
#define PRE0 5
#define NBX 22
#define FS1 128
#define FS2 8
#define PSLEEP 1
#define CODEPF 22656
#define NB1 5

#define MFMA(a, b, c) __builtin_amdgcn_mfma_f32_16x16x32_bf16(__builtin_bit_cast(bfx8, (a)), __builtin_bit_cast(bfx8, (b)), (c), 0, 0, 0)
#define RLX_AGENT __ATOMIC_RELAXED, __HIP_MEMORY_SCOPE_AGENT
#define HOFF(h) (36 * (h) + ((h) & 1) * 4)

DI void glds16(unsigned voff, const void* sbase, unsigned lds_addr) {
  asm volatile("s_mov_b32 m0, %2\n\ts_nop 0\n\tglobal_load_lds_dwordx4 %0, %1" ::"v"(voff), "s"(sbase), "s"(lds_addr) : "memory");
}
DI u32x2 trd(unsigned a) { return __builtin_bit_cast(u32x2, __builtin_amdgcn_ds_read_tr16_b64_v4i16((LDSP s16x4*)(size_t)a)); }
DI frag ldW(unsigned a0, unsigned a1) {
  u32x2 lo = trd(a0), hi = trd(a1);
  return __builtin_shufflevector(lo, hi, 0, 1, 2, 3);
}
DI frag ld128(unsigned a) { return *(LDSP frag*)(size_t)a; }
DI u32x2 ld64(unsigned a) { return *(LDSP u32x2*)(size_t)a; }
DI void st64(unsigned a, u32x2 v) { *(LDSP u32x2*)(size_t)a = v; }
DI unsigned cvtpk(float a, float b) {
  f32x2 f = {a, b};
  return __builtin_bit_cast(unsigned, __builtin_convertvector(f, bf16x2));
}
DI u32x2 pack4(f32x4 a) { return u32x2{cvtpk(a[0], a[1]), cvtpk(a[2], a[3])}; }
DI frag cat(u32x2 a, u32x2 b) { return __builtin_shufflevector(a, b, 0, 1, 2, 3); }
DI void swap32(float& a, float& b) {
  auto r = __builtin_amdgcn_permlane32_swap(__float_as_uint(a), __float_as_uint(b), false, false);
  a = __uint_as_float(r[0]); b = __uint_as_float(r[1]);
}
DI void swap16(float& a, float& b) {
  auto r = __builtin_amdgcn_permlane16_swap(__float_as_uint(a), __float_as_uint(b), false, false);
  a = __uint_as_float(r[0]); b = __uint_as_float(r[1]);
}
DI float fmx(float a, float b) { return __builtin_elementwise_maximum(a, b); }
DI float fmx3(float a, float b, float c) { return fmx(fmx(a, b), c); }
#define STAMP(i)
#define RSTAMP(i)
#define STAMP_OUT()
#define STAMP_ARG
#define STAMP_PASS
#define STAMP_DECL
#define SGB(m, n) __builtin_amdgcn_sched_group_barrier(m, n, 0)
#define SCHED0() __builtin_amdgcn_sched_barrier(0)
#define WAIT_LGKM0() do { __builtin_amdgcn_s_waitcnt(0xC07F); asm volatile("" ::: "memory"); } while (0)
#define WAIT_VM0() asm volatile("s_waitcnt vmcnt(0)" ::: "memory")
#define BARRIER() __builtin_amdgcn_s_barrier()
#define PIN(x) asm volatile("" : "+v"(x))
#define TAG_SCALAR() do { unsigned lo_ = __builtin_amdgcn_readfirstlane((unsigned)tag), hi_ = __builtin_amdgcn_readfirstlane((unsigned)(tag >> 32)); \
    tag = (((u64)hi_ << 32) | lo_) + 1; asm volatile("" : "+s"(tag)); } while (0)
#define PINS(x) asm volatile("" : "+s"(x))
#define WR2(addr, a, b, o0, o1) asm volatile("ds_write2st64_b64 %0, %1, %2 offset0:%3 offset1:%4" ::"v"(addr), "v"(a), "v"(b), "i"(o0), "i"(o1) : "memory")

DI void pv_store(u32x4 pk, const frag (&V)[5], u32x4 sel, unsigned vmain, unsigned vleft, __amdgpu_buffer_rsrc_t ors, unsigned so, unsigned sl, bool g0) {
  const f32x4 z = {0.f, 0.f, 0.f, 0.f};
  frag P[4];
  f32x4 o[4], os[4];
  auto mkP = [&](int h) {
    unsigned u = pk[h];
    P[h] = frag{__builtin_amdgcn_perm(u, u, sel[0]), __builtin_amdgcn_perm(u, u, sel[1]), __builtin_amdgcn_perm(u, u, sel[2]), __builtin_amdgcn_perm(u, u, sel[3])};
    PIN(P[h]);
  };
  mkP(0); mkP(1);
  SCHED0();
  u32x4 f0, f1;
  os[0] = MFMA(V[4], P[0], z); PIN(os[0]); o[0] = MFMA(V[0], P[0], z); PIN(o[0]); mkP(2); SCHED0();
  os[1] = MFMA(V[4], P[1], z); PIN(os[1]); o[1] = MFMA(V[1], P[1], z); PIN(o[1]); mkP(3); SCHED0();
  os[2] = MFMA(V[4], P[2], z); PIN(os[2]); os[3] = MFMA(V[4], P[3], z); PIN(os[3]);
  o[2] = MFMA(V[2], P[2], z); PIN(o[2]); o[3] = MFMA(V[3], P[3], z); PIN(o[3]); SCHED0();
  f0 = u32x4{cvtpk(o[0][0], o[0][1]), cvtpk(o[0][2], o[0][3]), cvtpk(o[1][0], o[1][1]), cvtpk(o[1][2], o[1][3])};
  PIN(f0); SCHED0();
  __builtin_amdgcn_raw_buffer_store_b128(f0, ors, vmain, so, 16);
  SCHED0();
  u32x2 f2 = {cvtpk(g0 ? os[0][0] : os[2][0], g0 ? os[0][1] : os[2][1]), cvtpk(g0 ? os[1][2] : os[3][2], g0 ? os[1][3] : os[3][3])};
  PIN(f2); SCHED0();
  f1 = u32x4{cvtpk(o[2][0], o[2][1]), cvtpk(o[2][2], o[2][3]), cvtpk(o[3][0], o[3][1]), cvtpk(o[3][2], o[3][3])};
  PIN(f1); PIN(f0); SCHED0();
  __builtin_amdgcn_raw_buffer_store_b128(f1, ors, vmain + 1024, so, 16);
  __builtin_amdgcn_raw_buffer_store_b64(f2, ors, vleft, sl, 16);
  SCHED0();
}

template <int MT>
struct P2 {
  unsigned so[9], vo[MT], bw[4], rb[4];
  GAS u64* fp;
  DI void setup(unsigned L, char* ws, int tt0, int mrow, int kq, int rot2, int lane) {
    const int g = lane >> 4, q = (lane >> 2) & 3, p = lane & 3, c = lane & 15;
    const unsigned sw = (2 * g + (q >> 1)) & 3;
#pragma unroll
    for (int n = 0; n < 4; ++n) { bw[n] = L + kq * 36864 + (4 * g + q) * 128 + 8 * p + ((n ^ sw) * 32); PIN(bw[n]); }
#pragma unroll
    for (int i = 0; i < 9; ++i) { so[i] = (i < 8 ? 8 * kq + ((i + rot2) & 7) : 32 + kq) * 1024; PINS(so[i]); }
#pragma unroll
    for (int m = 0; m < MT; ++m) { int tt = tt0 + m; tt = tt > 127 ? 127 : tt; vo[m] = lane * 16 + tt * 36864; PIN(vo[m]); }
    int tf = (lane & 4) ? tt0 + MT - 1 : tt0; tf = tf > 127 ? 127 : tf;
    fp = (GAS u64*)(ws + FLAG_OFF + (kq * 64 + (tf >> 1)) * FS1 + (lane & 3) * FS2);
    PIN(fp);
#pragma unroll
    for (int n = 0; n < 4; ++n) { rb[n] = L + kq * 36864 + (mrow + c) * 256 + (((4 * n + g) ^ c) * 16); PIN(rb[n]); }
  }
  DI void run(__amdgpu_buffer_rsrc_t ors, u64 tag, int lane STAMP_ARG) {
    frag wf[9][4];
    auto rdw = [&](int i) {
#pragma unroll
      for (int n = 0; n < 4; ++n) wf[i][n] = ldW(bw[n] + i * 4096, bw[n] + i * 4096 + 2048);
    };
    rdw(0); rdw(1); rdw(2); rdw(3); rdw(4);
    SCHED0();
    if (MT == 3) WAIT_VM0();
    WAIT_LGKM0();
    BARRIER();
    STAMP(27);
#pragma clang loop unroll(disable)
    for (unsigned spins = 0; spins < (1u << 17); ++spins) {
      u64 v = tag, zz = 0; asm volatile("" : "+v"(zz)); if (lane < 8) v = __hip_atomic_fetch_add(fp, zz, RLX_AGENT);
      if (__all(v == tag)) break;
      __builtin_amdgcn_s_sleep(PSLEEP);
    }
    __builtin_amdgcn_fence(__ATOMIC_ACQUIRE, "wavefront");
    asm volatile("" ::: "memory");
    STAMP(28);
    RSTAMP(62);
    frag of[9][MT];
    constexpr int WIN = 2;
#pragma unroll
    for (int i = 0; i < WIN; ++i)
#pragma unroll
      for (int m = 0; m < MT; ++m) { of[i][m] = __builtin_amdgcn_raw_buffer_load_b128(ors, vo[m], so[i], 16); SCHED0(); }
    f32x4 acc[MT][4];
#pragma unroll
    for (int i = 0; i < 9; ++i) {
      const bool rd = i >= 2 && i + 3 < 9;
      if (rd) rdw(i + 3);
#pragma unroll
      for (int m = 0; m < MT; ++m) {
#pragma unroll
        for (int n = 0; n < 4; ++n) acc[m][n] = MFMA(wf[i][n], of[i][m], i == 0 ? (f32x4{0, 0, 0, 0}) : acc[m][n]);
        if (i + WIN < 9) of[i + WIN][m] = __builtin_amdgcn_raw_buffer_load_b128(ors, vo[m], so[i + WIN], 16);
      }
#pragma unroll
      for (int m = 0; m < MT; ++m) {
#pragma unroll
        for (int n = 0; n < 4; ++n) { SGB(0x8, 1); if (m < 2 && rd) SGB(0x100, 1); }
        if (i + WIN < 9) SGB(0x20, 1);
      }
      SCHED0();
    }
    STAMP(29);
#pragma unroll
    for (int m = 0; m < MT; ++m)
#pragma unroll
      for (int n = 0; n < 4; ++n) *(LDSP f32x4*)(size_t)(rb[n] + m * 4096) = acc[m][n];
  }
};

extern "C" __global__ __launch_bounds__(512, 2) void fused_kernel(const u16* __restrict__ x, const u16* __restrict__ wA, const u16* __restrict__ wB,
                                                        const u16* __restrict__ woA, const u16* __restrict__ woB, const u16* __restrict__ gw,
                                                        char* __restrict__ ws, u16* __restrict__ out) {
  __shared__ __attribute__((aligned(1024))) char lds[LDS_BYTES];
  const unsigned L = (unsigned)(size_t)(LDSP char*)lds;
  const int bid = blockIdx.x;
  const int xcd = bid & 7, idx = bid >> 3;
  const int tl = idx & 7, gl = idx >> 3;
  const int tile = (xcd & 1) * 8 + tl;
  const int grp = (xcd >> 1) * 4 + gl;
  int rot = ROT == 1 ? tl + 2 * gl : ROT == 2 ? (tl >> 1) * 2 + 2 * gl : ROT == 6 ? (tl % 3) * 3 + (gl % 3) * 3 : ROT == 7 ? (((tl >> 1) + gl) & 3) * 2 + (tl & 1 & (gl >> 1))
          : ROT == 8 ? (((tl + gl) & 3) * 9) >> 2 : ROT == 9 ? ((tl + gl + (xcd >> 1)) % 3) * 3 : ROT == 10 ? (((tl + gl) & 3) * 9 >> 2) + (xcd & 1)
          : ROT == 11 ? ((tl + gl) & 3) * 2 + ((xcd ^ (xcd >> 1)) & 1) : ROT == 12 ? ((tl + gl) % 3) * 3 + ((xcd & 1) + (xcd >> 1)) % 3
          : ROT == 13 ? ((tl + gl) & 1) * 4 + (((xcd & 1) * 2 + (xcd >> 1)) & 3) : ROT == 14 ? (tl + 2 * gl + ((xcd >> 1) & 1)) % 9
          : ROT == 15 ? (((tl + gl) & 3) * 2 + ((xcd ^ (xcd >> 1)) & 1) + (xcd >> 2)) : 0;
  if (rot >= 9) rot -= 9;
  const int tid = threadIdx.x, lane = tid & 63;
  const int wave = __builtin_amdgcn_readfirstlane(tid >> 6);
  const int vl = lane >> 3;
  STAMP_DECL;
  STAMP(0);
  RSTAMP(60);
  if (tid < 7) {
    u64 pc0;
    unsigned len;
    asm volatile("s_getpc_b64 %0\n\ts_mov_b32 %1, .Lfk_tail-fused_kernel+128" : "=s"(pc0), "=s"(len));
    unsigned off = (idx * 7 + tid) * 128;
    off = off < len ? off : 0;
    asm volatile("s_mov_b32 m0, %2\n\ts_nop 0\n\tglobal_load_lds_dword %0, %1" ::"v"(off), "s"(pc0 & ~255ull), "s"(L + SCR_OFF) : "memory");
  }
  asm volatile("" ::"s"(x), "s"(wA), "s"(wB), "s"(woA), "s"(woB), "s"(gw), "s"(ws), "s"(out));
  u64 tag;
  u32x2 gv = {0u, 0u};
  if (tid < 144) gv = *(const u32x2*)(gw + tid * 4);
  const int lin0 = xcd * 30 + idx;
  const bool act2 = idx < 30 && lin0 < 234;
  const int lin = act2 ? lin0 : 0;
  const int rt = (lin * 57) >> 9, ct = lin - rt * 9;
  const int rot2 = 3 * (rt - ((rt * 171) >> 9) * 3);
  const float sg = (grp >= 8) ? 1.4426950408889634f : -1.4426950408889634f;
  const unsigned wsch = ((((lane & 7) >> 1) ^ ((vl >> 1) & 3)) << 5) | ((lane & 1) << 4);
  const unsigned offn = vl * XROW + wsch, offl = ((vl >> 2) * 72 + (vl & 3)) * XROW + wsch;
  auto wo_piece = [&](int kq, int i, int j) {
    const char* b = (const char*)(kq >= 2 ? woB : woA) + ct * 128 + (kq & 1) * (288 * XROW);
    if (i == 8) b += ((j & 1) * 144 + ((j >> 1) ? 52 : 16)) * XROW;
    else b += (((i + rot2) & 7) * 36 + (j >> 1) * 20 + 8 * (j & 1)) * XROW;
    glds16(i == 8 ? offl : offn, b, L + (kq * 9 + i) * 4096 + j * 1024);
  };
  const __amdgpu_buffer_rsrc_t ors = __builtin_amdgcn_make_buffer_rsrc(ws, 0, O_BYTES, 0x00020000);
  const int g = lane >> 4, q = (lane >> 2) & 3, p = lane & 3, c = lane & 15;
  const int cw = wave & 3;

  auto retire = [&]() {
    if (bid == 255) {
      if (wave < 4) {
        const int b = wave * 64 + lane;
        GAS u64* fp = (GAS u64*)(ws + FLAG_OFF + (((b & 15) >> 2) * 64 + (b >> 4) * 4) * FS1 + (b & 3) * FS2);
#pragma clang loop unroll(disable)
        for (unsigned spins = 0; spins < (1u << 17); ++spins) {
          u64 v = __hip_atomic_load(fp, RLX_AGENT);
          if (__all(v == tag)) break;
          __builtin_amdgcn_s_sleep(8);
        }
      }
      BARRIER();
      if (tid == 0) __hip_atomic_store((GAS u64*)(ws + EPOCH_OFF), tag, RLX_AGENT);
    }
  };

  if (__builtin_expect(wave >= 4, 1)) {
    __builtin_amdgcn_s_setprio(2);
    const int lw = cw, tl256 = tid - 256;
    const unsigned offx = (tile * 128 + 8 * lw + vl) * XROW + ((lane & 7) ^ ((4 * lw + (vl >> 1)) & 7)) * 16;
    const char* w = (const char*)((grp >= 8) ? wB : wA) + (grp & 7) * 144;
    unsigned offw[7], offh;
    auto kchunk = [&](int st) { int kc = st + rot; return kc >= 9 ? kc - 9 : kc; };
    auto issueX = [&](int kc, unsigned sb, int jj) { glds16(offx, (const char*)x + kc * 128 + jj * (32 * XROW), sb + STG_W + (2 + lw + 4 * jj) * 1024); };
    auto issueW = [&](int kc, unsigned sb, int j) { glds16(offw[j], w + kc * (64 * WROW), sb + (lw + 4 * j) * 1024); };
    auto issue6 = [&](int kc, unsigned sb) {
      if (lw < 3) issueW(kc, sb, 6);
      else { glds16(offw[6], (const char*)x + kc * 128, sb + STG_W); glds16(offh, (const char*)x + kc * 128, sb + STG_W + 1024); }
    };
    const int kc0 = rot, kc1 = kchunk(1);
    const unsigned sb0 = L, sb1 = L + STG;
    auto calcw = [&](int j) {
      unsigned qq = 256 * j + tl256;
      unsigned u = (qq * 2428u) >> 16, cc = qq - u * 27;
      unsigned row = (u & 0x38) | ((u & 1) << 2) | ((u >> 1) & 3);
      offw[j] = row * WROW + cc * 16 + ((cc * 57u) >> 9) * 1008;
    };
    issueX(kc0, sb0, 0);
#pragma unroll
    for (int j = 0; j < 6; ++j) {
      SCHED0();
      calcw(j);
      SCHED0();
      issueW(kc0, sb0, j);
      if (j < 3) issueX(kc0, sb0, j + 1);
    }
    calcw(6);
    {
      int t0 = tile * 128 - 16 + vl, t1 = t0 + 8;
      t0 = t0 < 0 ? 0 : t0; t1 = t1 < 0 ? 0 : t1;
      unsigned h0 = t0 * XROW + ((lane & 7) ^ (vl >> 1)) * 16;
      offh = t1 * XROW + ((lane & 7) ^ (4 + (vl >> 1))) * 16;
      offw[6] = lw == 3 ? h0 : offw[6];
    }
    issue6(kc0, sb0);
    STAMP(1);
#pragma unroll
    for (int j = 0; j < PRE0; ++j) issueW(kc1, sb1, j);
    STAMP(2);
    f32x4 he = {0, 0, 0, 0};
    const unsigned aE = L + (8 * g + 2 * q) * PITCH + (lw == 0 ? 144 : 288) + ((p & 1) ? 104 : 32);
    const unsigned aX = L + STG_W + c * 128 + ((g ^ (c >> 1)) * 16);
    frag fe[2], fx[2];
    auto halo_rd = [&](unsigned so) {
      if (lw < 2) {
#pragma unroll
        for (int ks = 0; ks < 2; ++ks) {
          fx[ks] = ld128((aX ^ (ks * 64)) + so);
          fe[ks] = ldW(aE + so + ks * KO, aE + so + ks * KO + PITCH);
        }
      }
    };
    auto halo_mma = [&]() {
      if (lw == 0) { he = MFMA(fe[0], fx[0], he); he = MFMA(fe[1], fx[1], he); }
      if (lw == 1) { he = MFMA(fx[0], fe[0], he); he = MFMA(fx[1], fe[1], he); }
    };
    int slot = 0, kn = kc1;
    asm volatile("s_waitcnt vmcnt(%0)" ::"i"(PRE0) : "memory");
#pragma clang loop unroll(disable)
    for (int st = 0; st < 8; ++st) {
      BARRIER();
      STAMP(3 + st);
      if (st == 0) {
#pragma unroll
        for (int j = PRE0; j < 6; ++j) issueW(kc1, sb1, j);
        tag = __hip_atomic_load((GAS u64*)(ws + EPOCH_OFF), RLX_AGENT);
      }
      const unsigned b1 = L + (slot == 2 ? 0 : slot + 1) * STG, b2 = L + (slot == 0 ? 2 : slot - 1) * STG;
      issue6(kn, b1);
      issueX(kn, b1, 0);
      halo_rd(slot * STG);
      issueX(kn, b1, 1); issueX(kn, b1, 2); issueX(kn, b1, 3);
      SCHED0();
      halo_mma();
      SCHED0();
      kn = kn == 8 ? 0 : kn + 1;
      if (st < 7) {
#pragma unroll
        for (int j = 0; j < 6; ++j) issueW(kn, b2, j);
      } else {
#pragma unroll
        for (int I = 0; I < 6; ++I) wo_piece(0, I, lw);
      }
      slot = slot == 2 ? 0 : slot + 1;
      WAIT_LGKM0(); asm volatile("s_waitcnt vmcnt(6)" ::: "memory");
    }
    BARRIER();
    STAMP(11);
    halo_rd(2 * STG);
    wo_piece(0, 6, lw); wo_piece(0, 7, lw);
    SCHED0();
    halo_mma();
    SCHED0();
    if (lw < 2) {
      u32x2 v = pack4(he);
      if (lw == 0 && tile == 0) v = u32x2{0u, 0u};
      st64(L + EX_OFF + lane * 8 + (lw == 0 ? 4 : 9) * 512, v);
    }
    __builtin_amdgcn_s_setprio(0);
#pragma unroll
    for (int I = 8; I < NBX; ++I) wo_piece(I / 9, I % 9, lw);
    WAIT_LGKM0();
    STAMP(12);
    BARRIER();
    STAMP(13);
#pragma unroll
    for (int I = NBX; I < 34; ++I) wo_piece(I / 9, I % 9, lw);
    P2<3> p2;
    p2.setup(L, ws, rt * 5, 0, cw, rot2, lane);
    const char* l7 = (const char*)woB + ct * 128 + (288 + ((7 + rot2) & 7) * 36 + (lw >> 1) * 20 + 8 * (lw & 1)) * XROW;
    const char* l8 = (const char*)woB + ct * 128 + (288 + (lw & 1) * 144 + ((lw >> 1) ? 52 : 16)) * XROW;
    PINS(l7); PINS(l8);
    WAIT_VM0();
    TAG_SCALAR();
    STAMP(14);
    BARRIER();
    if (!act2) { retire(); return; }
    glds16(offn, l7, L + 34 * 4096 + lw * 1024);
    glds16(offl, l8, L + 35 * 4096 + lw * 1024);
    p2.run(ors, tag, lane STAMP_PASS);
  } else {
    const unsigned wrow = L + (8 * g + 2 * q) * PITCH;
    tag = __hip_atomic_load((GAS u64*)(ws + EPOCH_OFF), RLX_AGENT);
    f32x4 maskP, maskC, maskP0;
    {
      const float ninf = -__builtin_inff();
#pragma unroll
      for (int r = 0; r < 4; ++r) {
        bool pm = (r & 1) == (c & 1);
        maskC[r] = (pm && 4 * g + r <= c) ? 0.f : ninf;
        maskP[r] = (pm && 4 * g + r > c) ? 0.f : ninf;
      }
      maskP0 = maskP + ((tile == 0 && wave == 0) ? -998244352.f * 4.242640687119285f : 0.f);
      asm volatile("" : "+v"(maskP), "+v"(maskC), "+v"(maskP0));
    }
    u32x4 sel;
    {
      const int par = c & 1;
      const bool cur1 = 4 * g + par <= c, cur2 = 4 * g + 2 + par <= c;
      const unsigned s1 = par ? 0x01000c0cu : 0x0c0c0100u;
      const unsigned s2 = par ? 0x03020c0cu : 0x0c0c0302u;
      sel[0] = cur1 ? 0x0c0c0c0cu : s1;
      sel[1] = cur2 ? 0x0c0c0c0cu : s2;
      sel[2] = cur1 ? s1 : 0x0c0c0c0cu;
      sel[3] = cur2 ? s2 : 0x0c0c0c0cu;
    }
    unsigned vmain = lane * 16, vleft = lane < 32 ? c * 16 + g * 8 : 0x40000000u;
    unsigned osoff = (tile * 8 + 2 * wave) * 36864 + grp * 2048, oloff = (tile * 8 + 2 * wave) * 36864 + 32768 + grp * 256;
    GAS u64* myflag = (GAS u64*)(ws + FLAG_OFF + ((grp >> 2) * 64 + tile * 4 + cw) * FS1 + (grp & 3) * FS2);
    PINS(myflag);
    unsigned osoff1 = osoff + 36864, oloff1 = oloff + 36864;
    PINS(osoff); PINS(oloff); PINS(osoff1); PINS(oloff1);
    asm volatile("" : "+v"(sel), "+v"(vmain), "+v"(vleft));
    f32x4 aK[2][4], aQ[2][4], aKQ[2], aV[2][5], hk, hv;
    frag cv[5], xb[3];
    unsigned exr = L + EX_OFF + wave * 5120 + lane * 8, exh = L + EX_OFF + wave * 512 + lane * 8;
    unsigned exgw = L + SCR_OFF + wave * 128 + g * 16, exgr = L + SCR_OFF + wave * 128 + c * 4;
    PIN(exr); PIN(exh); PIN(exgw); PIN(exgr);
    auto mainloop = [&](auto WC) {
      constexpr int W = decltype(WC)::value;
      constexpr int XO = (1 + 2 * W) * 2048;
    hk = f32x4{0, 0, 0, 0}; hv = f32x4{0, 0, 0, 0};
#pragma unroll
    for (int t = 0; t < 2; ++t) {
#pragma unroll
      for (int f = 0; f < 4; ++f) { aK[t][f] = f32x4{0, 0, 0, 0}; aQ[t][f] = f32x4{0, 0, 0, 0}; }
      aKQ[t] = f32x4{0, 0, 0, 0};
#pragma unroll
      for (int f = 0; f < 5; ++f) aV[t][f] = f32x4{0, 0, 0, 0};
    }
    unsigned cW = wrow + 8 * p;
    unsigned cKQ = wrow + (p < 2 ? 144 : 0) + ((p & 1) ? 104 : 32);
    unsigned cX0 = L + STG_W + c * 128 + ((g ^ (c >> 1)) * 16), cX1 = cX0 ^ 64;
    unsigned cV[4];
#pragma unroll
    for (int i = 0; i < 4; ++i)
      cV[i] = p < 2 ? wrow + 288 + (p ? 104 : 32) + (i >> 1) * KO + (i & 1) * PITCH : L + G_OFF + rot * 512 + (8 * g + q) * 8 + (i >> 1) * 256 + (i & 1) * 32;
#pragma unroll
    for (int f = 0; f < 5; ++f) cv[f] = frag{0, 0, 0, 0};
    xb[0] = frag{0, 0, 0, 0}; xb[1] = frag{0, 0, 0, 0}; xb[2] = frag{0, 0, 0, 0};
    asm volatile("" : "+v"(aK[0][0]), "+v"(aK[0][1]), "+v"(aK[0][2]), "+v"(aK[0][3]), "+v"(aK[1][0]), "+v"(aK[1][1]), "+v"(aK[1][2]), "+v"(aK[1][3]),
                      "+v"(aQ[0][0]), "+v"(aQ[0][1]), "+v"(aQ[0][2]), "+v"(aQ[0][3]), "+v"(aQ[1][0]), "+v"(aQ[1][1]), "+v"(aQ[1][2]), "+v"(aQ[1][3]),
                      "+v"(aKQ[0]), "+v"(aKQ[1]), "+v"(hk), "+v"(hv), "+v"(xb[0]), "+v"(xb[1]), "+v"(xb[2]));
    asm volatile("" : "+v"(aV[0][0]), "+v"(aV[0][1]), "+v"(aV[0][2]), "+v"(aV[0][3]), "+v"(aV[0][4]), "+v"(aV[1][0]), "+v"(aV[1][1]), "+v"(aV[1][2]),
                      "+v"(aV[1][3]), "+v"(aV[1][4]), "+v"(cv[0]), "+v"(cv[1]), "+v"(cv[2]), "+v"(cv[3]), "+v"(cv[4]),
                      "+v"(cW), "+v"(cKQ), "+v"(cX0), "+v"(cX1), "+v"(cV[0]), "+v"(cV[1]), "+v"(cV[2]), "+v"(cV[3]));
    SCHED0();
    STAMP(1);
    if (W < 2 || (W == 2 && lane < 16)) {
      unsigned a0 = __builtin_amdgcn_perm(gv[0], gv[0], 0x01000100), a1 = __builtin_amdgcn_perm(gv[0], gv[0], 0x03020302);
      unsigned a2 = __builtin_amdgcn_perm(gv[1], gv[1], 0x01000100), a3 = __builtin_amdgcn_perm(gv[1], gv[1], 0x03020302);
      *(LDSP u32x4*)(size_t)(L + G_OFF + tid * 32) = u32x4{a0, a0, a1, a1};
      *(LDSP u32x4*)(size_t)(L + G_OFF + tid * 32 + 16) = u32x4{a2, a2, a3, a3};
    }
    STAMP(2);
    WAIT_LGKM0();
      int slot = 0, kc = rot;
#pragma clang loop unroll(disable)
      for (int st = 0; st < 9; ++st) {
        BARRIER();
        SCHED0();
        frag xa[3], W0[14], W1[9];
        xa[0] = ld128(cX0 + XO); xa[1] = ld128(cX0 + XO + 2048);
#pragma unroll
        for (int f = 0; f < 4; ++f) W0[f ^ 1] = ldW(cW + 144 + HOFF(f ^ 1), cW + 144 + HOFF(f ^ 1) + PITCH);
        xa[2] = ld128(cX0);
#pragma unroll
        for (int f = 0; f < 5; ++f) { aV[0][f] = MFMA(xb[0], cv[f], aV[0][f]); aV[1][f] = MFMA(xb[1], cv[f], aV[1][f]); }
        hv = MFMA(xb[2], cv[W], hv);
        W0[5] = ldW(cW + HOFF(0), cW + HOFF(0) + PITCH);
        W0[4] = ldW(cKQ, cKQ + PITCH);
        W0[7] = ldW(cW + HOFF(2), cW + HOFF(2) + PITCH);
        W0[6] = ldW(cW + HOFF(1), cW + HOFF(1) + PITCH);
        W0[9] = ldW(cW + 288 + HOFF(0), cW + 288 + HOFF(0) + PITCH);
        W0[8] = ldW(cW + HOFF(3), cW + HOFF(3) + PITCH);
        W0[11] = ldW(cW + 288 + HOFF(2), cW + 288 + HOFF(2) + PITCH);
        W0[10] = ldW(cW + 288 + HOFF(1), cW + 288 + HOFF(1) + PITCH);
        W0[13] = ldW(cV[0], cV[1]);
        W0[12] = ldW(cW + 288 + HOFF(3), cW + 288 + HOFF(3) + PITCH);
        xb[0] = ld128(cX1 + XO); xb[1] = ld128(cX1 + XO + 2048); xb[2] = ld128(cX1);
#pragma unroll
        for (int f = 0; f < 4; ++f) W1[f ^ 1] = ldW(cW + KO + 144 + HOFF(f ^ 1), cW + KO + 144 + HOFF(f ^ 1) + PITCH);
        W1[5] = ldW(cW + KO + HOFF(0), cW + KO + HOFF(0) + PITCH);
        W1[4] = ldW(cKQ + KO, cKQ + KO + PITCH);
        W1[8] = ldW(cW + KO + HOFF(3), cW + KO + HOFF(3) + PITCH);
        W1[7] = ldW(cW + KO + HOFF(2), cW + KO + HOFF(2) + PITCH);
        W1[6] = ldW(cW + KO + HOFF(1), cW + KO + HOFF(1) + PITCH);
#pragma unroll
        for (int f = 0; f < 4; ++f) cv[f] = ldW(cW + KO + 288 + HOFF(f), cW + KO + 288 + HOFF(f) + PITCH);
        cv[4] = ldW(cV[2], cV[3]);
#pragma unroll
        for (int f = 0; f < 4; ++f) { aK[0][f] = MFMA(W0[f], xa[0], aK[0][f]); aK[1][f] = MFMA(W0[f], xa[1], aK[1][f]); }
        hk = MFMA(W0[W], xa[2], hk);
        aKQ[0] = MFMA(W0[4], xa[0], aKQ[0]); aKQ[1] = MFMA(W0[4], xa[1], aKQ[1]);
#pragma unroll
        for (int f = 0; f < 4; ++f) { aQ[0][f] = MFMA(W0[5 + f], xa[0], aQ[0][f]); aQ[1][f] = MFMA(W0[5 + f], xa[1], aQ[1][f]); }
#pragma unroll
        for (int f = 0; f < 5; ++f) { aV[0][f] = MFMA(xa[0], W0[9 + f], aV[0][f]); aV[1][f] = MFMA(xa[1], W0[9 + f], aV[1][f]); }
        hv = MFMA(xa[2], W0[9 + W], hv);
#pragma unroll
        for (int f = 0; f < 4; ++f) { aK[0][f] = MFMA(W1[f], xb[0], aK[0][f]); aK[1][f] = MFMA(W1[f], xb[1], aK[1][f]); }
        hk = MFMA(W1[W], xb[2], hk);
        aKQ[0] = MFMA(W1[4], xb[0], aKQ[0]); aKQ[1] = MFMA(W1[4], xb[1], aKQ[1]);
#pragma unroll
        for (int f = 0; f < 4; ++f) { aQ[0][f] = MFMA(W1[5 + f], xb[0], aQ[0][f]); aQ[1][f] = MFMA(W1[5 + f], xb[1], aQ[1][f]); }
        const int ds = slot == 2 ? -2 * STG : STG, dg = kc == 8 ? -8 * 512 : 512;
        slot = slot == 2 ? 0 : slot + 1;
        kc = kc == 8 ? 0 : kc + 1;
        const int dv = p < 2 ? ds : dg;
        cW += ds; cKQ += ds; cX0 += ds; cX1 += ds;
#pragma unroll
        for (int i = 0; i < 4; ++i) cV[i] += dv;
        SGB(0x100, 2);
#pragma unroll
        for (int i = 0; i < 54; ++i) {
          SGB(0x8, 1);
          if (i < 3 || i == 16 || i == 28 || i == 40) SGB(0x100, 2); else SGB(0x100, 1);
          if (i >= 3 && i < 11) SGB(0x4, 1);
          if (i >= 51) SGB(0x2, 1);
        }
#pragma unroll
        for (int i = 0; i < 4; ++i) { SGB(0x8, 1); SGB(0x2, 2); }
        SGB(0x8, 2);
        SCHED0();
        WAIT_LGKM0();
      }
      hv = MFMA(xb[2], cv[W], hv);
    };
    if (wave == 0) mainloop(std::integral_constant<int, 0>{});
    else if (wave == 1) mainloop(std::integral_constant<int, 1>{});
    else if (wave == 2) mainloop(std::integral_constant<int, 2>{});
    else mainloop(std::integral_constant<int, 3>{});
    STAMP(12);
    const bool g0 = g == 0;
    frag K[3][4], Q[2][4], Vb[5], Va[5];
    u32x2 ks[2], qs[2];
    aV[1][4] = MFMA(xb[1], cv[4], aV[1][4]); PIN(aV[1][4]);
    aV[0][4] = MFMA(xb[0], cv[4], aV[0][4]); PIN(aV[0][4]);
#pragma unroll
    for (int t = 1; t >= 0; --t) {
      u32x2 kq = pack4(aKQ[t]);
      auto r0 = __builtin_amdgcn_permlane32_swap(kq[0], 0u, false, false);
      auto r1 = __builtin_amdgcn_permlane32_swap(kq[1], 0u, false, false);
      ks[t] = u32x2{r0[0], r1[0]}; qs[t] = u32x2{r0[1], r1[1]};
      PIN(ks[t]); PIN(qs[t]);
    }
    SCHED0();
#pragma unroll
    for (int f = 0; f < 4; ++f) { aV[1][f] = MFMA(xb[1], cv[f], aV[1][f]); PIN(aV[1][f]); K[2][f] = cat(pack4(aK[1][f]), ks[1]); PIN(K[2][f]); SCHED0(); }
    {
      u32x2 h = pack4(hk), hh = pack4(hv);
      if (tile == 0) h = u32x2{0u, 0u};
      WR2(exh, h, hh, 0, 5);
    }
    if (c >= 8) { *(LDSP f32x4*)(size_t)(exgw) = aV[0][4]; *(LDSP f32x4*)(size_t)(exgw + 64) = aV[1][4]; }
    SCHED0();
#pragma unroll
    for (int f = 0; f < 4; ++f) {
      aV[0][f] = MFMA(xb[0], cv[f], aV[0][f]); PIN(aV[0][f]);
      u32x2 v = pack4(aV[1][f]); Vb[f][2] = v[0]; Vb[f][3] = v[1]; PIN(Vb[f]); SCHED0();
    }
    { u32x2 v = pack4(aV[1][4]); Vb[4][2] = v[0]; Vb[4][3] = v[1]; PIN(Vb[4]); }
    if (wave < 3) {
#pragma unroll
      for (int f = 0; f < 4; ++f) WR2(exr, (u32x2{K[2][f][0], K[2][f][1]}), (u32x2{Vb[f][2], Vb[f][3]}), 10 + f, 15 + f);
      WR2(exr, ks[1], (u32x2{Vb[4][2], Vb[4][3]}), 14, 19);
    }
    SCHED0();
#pragma unroll
    for (int h = 0; h < 4; ++h) {
      K[1][h] = cat(pack4(aK[0][h]), ks[0]); PIN(K[1][h]);
      u32x2 qt = pack4(aQ[1][h]);
      unsigned e = ((h < 2) == g0) ? qs[1][h & 1] : 0u;
      Q[1][h] = (h & 1) ? frag{qt[0], qt[1], 0u, e} : frag{qt[0], qt[1], e, 0u}; PIN(Q[1][h]);
    }
    SCHED0();
    TAG_SCALAR();
    WAIT_VM0(); WAIT_LGKM0();
    STAMP(23);
    BARRIER();
    STAMP(24);
    SCHED0();
    u32x2 gz;
    asm volatile("ds_read2_b32 %0, %1 offset0:0 offset1:16" : "=v"(gz) : "v"(exgr) : "memory");
#pragma unroll
    for (int h = 0; h < 4; ++h) asm volatile("ds_read2st64_b64 %0, %1 offset0:%2 offset1:4" : "=v"(K[0][h]) : "v"(exr), "i"(h) : "memory");
    f32x4 sc[2][4], sp[2][4];
#pragma unroll
    for (int h = 0; h < 4; ++h) {
      sc[1][h] = MFMA(K[2][h], Q[1][h], maskC); PIN(sc[1][h]);
      sp[1][h] = MFMA(K[1][h], Q[1][h], maskP); PIN(sp[1][h]);
      u32x2 qt = pack4(aQ[0][h]);
      unsigned e = ((h < 2) == g0) ? qs[0][h & 1] : 0u;
      Q[0][h] = (h & 1) ? frag{qt[0], qt[1], 0u, e} : frag{qt[0], qt[1], e, 0u}; PIN(Q[0][h]);
      SCHED0();
    }
    asm volatile("s_waitcnt lgkmcnt(0)" : "+v"(gz), "+v"(K[0][0]), "+v"(K[0][1]), "+v"(K[0][2]), "+v"(K[0][3])::"memory");
    float v[2][4][2];
#pragma unroll
    for (int h = 0; h < 4; ++h) {
      sc[0][h] = MFMA(K[1][h], Q[0][h], maskC); PIN(sc[0][h]);
      sp[0][h] = MFMA(K[0][h], Q[0][h], maskP0); PIN(sp[0][h]);
      v[1][h][0] = fmx(fmx3(sp[1][h][0], sp[1][h][1], sc[1][h][0]), sc[1][h][1]); PIN(v[1][h][0]);
      v[1][h][1] = fmx(fmx3(sp[1][h][2], sp[1][h][3], sc[1][h][2]), sc[1][h][3]); PIN(v[1][h][1]);
      SCHED0();
    }
#pragma unroll
    for (int f = 0; f < 5; ++f) {
      u32x2 a = pack4(aV[0][f]); Vb[f][0] = a[0]; Vb[f][1] = a[1]; PIN(Vb[f]);
      Va[f] = __builtin_shufflevector(Vb[f], Vb[f], -1, -1, 0, 1); PIN(Va[f]);
    }
#pragma unroll
    for (int f = 0; f < 5; ++f) { u32x2 n = *(volatile LDSP u32x2*)(size_t)(exr + (5 + f) * 512); Va[f][0] = n[0]; Va[f][1] = n[1]; }
    u32x4 pk[2];
    auto softmax = [&](float (&w)[4][2], float gate_logit) {
      swap32(w[0][0], w[2][0]); swap32(w[1][0], w[3][0]); swap32(w[0][1], w[2][1]); swap32(w[1][1], w[3][1]);
      swap16(w[0][0], w[1][0]); swap16(w[2][0], w[3][0]); swap16(w[0][1], w[1][1]); swap16(w[2][1], w[3][1]);
      const float cs = 0.23570226039551584f * 1.4426950408889634f;
      float m = fmx(fmx3(fmx3(w[0][0], w[1][0], w[2][0]), w[3][0], w[0][1]), fmx3(w[1][1], w[2][1], w[3][1]));
      float mc = -m * cs;
      f32x2 e[4];
#pragma unroll
      for (int i = 0; i < 4; ++i) {
        f32x2 a = __builtin_elementwise_fma(f32x2{w[i][0], w[i][1]}, f32x2{cs, cs}, f32x2{mc, mc});
        e[i] = f32x2{__builtin_amdgcn_exp2f(a[0]), __builtin_amdgcn_exp2f(a[1])};
      }
      f32x2 s2 = (e[0] + e[1]) + (e[2] + e[3]);
      float s = s2[0] + s2[1];
      float r = __builtin_amdgcn_rcpf(__builtin_fmaf(s, __builtin_amdgcn_exp2f(gate_logit * sg), s));
      float pf[4];
#pragma unroll
      for (int i = 0; i < 4; ++i) { f32x2 pp = e[i] * r; pf[i] = __uint_as_float(cvtpk(pp[0], pp[1])); }
      swap32(pf[0], pf[2]); swap32(pf[1], pf[3]);
      swap16(pf[0], pf[1]); swap16(pf[2], pf[3]);
      return u32x4{__float_as_uint(pf[0]), __float_as_uint(pf[1]), __float_as_uint(pf[2]), __float_as_uint(pf[3])};
    };
    SCHED0();
#pragma unroll
    for (int h = 0; h < 4; ++h) {
      v[0][h][0] = fmx(fmx3(sp[0][h][0], sp[0][h][1], sc[0][h][0]), sc[0][h][1]);
      v[0][h][1] = fmx(fmx3(sp[0][h][2], sp[0][h][3], sc[0][h][2]), sc[0][h][3]);
    }
    pk[1] = softmax(v[1], __uint_as_float(gz[1]));
#pragma unroll
    for (int h = 0; h < 4; ++h) { PIN(v[0][h][0]); PIN(v[0][h][1]); }
    SCHED0();
    pv_store(pk[1], Vb, sel, vmain, vleft, ors, osoff1, oloff1, g0);
    pk[0] = softmax(v[0], __uint_as_float(gz[0]));
    SCHED0();
    pv_store(pk[0], Va, sel, vmain, vleft, ors, osoff, oloff, g0);
    STAMP(25);
    P2<2> p2;
    p2.setup(L, ws, rt * 5 + 3, 48, cw, rot2, lane);
    SCHED0();
    WAIT_VM0();
    if (lane == 0) __hip_atomic_store(myflag, tag, RLX_AGENT);
    STAMP(26);
    RSTAMP(61);
    BARRIER();
    if (!act2) { retire(); return; }
    p2.run(ors, tag, lane STAMP_PASS);
    asm volatile(".Lfk_tail:");
  }
  unsigned ra = L + (wave * 16 + vl) * 256 + ((((lane & 7) * 2) ^ vl) * 16);
  char* op = (char*)out + (size_t)(rt * 80 + wave * 16 + vl) * XROW + ct * 128 + (lane & 7) * 16;
  PIN(ra); PIN(op);
  WAIT_LGKM0();
  BARRIER();
  STAMP(30);
  if (wave < 5 && rt * 5 + wave < 128) {
    f32x4 s[2][2];
#pragma unroll
    for (int it = 0; it < 2; ++it) {
      const unsigned a0 = (ra ^ (it * 128)) + it * 2048;
#pragma unroll
      for (int w = 0; w < 4; ++w) {
        f32x4 u0 = *(LDSP f32x4*)(size_t)(a0 + w * 36864), u1 = *(LDSP f32x4*)(size_t)((a0 ^ 16) + w * 36864);
        s[it][0] = w ? s[it][0] + u0 : u0; s[it][1] = w ? s[it][1] + u1 : u1;
      }
    }
#pragma unroll
    for (int it = 0; it < 2; ++it) {
      u32x4 o = {cvtpk(s[it][0][0], s[it][0][1]), cvtpk(s[it][0][2], s[it][0][3]), cvtpk(s[it][1][0], s[it][1][1]), cvtpk(s[it][1][2], s[it][1][3])};
      *(GAS u32x4*)(op + it * 8 * XROW) = o;
    }
  }
  STAMP(31);
  RSTAMP(63);
  STAMP_OUT();
}

extern "C" void kernel_launch(void* const* d_in, const int* in_sizes, int n_in, void* d_out, int out_size, void* d_ws, size_t ws_size,
                              hipStream_t stream) {
  fused_kernel<<<256, 512, 0, stream>>>((const u16*)d_in[0], (const u16*)d_in[1], (const u16*)d_in[4], (const u16*)d_in[2], (const u16*)d_in[5],
                                        (const u16*)d_in[6], (char*)d_ws, (u16*)d_out);
}
```

```cpp
#define FS1 512
#define FS2 128
#include <hip/hip_runtime.h>
#include <hip/hip_bf16.h>
#include <stdint.h>
#include <type_traits>

typedef unsigned short u16;
typedef unsigned long long u64;
typedef short s16x4 __attribute__((ext_vector_type(4)));
typedef float f32x2 __attribute__((ext_vector_type(2)));
typedef float f32x4 __attribute__((ext_vector_type(4)));
typedef unsigned u32x2 __attribute__((ext_vector_type(2)));
typedef unsigned u32x4 __attribute__((ext_vector_type(4)));
typedef u32x4 frag;
typedef __bf16 bf16x2 __attribute__((ext_vector_type(2)));
typedef __bf16 bfx8 __attribute__((ext_vector_type(8)));
#define LDSP __attribute__((address_space(3)))
#define GAS __attribute__((address_space(1)))
#define DI __device__ __forceinline__

constexpr int WROW = 3456;
constexpr int XROW = 1152;
constexpr int PITCH = 432;
constexpr int KO = 32 * PITCH;
constexpr int STG_W = 64 * PITCH;
constexpr int STG = STG_W + 144 * 128;
constexpr int G_OFF = 3 * STG;
constexpr int SCR_OFF = G_OFF + 4608;
constexpr int EX_OFF = 143360;
constexpr int LDS_BYTES = 163840;
constexpr int O_BYTES = 128 * 36 * 1024;
constexpr int FLAG_OFF = 8 << 20;
constexpr int EPOCH_OFF = FLAG_OFF + (1 << 20);
#define ROT 11
#define PRE0 5
#define NBX 22
#define PSLEEP 1
#define CODEPF 22656
#define NB1 5

#define MFMA(a, b, c) __builtin_amdgcn_mfma_f32_16x16x32_bf16(__builtin_bit_cast(bfx8, (a)), __builtin_bit_cast(bfx8, (b)), (c), 0, 0, 0)
#define RLX_AGENT __ATOMIC_RELAXED, __HIP_MEMORY_SCOPE_AGENT
#define HOFF(h) (36 * (h) + ((h) & 1) * 4)

DI void glds16(unsigned voff, const void* sbase, unsigned lds_addr) {
  asm volatile("s_mov_b32 m0, %2\n\ts_nop 0\n\tglobal_load_lds_dwordx4 %0, %1" ::"v"(voff), "s"(sbase), "s"(lds_addr) : "memory");
}
DI u32x2 trd(unsigned a) { return __builtin_bit_cast(u32x2, __builtin_amdgcn_ds_read_tr16_b64_v4i16((LDSP s16x4*)(size_t)a)); }
DI frag ldW(unsigned a0, unsigned a1) {
  u32x2 lo = trd(a0), hi = trd(a1);
  return __builtin_shufflevector(lo, hi, 0, 1, 2, 3);
}
DI frag ld128(unsigned a) { return *(LDSP frag*)(size_t)a; }
DI u32x2 ld64(unsigned a) { return *(LDSP u32x2*)(size_t)a; }
DI void st64(unsigned a, u32x2 v) { *(LDSP u32x2*)(size_t)a = v; }
DI unsigned cvtpk(float a, float b) {
  f32x2 f = {a, b};
  return __builtin_bit_cast(unsigned, __builtin_convertvector(f, bf16x2));
}
DI u32x2 pack4(f32x4 a) { return u32x2{cvtpk(a[0], a[1]), cvtpk(a[2], a[3])}; }
DI frag cat(u32x2 a, u32x2 b) { return __builtin_shufflevector(a, b, 0, 1, 2, 3); }
DI void swap32(float& a, float& b) {
  auto r = __builtin_amdgcn_permlane32_swap(__float_as_uint(a), __float_as_uint(b), false, false);
  a = __uint_as_float(r[0]); b = __uint_as_float(r[1]);
}
DI void swap16(float& a, float& b) {
  auto r = __builtin_amdgcn_permlane16_swap(__float_as_uint(a), __float_as_uint(b), false, false);
  a = __uint_as_float(r[0]); b = __uint_as_float(r[1]);
}
DI float fmx(float a, float b) { return __builtin_elementwise_maximum(a, b); }
DI float fmx3(float a, float b, float c) { return fmx(fmx(a, b), c); }
#define STAMP(i)
#define RSTAMP(i)
#define STAMP_OUT()
#define STAMP_ARG
#define STAMP_PASS
#define STAMP_DECL
#define SGB(m, n) __builtin_amdgcn_sched_group_barrier(m, n, 0)
#define SCHED0() __builtin_amdgcn_sched_barrier(0)
#define WAIT_LGKM0() do { __builtin_amdgcn_s_waitcnt(0xC07F); asm volatile("" ::: "memory"); } while (0)
#define WAIT_VM0() asm volatile("s_waitcnt vmcnt(0)" ::: "memory")
#define BARRIER() __builtin_amdgcn_s_barrier()
#define PIN(x) asm volatile("" : "+v"(x))
#define TAG_SCALAR() do { unsigned lo_ = __builtin_amdgcn_readfirstlane((unsigned)tag), hi_ = __builtin_amdgcn_readfirstlane((unsigned)(tag >> 32)); \
    tag = (((u64)hi_ << 32) | lo_) + 1; asm volatile("" : "+s"(tag)); } while (0)
#define PINS(x) asm volatile("" : "+s"(x))
#define WR2(addr, a, b, o0, o1) asm volatile("ds_write2st64_b64 %0, %1, %2 offset0:%3 offset1:%4" ::"v"(addr), "v"(a), "v"(b), "i"(o0), "i"(o1) : "memory")

DI void pv_store(u32x4 pk, const frag (&V)[5], u32x4 sel, unsigned vmain, unsigned vleft, __amdgpu_buffer_rsrc_t ors, unsigned so, unsigned sl, bool g0) {
  const f32x4 z = {0.f, 0.f, 0.f, 0.f};
  frag P[4];
  f32x4 o[4], os[4];
  auto mkP = [&](int h) {
    unsigned u = pk[h];
    P[h] = frag{__builtin_amdgcn_perm(u, u, sel[0]), __builtin_amdgcn_perm(u, u, sel[1]), __builtin_amdgcn_perm(u, u, sel[2]), __builtin_amdgcn_perm(u, u, sel[3])};
    PIN(P[h]);
  };
  mkP(0); mkP(1);
  SCHED0();
  u32x4 f0, f1;
  os[0] = MFMA(V[4], P[0], z); PIN(os[0]); o[0] = MFMA(V[0], P[0], z); PIN(o[0]); mkP(2); SCHED0();
  os[1] = MFMA(V[4], P[1], z); PIN(os[1]); o[1] = MFMA(V[1], P[1], z); PIN(o[1]); mkP(3); SCHED0();
  os[2] = MFMA(V[4], P[2], z); PIN(os[2]); os[3] = MFMA(V[4], P[3], z); PIN(os[3]);
  o[2] = MFMA(V[2], P[2], z); PIN(o[2]); o[3] = MFMA(V[3], P[3], z); PIN(o[3]); SCHED0();
  f0 = u32x4{cvtpk(o[0][0], o[0][1]), cvtpk(o[0][2], o[0][3]), cvtpk(o[1][0], o[1][1]), cvtpk(o[1][2], o[1][3])};
  PIN(f0); SCHED0();
  __builtin_amdgcn_raw_buffer_store_b128(f0, ors, vmain, so, 16);
  SCHED0();
  u32x2 f2 = {cvtpk(g0 ? os[0][0] : os[2][0], g0 ? os[0][1] : os[2][1]), cvtpk(g0 ? os[1][2] : os[3][2], g0 ? os[1][3] : os[3][3])};
  PIN(f2); SCHED0();
  f1 = u32x4{cvtpk(o[2][0], o[2][1]), cvtpk(o[2][2], o[2][3]), cvtpk(o[3][0], o[3][1]), cvtpk(o[3][2], o[3][3])};
  PIN(f1); PIN(f0); SCHED0();
  __builtin_amdgcn_raw_buffer_store_b128(f1, ors, vmain + 1024, so, 16);
  __builtin_amdgcn_raw_buffer_store_b64(f2, ors, vleft, sl, 16);
  SCHED0();
}

template <int MT>
DI void phase2(unsigned L, char* ws, __amdgpu_buffer_rsrc_t ors, u64 tag, int tt0, int mrow, int kq, int rot2, int lane STAMP_ARG) {
  const int g = lane >> 4, q = (lane >> 2) & 3, p = lane & 3, c = lane & 15;
  const unsigned sw = (2 * g + (q >> 1)) & 3;
  unsigned bw[4];
#pragma unroll
  for (int n = 0; n < 4; ++n) bw[n] = L + kq * 36864 + (4 * g + q) * 128 + 8 * p + ((n ^ sw) * 32);
  frag wf[9][4];
  auto rdw = [&](int i) {
#pragma unroll
    for (int n = 0; n < 4; ++n) wf[i][n] = ldW(bw[n] + i * 4096, bw[n] + i * 4096 + 2048);
  };
  rdw(0); rdw(1); rdw(2); rdw(3); rdw(4);
  SCHED0();
  unsigned so[9], vo[MT];
#pragma unroll
  for (int i = 0; i < 9; ++i) so[i] = (i < 8 ? 8 * kq + ((i + rot2) & 7) : 32 + kq) * 1024;
#pragma unroll
  for (int m = 0; m < MT; ++m) { int tt = tt0 + m; tt = tt > 127 ? 127 : tt; vo[m] = lane * 16 + tt * 36864; }
  int tf = (lane & 4) ? tt0 + MT - 1 : tt0; tf = tf > 127 ? 127 : tf;
  GAS u64* fp = (GAS u64*)(ws + FLAG_OFF + (kq * 64 + (tf >> 1)) * FS1 + (lane & 3) * FS2);
  unsigned rb[4];
#pragma unroll
  for (int n = 0; n < 4; ++n) rb[n] = L + kq * 36864 + (mrow + c) * 256 + (((4 * n + g) ^ c) * 16);
#pragma unroll
  for (int i = 0; i < 9; ++i) PINS(so[i]);
#pragma unroll
  for (int m = 0; m < MT; ++m) PIN(vo[m]);
#pragma unroll
  for (int n = 0; n < 4; ++n) PIN(rb[n]);
  PIN(fp);
  WAIT_LGKM0();
  BARRIER();
  STAMP(27);
#pragma clang loop unroll(disable)
  for (unsigned spins = 0; spins < (1u << 17); ++spins) {
    u64 v = __hip_atomic_load(fp, RLX_AGENT);
    if (__all(v == tag)) break;
    __builtin_amdgcn_s_sleep(PSLEEP);
  }
  __builtin_amdgcn_fence(__ATOMIC_ACQUIRE, "wavefront");
  asm volatile("" ::: "memory");
  STAMP(28);
  RSTAMP(62);
  frag of[9][MT];
  constexpr int WIN = 2;
#pragma unroll
  for (int i = 0; i < WIN; ++i)
#pragma unroll
    for (int m = 0; m < MT; ++m) { of[i][m] = __builtin_amdgcn_raw_buffer_load_b128(ors, vo[m], so[i], 16); SCHED0(); }
  f32x4 acc[MT][4];
#pragma unroll
  for (int i = 0; i < 9; ++i) {
    const bool rd = i >= 2 && i + 3 < 9;
    if (rd) rdw(i + 3);
#pragma unroll
    for (int m = 0; m < MT; ++m) {
#pragma unroll
      for (int n = 0; n < 4; ++n) acc[m][n] = MFMA(wf[i][n], of[i][m], i == 0 ? (f32x4{0, 0, 0, 0}) : acc[m][n]);
      if (i + WIN < 9) of[i + WIN][m] = __builtin_amdgcn_raw_buffer_load_b128(ors, vo[m], so[i + WIN], 16);
    }
#pragma unroll
    for (int m = 0; m < MT; ++m) {
#pragma unroll
      for (int n = 0; n < 4; ++n) { SGB(0x8, 1); if (m < 2 && rd) SGB(0x100, 1); }
      if (i + WIN < 9) SGB(0x20, 1);
    }
    SCHED0();
  }
  STAMP(29);
#pragma unroll
  for (int m = 0; m < MT; ++m)
#pragma unroll
    for (int n = 0; n < 4; ++n) *(LDSP f32x4*)(size_t)(rb[n] + m * 4096) = acc[m][n];
  WAIT_LGKM0();
  BARRIER();
}

extern "C" __global__ __launch_bounds__(512, 2) void fused_kernel(const u16* __restrict__ x, const u16* __restrict__ wA, const u16* __restrict__ wB,
                                                        const u16* __restrict__ woA, const u16* __restrict__ woB, const u16* __restrict__ gw,
                                                        char* __restrict__ ws, u16* __restrict__ out) {
  __shared__ __attribute__((aligned(1024))) char lds[LDS_BYTES];
  const unsigned L = (unsigned)(size_t)(LDSP char*)lds;
  const int bid = blockIdx.x;
  const int xcd = bid & 7, idx = bid >> 3;
  const int tl = idx & 7, gl = idx >> 3;
  const int tile = (xcd & 1) * 8 + tl;
  const int grp = (xcd >> 1) * 4 + gl;
  int rot = ROT == 1 ? tl + 2 * gl : ROT == 2 ? (tl >> 1) * 2 + 2 * gl : ROT == 6 ? (tl % 3) * 3 + (gl % 3) * 3 : ROT == 7 ? (((tl >> 1) + gl) & 3) * 2 + (tl & 1 & (gl >> 1))
          : ROT == 8 ? (((tl + gl) & 3) * 9) >> 2 : ROT == 9 ? ((tl + gl + (xcd >> 1)) % 3) * 3 : ROT == 10 ? (((tl + gl) & 3) * 9 >> 2) + (xcd & 1)
          : ROT == 11 ? ((tl + gl) & 3) * 2 + ((xcd ^ (xcd >> 1)) & 1) : ROT == 12 ? ((tl + gl) % 3) * 3 + ((xcd & 1) + (xcd >> 1)) % 3
          : ROT == 13 ? ((tl + gl) & 1) * 4 + (((xcd & 1) * 2 + (xcd >> 1)) & 3) : ROT == 14 ? (tl + 2 * gl + ((xcd >> 1) & 1)) % 9
          : ROT == 15 ? (((tl + gl) & 3) * 2 + ((xcd ^ (xcd >> 1)) & 1) + (xcd >> 2)) : 0;
  if (rot >= 9) rot -= 9;
  const int tid = threadIdx.x, lane = tid & 63;
  const int wave = __builtin_amdgcn_readfirstlane(tid >> 6);
  const int vl = lane >> 3;
  STAMP_DECL;
  STAMP(0);
  RSTAMP(60);
  if (tid < 7) {
    u64 pc0;
    unsigned len;
    asm volatile("s_getpc_b64 %0\n\ts_mov_b32 %1, .Lfk_tail-fused_kernel+128" : "=s"(pc0), "=s"(len));
    unsigned off = (idx * 7 + tid) * 128;
    off = off < len ? off : 0;
    asm volatile("s_mov_b32 m0, %2\n\ts_nop 0\n\tglobal_load_lds_dword %0, %1" ::"v"(off), "s"(pc0 & ~255ull), "s"(L + SCR_OFF) : "memory");
  }
  asm volatile("" ::"s"(x), "s"(wA), "s"(wB), "s"(woA), "s"(woB), "s"(gw), "s"(ws), "s"(out));
  u64 tag;
  u32x2 gv = {0u, 0u};
  if (tid < 144) gv = *(const u32x2*)(gw + tid * 4);
  const int lin0 = xcd * 30 + idx;
  const bool act2 = idx < 30 && lin0 < 234;
  const int lin = act2 ? lin0 : 0;
  const int rt = (lin * 57) >> 9, ct = lin - rt * 9;
  const int rot2 = 3 * (rt - ((rt * 171) >> 9) * 3);
  const float sg = (grp >= 8) ? 1.4426950408889634f : -1.4426950408889634f;
  const unsigned wsch = ((((lane & 7) >> 1) ^ ((vl >> 1) & 3)) << 5) | ((lane & 1) << 4);
  const unsigned offn = vl * XROW + wsch, offl = ((vl >> 2) * 72 + (vl & 3)) * XROW + wsch;
  auto wo_piece = [&](int kq, int i, int j) {
    const char* b = (const char*)(kq >= 2 ? woB : woA) + ct * 128 + (kq & 1) * (288 * XROW);
    if (i == 8) b += ((j & 1) * 144 + ((j >> 1) ? 52 : 16)) * XROW;
    else b += (((i + rot2) & 7) * 36 + (j >> 1) * 20 + 8 * (j & 1)) * XROW;
    glds16(i == 8 ? offl : offn, b, L + (kq * 9 + i) * 4096 + j * 1024);
  };
  const __amdgpu_buffer_rsrc_t ors = __builtin_amdgcn_make_buffer_rsrc(ws, 0, O_BYTES, 0x00020000);
  const int g = lane >> 4, q = (lane >> 2) & 3, p = lane & 3, c = lane & 15;
  const int cw = wave & 3;

  if (__builtin_expect(wave >= 4, 1)) {
    __builtin_amdgcn_s_setprio(2);
    const int lw = cw, tl256 = tid - 256;
    const unsigned offx = (tile * 128 + 8 * lw + vl) * XROW + ((lane & 7) ^ ((4 * lw + (vl >> 1)) & 7)) * 16;
    const char* w = (const char*)((grp >= 8) ? wB : wA) + (grp & 7) * 144;
    unsigned offw[7], offh;
    auto kchunk = [&](int st) { int kc = st + rot; return kc >= 9 ? kc - 9 : kc; };
    auto issueX = [&](int kc, unsigned sb, int jj) { glds16(offx, (const char*)x + kc * 128 + jj * (32 * XROW), sb + STG_W + (2 + lw + 4 * jj) * 1024); };
    auto issueW = [&](int kc, unsigned sb, int j) { glds16(offw[j], w + kc * (64 * WROW), sb + (lw + 4 * j) * 1024); };
    auto issue6 = [&](int kc, unsigned sb) {
      if (lw < 3) issueW(kc, sb, 6);
      else { glds16(offw[6], (const char*)x + kc * 128, sb + STG_W); glds16(offh, (const char*)x + kc * 128, sb + STG_W + 1024); }
    };
    const int kc0 = rot, kc1 = kchunk(1);
    const unsigned sb0 = L, sb1 = L + STG;
    auto calcw = [&](int j) {
      unsigned qq = 256 * j + tl256;
      unsigned u = (qq * 2428u) >> 16, cc = qq - u * 27;
      unsigned row = (u & 0x38) | ((u & 1) << 2) | ((u >> 1) & 3);
      offw[j] = row * WROW + cc * 16 + ((cc * 57u) >> 9) * 1008;
    };
    issueX(kc0, sb0, 0);
#pragma unroll
    for (int j = 0; j < 6; ++j) {
      SCHED0();
      calcw(j);
      SCHED0();
      issueW(kc0, sb0, j);
      if (j < 3) issueX(kc0, sb0, j + 1);
    }
    calcw(6);
    {
      int t0 = tile * 128 - 16 + vl, t1 = t0 + 8;
      t0 = t0 < 0 ? 0 : t0; t1 = t1 < 0 ? 0 : t1;
      unsigned h0 = t0 * XROW + ((lane & 7) ^ (vl >> 1)) * 16;
      offh = t1 * XROW + ((lane & 7) ^ (4 + (vl >> 1))) * 16;
      offw[6] = lw == 3 ? h0 : offw[6];
    }
    issue6(kc0, sb0);
    STAMP(1);
#pragma unroll
    for (int j = 0; j < PRE0; ++j) issueW(kc1, sb1, j);
    STAMP(2);
    f32x4 he = {0, 0, 0, 0};
    const unsigned aE = L + (8 * g + 2 * q) * PITCH + (lw == 0 ? 144 : 288) + ((p & 1) ? 104 : 32);
    const unsigned aX = L + STG_W + c * 128 + ((g ^ (c >> 1)) * 16);
    frag fe[2], fx[2];
    auto halo_rd = [&](unsigned so) {
      if (lw < 2) {
#pragma unroll
        for (int ks = 0; ks < 2; ++ks) {
          fx[ks] = ld128((aX ^ (ks * 64)) + so);
          fe[ks] = ldW(aE + so + ks * KO, aE + so + ks * KO + PITCH);
        }
      }
    };
    auto halo_mma = [&]() {
      if (lw == 0) { he = MFMA(fe[0], fx[0], he); he = MFMA(fe[1], fx[1], he); }
      if (lw == 1) { he = MFMA(fx[0], fe[0], he); he = MFMA(fx[1], fe[1], he); }
    };
    int slot = 0, kn = kc1;
    asm volatile("s_waitcnt vmcnt(%0)" ::"i"(PRE0) : "memory");
#pragma clang loop unroll(disable)
    for (int st = 0; st < 8; ++st) {
      BARRIER();
      STAMP(3 + st);
      if (st == 0) {
#pragma unroll
        for (int j = PRE0; j < 6; ++j) issueW(kc1, sb1, j);
        tag = __hip_atomic_load((GAS u64*)(ws + EPOCH_OFF), RLX_AGENT);
      }
      const unsigned b1 = L + (slot == 2 ? 0 : slot + 1) * STG, b2 = L + (slot == 0 ? 2 : slot - 1) * STG;
      issue6(kn, b1);
      issueX(kn, b1, 0);
      halo_rd(slot * STG);
      issueX(kn, b1, 1); issueX(kn, b1, 2); issueX(kn, b1, 3);
      SCHED0();
      halo_mma();
      SCHED0();
      kn = kn == 8 ? 0 : kn + 1;
      if (st < 7) {
#pragma unroll
        for (int j = 0; j < 6; ++j) issueW(kn, b2, j);
      } else {
#pragma unroll
        for (int I = 0; I < 6; ++I) wo_piece(0, I, lw);
      }
      slot = slot == 2 ? 0 : slot + 1;
      WAIT_LGKM0(); asm volatile("s_waitcnt vmcnt(6)" ::: "memory");
    }
    BARRIER();
    STAMP(11);
    halo_rd(2 * STG);
    wo_piece(0, 6, lw); wo_piece(0, 7, lw);
    SCHED0();
    halo_mma();
    SCHED0();
    if (lw < 2) {
      u32x2 v = pack4(he);
      if (lw == 0 && tile == 0) v = u32x2{0u, 0u};
      st64(L + EX_OFF + lane * 8 + (lw == 0 ? 4 : 9) * 512, v);
    }
    __builtin_amdgcn_s_setprio(0);
#pragma unroll
    for (int I = 8; I < NBX; ++I) wo_piece(I / 9, I % 9, lw);
    WAIT_LGKM0();
    STAMP(12);
    BARRIER();
    STAMP(13);
#pragma unroll
    for (int I = NBX; I < 34; ++I) wo_piece(I / 9, I % 9, lw);
    WAIT_VM0();
    TAG_SCALAR();
    STAMP(14);
  } else {
    const unsigned wrow = L + (8 * g + 2 * q) * PITCH;
    tag = __hip_atomic_load((GAS u64*)(ws + EPOCH_OFF), RLX_AGENT);
    f32x4 maskP, maskC, maskP0;
    {
      const float ninf = -__builtin_inff();
#pragma unroll
      for (int r = 0; r < 4; ++r) {
        bool pm = (r & 1) == (c & 1);
        maskC[r] = (pm && 4 * g + r <= c) ? 0.f : ninf;
        maskP[r] = (pm && 4 * g + r > c) ? 0.f : ninf;
      }
      maskP0 = maskP + ((tile == 0 && wave == 0) ? -998244352.f * 4.242640687119285f : 0.f);
      asm volatile("" : "+v"(maskP), "+v"(maskC), "+v"(maskP0));
    }
    u32x4 sel;
    {
      const int par = c & 1;
      const bool cur1 = 4 * g + par <= c, cur2 = 4 * g + 2 + par <= c;
      const unsigned s1 = par ? 0x01000c0cu : 0x0c0c0100u;
      const unsigned s2 = par ? 0x03020c0cu : 0x0c0c0302u;
      sel[0] = cur1 ? 0x0c0c0c0cu : s1;
      sel[1] = cur2 ? 0x0c0c0c0cu : s2;
      sel[2] = cur1 ? s1 : 0x0c0c0c0cu;
      sel[3] = cur2 ? s2 : 0x0c0c0c0cu;
    }
    unsigned vmain = lane * 16, vleft = lane < 32 ? c * 16 + g * 8 : 0x40000000u;
    unsigned osoff = (tile * 8 + 2 * wave) * 36864 + grp * 2048, oloff = (tile * 8 + 2 * wave) * 36864 + 32768 + grp * 256;
    GAS u64* myflag = (GAS u64*)(ws + FLAG_OFF + ((grp >> 2) * 64 + tile * 4 + cw) * FS1 + (grp & 3) * FS2);
    PINS(myflag);
    unsigned osoff1 = osoff + 36864, oloff1 = oloff + 36864;
    PINS(osoff); PINS(oloff); PINS(osoff1); PINS(oloff1);
    asm volatile("" : "+v"(sel), "+v"(vmain), "+v"(vleft));
    f32x4 aK[2][4], aQ[2][4], aKQ[2], aV[2][5], hk, hv;
    frag cv[5], xb[3];
    unsigned exr = L + EX_OFF + wave * 5120 + lane * 8, exh = L + EX_OFF + wave * 512 + lane * 8;
    unsigned exgw = L + SCR_OFF + wave * 128 + g * 16, exgr = L + SCR_OFF + wave * 128 + c * 4;
    PIN(exr); PIN(exh); PIN(exgw); PIN(exgr);
    auto mainloop = [&](auto WC) {
      constexpr int W = decltype(WC)::value;
      constexpr int XO = (1 + 2 * W) * 2048;
    hk = f32x4{0, 0, 0, 0}; hv = f32x4{0, 0, 0, 0};
#pragma unroll
    for (int t = 0; t < 2; ++t) {
#pragma unroll
      for (int f = 0; f < 4; ++f) { aK[t][f] = f32x4{0, 0, 0, 0}; aQ[t][f] = f32x4{0, 0, 0, 0}; }
      aKQ[t] = f32x4{0, 0, 0, 0};
#pragma unroll
      for (int f = 0; f < 5; ++f) aV[t][f] = f32x4{0, 0, 0, 0};
    }
    unsigned cW = wrow + 8 * p;
    unsigned cKQ = wrow + (p < 2 ? 144 : 0) + ((p & 1) ? 104 : 32);
    unsigned cX0 = L + STG_W + c * 128 + ((g ^ (c >> 1)) * 16), cX1 = cX0 ^ 64;
    unsigned cV[4];
#pragma unroll
    for (int i = 0; i < 4; ++i)
      cV[i] = p < 2 ? wrow + 288 + (p ? 104 : 32) + (i >> 1) * KO + (i & 1) * PITCH : L + G_OFF + rot * 512 + (8 * g + q) * 8 + (i >> 1) * 256 + (i & 1) * 32;
#pragma unroll
    for (int f = 0; f < 5; ++f) cv[f] = frag{0, 0, 0, 0};
    xb[0] = frag{0, 0, 0, 0}; xb[1] = frag{0, 0, 0, 0}; xb[2] = frag{0, 0, 0, 0};
    asm volatile("" : "+v"(aK[0][0]), "+v"(aK[0][1]), "+v"(aK[0][2]), "+v"(aK[0][3]), "+v"(aK[1][0]), "+v"(aK[1][1]), "+v"(aK[1][2]), "+v"(aK[1][3]),
                      "+v"(aQ[0][0]), "+v"(aQ[0][1]), "+v"(aQ[0][2]), "+v"(aQ[0][3]), "+v"(aQ[1][0]), "+v"(aQ[1][1]), "+v"(aQ[1][2]), "+v"(aQ[1][3]),
                      "+v"(aKQ[0]), "+v"(aKQ[1]), "+v"(hk), "+v"(hv), "+v"(xb[0]), "+v"(xb[1]), "+v"(xb[2]));
    asm volatile("" : "+v"(aV[0][0]), "+v"(aV[0][1]), "+v"(aV[0][2]), "+v"(aV[0][3]), "+v"(aV[0][4]), "+v"(aV[1][0]), "+v"(aV[1][1]), "+v"(aV[1][2]),
                      "+v"(aV[1][3]), "+v"(aV[1][4]), "+v"(cv[0]), "+v"(cv[1]), "+v"(cv[2]), "+v"(cv[3]), "+v"(cv[4]),
                      "+v"(cW), "+v"(cKQ), "+v"(cX0), "+v"(cX1), "+v"(cV[0]), "+v"(cV[1]), "+v"(cV[2]), "+v"(cV[3]));
    SCHED0();
    STAMP(1);
    if (W < 2 || (W == 2 && lane < 16)) {
      unsigned a0 = __builtin_amdgcn_perm(gv[0], gv[0], 0x01000100), a1 = __builtin_amdgcn_perm(gv[0], gv[0], 0x03020302);
      unsigned a2 = __builtin_amdgcn_perm(gv[1], gv[1], 0x01000100), a3 = __builtin_amdgcn_perm(gv[1], gv[1], 0x03020302);
      *(LDSP u32x4*)(size_t)(L + G_OFF + tid * 32) = u32x4{a0, a0, a1, a1};
      *(LDSP u32x4*)(size_t)(L + G_OFF + tid * 32 + 16) = u32x4{a2, a2, a3, a3};
    }
    STAMP(2);
    WAIT_LGKM0();
      int slot = 0, kc = rot;
#pragma clang loop unroll(disable)
      for (int st = 0; st < 9; ++st) {
        BARRIER();
        SCHED0();
        frag xa[3], W0[14], W1[9];
        xa[0] = ld128(cX0 + XO); xa[1] = ld128(cX0 + XO + 2048);
#pragma unroll
        for (int f = 0; f < 4; ++f) W0[f ^ 1] = ldW(cW + 144 + HOFF(f ^ 1), cW + 144 + HOFF(f ^ 1) + PITCH);
        xa[2] = ld128(cX0);
#pragma unroll
        for (int f = 0; f < 5; ++f) { aV[0][f] = MFMA(xb[0], cv[f], aV[0][f]); aV[1][f] = MFMA(xb[1], cv[f], aV[1][f]); }
        hv = MFMA(xb[2], cv[W], hv);
        W0[5] = ldW(cW + HOFF(0), cW + HOFF(0) + PITCH);
        W0[4] = ldW(cKQ, cKQ + PITCH);
        W0[7] = ldW(cW + HOFF(2), cW + HOFF(2) + PITCH);
        W0[6] = ldW(cW + HOFF(1), cW + HOFF(1) + PITCH);
        W0[9] = ldW(cW + 288 + HOFF(0), cW + 288 + HOFF(0) + PITCH);
        W0[8] = ldW(cW + HOFF(3), cW + HOFF(3) + PITCH);
        W0[11] = ldW(cW + 288 + HOFF(2), cW + 288 + HOFF(2) + PITCH);
        W0[10] = ldW(cW + 288 + HOFF(1), cW + 288 + HOFF(1) + PITCH);
        W0[13] = ldW(cV[0], cV[1]);
        W0[12] = ldW(cW + 288 + HOFF(3), cW + 288 + HOFF(3) + PITCH);
        xb[0] = ld128(cX1 + XO); xb[1] = ld128(cX1 + XO + 2048); xb[2] = ld128(cX1);
#pragma unroll
        for (int f = 0; f < 4; ++f) W1[f ^ 1] = ldW(cW + KO + 144 + HOFF(f ^ 1), cW + KO + 144 + HOFF(f ^ 1) + PITCH);
        W1[5] = ldW(cW + KO + HOFF(0), cW + KO + HOFF(0) + PITCH);
        W1[4] = ldW(cKQ + KO, cKQ + KO + PITCH);
        W1[8] = ldW(cW + KO + HOFF(3), cW + KO + HOFF(3) + PITCH);
        W1[7] = ldW(cW + KO + HOFF(2), cW + KO + HOFF(2) + PITCH);
        W1[6] = ldW(cW + KO + HOFF(1), cW + KO + HOFF(1) + PITCH);
#pragma unroll
        for (int f = 0; f < 4; ++f) cv[f] = ldW(cW + KO + 288 + HOFF(f), cW + KO + 288 + HOFF(f) + PITCH);
        cv[4] = ldW(cV[2], cV[3]);
#pragma unroll
        for (int f = 0; f < 4; ++f) { aK[0][f] = MFMA(W0[f], xa[0], aK[0][f]); aK[1][f] = MFMA(W0[f], xa[1], aK[1][f]); }
        hk = MFMA(W0[W], xa[2], hk);
        aKQ[0] = MFMA(W0[4], xa[0], aKQ[0]); aKQ[1] = MFMA(W0[4], xa[1], aKQ[1]);
#pragma unroll
        for (int f = 0; f < 4; ++f) { aQ[0][f] = MFMA(W0[5 + f], xa[0], aQ[0][f]); aQ[1][f] = MFMA(W0[5 + f], xa[1], aQ[1][f]); }
#pragma unroll
        for (int f = 0; f < 5; ++f) { aV[0][f] = MFMA(xa[0], W0[9 + f], aV[0][f]); aV[1][f] = MFMA(xa[1], W0[9 + f], aV[1][f]); }
        hv = MFMA(xa[2], W0[9 + W], hv);
#pragma unroll
        for (int f = 0; f < 4; ++f) { aK[0][f] = MFMA(W1[f], xb[0], aK[0][f]); aK[1][f] = MFMA(W1[f], xb[1], aK[1][f]); }
        hk = MFMA(W1[W], xb[2], hk);
        aKQ[0] = MFMA(W1[4], xb[0], aKQ[0]); aKQ[1] = MFMA(W1[4], xb[1], aKQ[1]);
#pragma unroll
        for (int f = 0; f < 4; ++f) { aQ[0][f] = MFMA(W1[5 + f], xb[0], aQ[0][f]); aQ[1][f] = MFMA(W1[5 + f], xb[1], aQ[1][f]); }
        const int ds = slot == 2 ? -2 * STG : STG, dg = kc == 8 ? -8 * 512 : 512;
        slot = slot == 2 ? 0 : slot + 1;
        kc = kc == 8 ? 0 : kc + 1;
        const int dv = p < 2 ? ds : dg;
        cW += ds; cKQ += ds; cX0 += ds; cX1 += ds;
#pragma unroll
        for (int i = 0; i < 4; ++i) cV[i] += dv;
        SGB(0x100, 2);
#pragma unroll
        for (int i = 0; i < 54; ++i) {
          SGB(0x8, 1);
          if (i < 3 || i == 16 || i == 28 || i == 40) SGB(0x100, 2); else SGB(0x100, 1);
          if (i >= 3 && i < 11) SGB(0x4, 1);
          if (i >= 51) SGB(0x2, 1);
        }
#pragma unroll
        for (int i = 0; i < 4; ++i) { SGB(0x8, 1); SGB(0x2, 2); }
        SGB(0x8, 2);
        SCHED0();
        WAIT_LGKM0();
      }
      hv = MFMA(xb[2], cv[W], hv);
    };
    if (wave == 0) mainloop(std::integral_constant<int, 0>{});
    else if (wave == 1) mainloop(std::integral_constant<int, 1>{});
    else if (wave == 2) mainloop(std::integral_constant<int, 2>{});
    else mainloop(std::integral_constant<int, 3>{});
    STAMP(12);
    const bool g0 = g == 0;
    frag K[3][4], Q[2][4], Vb[5], Va[5];
    u32x2 ks[2], qs[2];
    aV[1][4] = MFMA(xb[1], cv[4], aV[1][4]); PIN(aV[1][4]);
    aV[0][4] = MFMA(xb[0], cv[4], aV[0][4]); PIN(aV[0][4]);
#pragma unroll
    for (int t = 1; t >= 0; --t) {
      u32x2 kq = pack4(aKQ[t]);
      auto r0 = __builtin_amdgcn_permlane32_swap(kq[0], 0u, false, false);
      auto r1 = __builtin_amdgcn_permlane32_swap(kq[1], 0u, false, false);
      ks[t] = u32x2{r0[0], r1[0]}; qs[t] = u32x2{r0[1], r1[1]};
      PIN(ks[t]); PIN(qs[t]);
    }
    SCHED0();
#pragma unroll
    for (int f = 0; f < 4; ++f) { aV[1][f] = MFMA(xb[1], cv[f], aV[1][f]); PIN(aV[1][f]); K[2][f] = cat(pack4(aK[1][f]), ks[1]); PIN(K[2][f]); SCHED0(); }
    {
      u32x2 h = pack4(hk), hh = pack4(hv);
      if (tile == 0) h = u32x2{0u, 0u};
      WR2(exh, h, hh, 0, 5);
    }
    if (c >= 8) { *(LDSP f32x4*)(size_t)(exgw) = aV[0][4]; *(LDSP f32x4*)(size_t)(exgw + 64) = aV[1][4]; }
    SCHED0();
#pragma unroll
    for (int f = 0; f < 4; ++f) {
      aV[0][f] = MFMA(xb[0], cv[f], aV[0][f]); PIN(aV[0][f]);
      u32x2 v = pack4(aV[1][f]); Vb[f][2] = v[0]; Vb[f][3] = v[1]; PIN(Vb[f]); SCHED0();
    }
    { u32x2 v = pack4(aV[1][4]); Vb[4][2] = v[0]; Vb[4][3] = v[1]; PIN(Vb[4]); }
    if (wave < 3) {
#pragma unroll
      for (int f = 0; f < 4; ++f) WR2(exr, (u32x2{K[2][f][0], K[2][f][1]}), (u32x2{Vb[f][2], Vb[f][3]}), 10 + f, 15 + f);
      WR2(exr, ks[1], (u32x2{Vb[4][2], Vb[4][3]}), 14, 19);
    }
    SCHED0();
#pragma unroll
    for (int h = 0; h < 4; ++h) {
      K[1][h] = cat(pack4(aK[0][h]), ks[0]); PIN(K[1][h]);
      u32x2 qt = pack4(aQ[1][h]);
      unsigned e = ((h < 2) == g0) ? qs[1][h & 1] : 0u;
      Q[1][h] = (h & 1) ? frag{qt[0], qt[1], 0u, e} : frag{qt[0], qt[1], e, 0u}; PIN(Q[1][h]);
    }
    SCHED0();
    TAG_SCALAR();
    WAIT_VM0(); WAIT_LGKM0();
    STAMP(23);
    BARRIER();
    STAMP(24);
    SCHED0();
    u32x2 gz;
    asm volatile("ds_read2_b32 %0, %1 offset0:0 offset1:16" : "=v"(gz) : "v"(exgr) : "memory");
#pragma unroll
    for (int h = 0; h < 4; ++h) asm volatile("ds_read2st64_b64 %0, %1 offset0:%2 offset1:4" : "=v"(K[0][h]) : "v"(exr), "i"(h) : "memory");
    f32x4 sc[2][4], sp[2][4];
#pragma unroll
    for (int h = 0; h < 4; ++h) {
      sc[1][h] = MFMA(K[2][h], Q[1][h], maskC); PIN(sc[1][h]);
      sp[1][h] = MFMA(K[1][h], Q[1][h], maskP); PIN(sp[1][h]);
      u32x2 qt = pack4(aQ[0][h]);
      unsigned e = ((h < 2) == g0) ? qs[0][h & 1] : 0u;
      Q[0][h] = (h & 1) ? frag{qt[0], qt[1], 0u, e} : frag{qt[0], qt[1], e, 0u}; PIN(Q[0][h]);
      SCHED0();
    }
    asm volatile("s_waitcnt lgkmcnt(0)" : "+v"(gz), "+v"(K[0][0]), "+v"(K[0][1]), "+v"(K[0][2]), "+v"(K[0][3])::"memory");
    float v[2][4][2];
#pragma unroll
    for (int h = 0; h < 4; ++h) {
      sc[0][h] = MFMA(K[1][h], Q[0][h], maskC); PIN(sc[0][h]);
      sp[0][h] = MFMA(K[0][h], Q[0][h], maskP0); PIN(sp[0][h]);
      v[1][h][0] = fmx(fmx3(sp[1][h][0], sp[1][h][1], sc[1][h][0]), sc[1][h][1]); PIN(v[1][h][0]);
      v[1][h][1] = fmx(fmx3(sp[1][h][2], sp[1][h][3], sc[1][h][2]), sc[1][h][3]); PIN(v[1][h][1]);
      SCHED0();
    }
#pragma unroll
    for (int f = 0; f < 5; ++f) {
      u32x2 a = pack4(aV[0][f]); Vb[f][0] = a[0]; Vb[f][1] = a[1]; PIN(Vb[f]);
      Va[f] = __builtin_shufflevector(Vb[f], Vb[f], -1, -1, 0, 1); PIN(Va[f]);
    }
#pragma unroll
    for (int f = 0; f < 5; ++f) { u32x2 n = *(volatile LDSP u32x2*)(size_t)(exr + (5 + f) * 512); Va[f][0] = n[0]; Va[f][1] = n[1]; }
    u32x4 pk[2];
    auto softmax = [&](float (&w)[4][2], float gate_logit) {
      swap32(w[0][0], w[2][0]); swap32(w[1][0], w[3][0]); swap32(w[0][1], w[2][1]); swap32(w[1][1], w[3][1]);
      swap16(w[0][0], w[1][0]); swap16(w[2][0], w[3][0]); swap16(w[0][1], w[1][1]); swap16(w[2][1], w[3][1]);
      const float cs = 0.23570226039551584f * 1.4426950408889634f;
      float m = fmx(fmx3(fmx3(w[0][0], w[1][0], w[2][0]), w[3][0], w[0][1]), fmx3(w[1][1], w[2][1], w[3][1]));
      float mc = -m * cs;
      f32x2 e[4];
#pragma unroll
      for (int i = 0; i < 4; ++i) {
        f32x2 a = __builtin_elementwise_fma(f32x2{w[i][0], w[i][1]}, f32x2{cs, cs}, f32x2{mc, mc});
        e[i] = f32x2{__builtin_amdgcn_exp2f(a[0]), __builtin_amdgcn_exp2f(a[1])};
      }
      f32x2 s2 = (e[0] + e[1]) + (e[2] + e[3]);
      float s = s2[0] + s2[1];
      float r = __builtin_amdgcn_rcpf(__builtin_fmaf(s, __builtin_amdgcn_exp2f(gate_logit * sg), s));
      float pf[4];
#pragma unroll
      for (int i = 0; i < 4; ++i) { f32x2 pp = e[i] * r; pf[i] = __uint_as_float(cvtpk(pp[0], pp[1])); }
      swap32(pf[0], pf[2]); swap32(pf[1], pf[3]);
      swap16(pf[0], pf[1]); swap16(pf[2], pf[3]);
      return u32x4{__float_as_uint(pf[0]), __float_as_uint(pf[1]), __float_as_uint(pf[2]), __float_as_uint(pf[3])};
    };
    SCHED0();
#pragma unroll
    for (int h = 0; h < 4; ++h) {
      v[0][h][0] = fmx(fmx3(sp[0][h][0], sp[0][h][1], sc[0][h][0]), sc[0][h][1]);
      v[0][h][1] = fmx(fmx3(sp[0][h][2], sp[0][h][3], sc[0][h][2]), sc[0][h][3]);
    }
    pk[1] = softmax(v[1], __uint_as_float(gz[1]));
#pragma unroll
    for (int h = 0; h < 4; ++h) { PIN(v[0][h][0]); PIN(v[0][h][1]); }
    SCHED0();
    pv_store(pk[1], Vb, sel, vmain, vleft, ors, osoff1, oloff1, g0);
    pk[0] = softmax(v[0], __uint_as_float(gz[0]));
    SCHED0();
    pv_store(pk[0], Va, sel, vmain, vleft, ors, osoff, oloff, g0);
    STAMP(25);
    WAIT_VM0();
    if (lane < 16) __hip_atomic_store(myflag + lane, tag, RLX_AGENT);
    STAMP(26);
    RSTAMP(61);
    asm volatile(".Lfk_tail:");
  }
  BARRIER();
  if (!act2) {
    if (bid == 255) {
      if (wave < 4) {
        const int b = wave * 64 + lane;
        GAS u64* fp = (GAS u64*)(ws + FLAG_OFF + (((b & 15) >> 2) * 64 + (b >> 4) * 4) * FS1 + (b & 3) * FS2);
#pragma clang loop unroll(disable)
        for (unsigned spins = 0; spins < (1u << 17); ++spins) {
          u64 v = __hip_atomic_load(fp, RLX_AGENT);
          if (__all(v == tag)) break;
          __builtin_amdgcn_s_sleep(8);
        }
      }
      BARRIER();
      if (tid == 0) __hip_atomic_store((GAS u64*)(ws + EPOCH_OFF), tag, RLX_AGENT);
    }
    return;
  }
  if (cw == 3) {
#pragma unroll
    for (int j = 0; j < 4; ++j) { wo_piece(3, 7, j); wo_piece(3, 8, j); }
  }
  if (wave < 4) phase2<2>(L, ws, ors, tag, rt * 5 + 3, 48, cw, rot2, lane STAMP_PASS);
  else phase2<3>(L, ws, ors, tag, rt * 5, 0, cw, rot2, lane STAMP_PASS);
  STAMP(30);
  if (wave < 5 && rt * 5 + wave < 128) {
    f32x4 s[2][2];
#pragma unroll
    for (int it = 0; it < 2; ++it) {
      const int r = it * 8 + vl;
      const unsigned a0 = L + (wave * 16 + r) * 256 + ((((lane & 7) * 2) ^ r) * 16);
#pragma unroll
      for (int w = 0; w < 4; ++w) {
        f32x4 u0 = *(LDSP f32x4*)(size_t)(a0 + w * 36864), u1 = *(LDSP f32x4*)(size_t)((a0 ^ 16) + w * 36864);
        s[it][0] = w ? s[it][0] + u0 : u0; s[it][1] = w ? s[it][1] + u1 : u1;
      }
    }
#pragma unroll
    for (int it = 0; it < 2; ++it) {
      u32x4 o = {cvtpk(s[it][0][0], s[it][0][1]), cvtpk(s[it][0][2], s[it][0][3]), cvtpk(s[it][1][0], s[it][1][1]), cvtpk(s[it][1][2], s[it][1][3])};
      *(u32x4*)((char*)out + (size_t)(rt * 80 + wave * 16 + it * 8 + vl) * XROW + ct * 128 + (lane & 7) * 16) = o;
    }
  }
  STAMP(31);
  RSTAMP(63);
  STAMP_OUT();
}

extern "C" void kernel_launch(void* const* d_in, const int* in_sizes, int n_in, void* d_out, int out_size, void* d_ws, size_t ws_size,
                              hipStream_t stream) {
  fused_kernel<<<256, 512, 0, stream>>>((const u16*)d_in[0], (const u16*)d_in[1], (const u16*)d_in[4], (const u16*)d_in[2], (const u16*)d_in[5],
                                        (const u16*)d_in[6], (char*)d_ws, (u16*)d_out);
}
```

```cpp
#include <hip/hip_runtime.h>
#include <hip/hip_bf16.h>
#include <stdint.h>
#include <type_traits>

typedef unsigned short u16;
typedef unsigned long long u64;
typedef short s16x4 __attribute__((ext_vector_type(4)));
typedef float f32x2 __attribute__((ext_vector_type(2)));
typedef float f32x4 __attribute__((ext_vector_type(4)));
typedef unsigned u32x2 __attribute__((ext_vector_type(2)));
typedef unsigned u32x4 __attribute__((ext_vector_type(4)));
typedef u32x4 frag;
typedef __bf16 bf16x2 __attribute__((ext_vector_type(2)));
typedef __bf16 bfx8 __attribute__((ext_vector_type(8)));
#define LDSP __attribute__((address_space(3)))
#define GAS __attribute__((address_space(1)))
#define DI __device__ __forceinline__

constexpr int WROW = 3456;
constexpr int XROW = 1152;
constexpr int PITCH = 432;
constexpr int KO = 32 * PITCH;
constexpr int STG_W = 64 * PITCH;
constexpr int STG = STG_W + 144 * 128;
constexpr int G_OFF = 3 * STG;
constexpr int SCR_OFF = G_OFF + 4608;
constexpr int EX_OFF = 143360;
constexpr int LDS_BYTES = 163840;
constexpr int O_BYTES = 128 * 36 * 1024;
constexpr int FLAG_OFF = 8 << 20;
constexpr int EPOCH_OFF = FLAG_OFF + (1 << 20);

#define MFMA(a, b, c) __builtin_amdgcn_mfma_f32_16x16x32_bf16(__builtin_bit_cast(bfx8, (a)), __builtin_bit_cast(bfx8, (b)), (c), 0, 0, 0)
#define RLX_AGENT __ATOMIC_RELAXED, __HIP_MEMORY_SCOPE_AGENT
#define HOFF(h) (36 * (h) + ((h) & 1) * 4)

DI void glds16(unsigned voff, const void* sbase, unsigned lds_addr) {
  asm volatile("s_mov_b32 m0, %2\n\ts_nop 0\n\tglobal_load_lds_dwordx4 %0, %1" ::"v"(voff), "s"(sbase), "s"(lds_addr) : "memory");
}
DI u32x2 trd(unsigned a) { return __builtin_bit_cast(u32x2, __builtin_amdgcn_ds_read_tr16_b64_v4i16((LDSP s16x4*)(size_t)a)); }
DI frag ldW(unsigned a0, unsigned a1) {
  u32x2 lo = trd(a0), hi = trd(a1);
  return __builtin_shufflevector(lo, hi, 0, 1, 2, 3);
}
DI frag ld128(unsigned a) { return *(LDSP frag*)(size_t)a; }
DI void st64(unsigned a, u32x2 v) { *(LDSP u32x2*)(size_t)a = v; }
DI unsigned cvtpk(float a, float b) {
  f32x2 f = {a, b};
  return __builtin_bit_cast(unsigned, __builtin_convertvector(f, bf16x2));
}
DI u32x2 pack4(f32x4 a) { return u32x2{cvtpk(a[0], a[1]), cvtpk(a[2], a[3])}; }
DI frag cat(u32x2 a, u32x2 b) { return __builtin_shufflevector(a, b, 0, 1, 2, 3); }
DI void swap32(float& a, float& b) {
  auto r = __builtin_amdgcn_permlane32_swap(__float_as_uint(a), __float_as_uint(b), false, false);
  a = __uint_as_float(r[0]); b = __uint_as_float(r[1]);
}
DI void swap16(float& a, float& b) {
  auto r = __builtin_amdgcn_permlane16_swap(__float_as_uint(a), __float_as_uint(b), false, false);
  a = __uint_as_float(r[0]); b = __uint_as_float(r[1]);
}
DI float fmx(float a, float b) { return __builtin_elementwise_maximum(a, b); }
DI float fmx3(float a, float b, float c) { return fmx(fmx(a, b), c); }
#define SGB(m, n) __builtin_amdgcn_sched_group_barrier(m, n, 0)
#define SCHED0() __builtin_amdgcn_sched_barrier(0)
#define WAIT_LGKM0() do { __builtin_amdgcn_s_waitcnt(0xC07F); asm volatile("" ::: "memory"); } while (0)
#define WAIT_VM0() asm volatile("s_waitcnt vmcnt(0)" ::: "memory")
#define BARRIER() do { asm volatile("" ::: "memory"); __builtin_amdgcn_s_barrier(); asm volatile("" ::: "memory"); } while (0)
#define PIN(x) asm volatile("" : "+v"(x))
#define TAG_SCALAR() do { unsigned lo_ = __builtin_amdgcn_readfirstlane((unsigned)tag), hi_ = __builtin_amdgcn_readfirstlane((unsigned)(tag >> 32)); \
    tag = (((u64)hi_ << 32) | lo_) + 1; asm volatile("" : "+s"(tag)); } while (0)
#define PINS(x) asm volatile("" : "+s"(x))
#define WR2(addr, a, b, o0, o1) asm volatile("ds_write2st64_b64 %0, %1, %2 offset0:%3 offset1:%4" ::"v"(addr), "v"(a), "v"(b), "i"(o0), "i"(o1) : "memory")

DI void pv_store(u32x4 pk, const frag (&V)[5], u32x4 sel, unsigned vmain, unsigned vleft, __amdgpu_buffer_rsrc_t ors, unsigned so, unsigned sl, bool g0) {
  const f32x4 z = {0.f, 0.f, 0.f, 0.f};
  frag P[4];
  f32x4 o[4], os[4];
  auto mkP = [&](int h) {
    unsigned u = pk[h];
    P[h] = frag{__builtin_amdgcn_perm(u, u, sel[0]), __builtin_amdgcn_perm(u, u, sel[1]), __builtin_amdgcn_perm(u, u, sel[2]), __builtin_amdgcn_perm(u, u, sel[3])};
    PIN(P[h]);
  };
  mkP(0); mkP(1);
  SCHED0();
  u32x4 f0, f1;
  os[0] = MFMA(V[4], P[0], z); PIN(os[0]); o[0] = MFMA(V[0], P[0], z); PIN(o[0]); mkP(2); SCHED0();
  os[1] = MFMA(V[4], P[1], z); PIN(os[1]); o[1] = MFMA(V[1], P[1], z); PIN(o[1]); mkP(3); SCHED0();
  os[2] = MFMA(V[4], P[2], z); PIN(os[2]); os[3] = MFMA(V[4], P[3], z); PIN(os[3]);
  o[2] = MFMA(V[2], P[2], z); PIN(o[2]); o[3] = MFMA(V[3], P[3], z); PIN(o[3]); SCHED0();
  f0 = u32x4{cvtpk(o[0][0], o[0][1]), cvtpk(o[0][2], o[0][3]), cvtpk(o[1][0], o[1][1]), cvtpk(o[1][2], o[1][3])};
  PIN(f0); SCHED0();
  __builtin_amdgcn_raw_buffer_store_b128(f0, ors, vmain, so, 16);
  SCHED0();
  u32x2 f2 = {cvtpk(g0 ? os[0][0] : os[2][0], g0 ? os[0][1] : os[2][1]), cvtpk(g0 ? os[1][2] : os[3][2], g0 ? os[1][3] : os[3][3])};
  PIN(f2); SCHED0();
  f1 = u32x4{cvtpk(o[2][0], o[2][1]), cvtpk(o[2][2], o[2][3]), cvtpk(o[3][0], o[3][1]), cvtpk(o[3][2], o[3][3])};
  PIN(f1); PIN(f0); SCHED0();
  __builtin_amdgcn_raw_buffer_store_b128(f1, ors, vmain + 1024, so, 16);
  __builtin_amdgcn_raw_buffer_store_b64(f2, ors, vleft, sl, 16);
  asm volatile("s_nop 0" ::"v"(f1) : "memory");
  SCHED0();
}

template <int MT>
DI void phase2(unsigned L, char* ws, __amdgpu_buffer_rsrc_t ors, u64 tag, int tt0, int mrow, int kq, int rot2, int lane) {
  const int g = lane >> 4, q = (lane >> 2) & 3, p = lane & 3, c = lane & 15;
  const unsigned sw = (2 * g + (q >> 1)) & 3;
  unsigned bw[4];
#pragma unroll
  for (int n = 0; n < 4; ++n) bw[n] = L + kq * 36864 + (4 * g + q) * 128 + 8 * p + ((n ^ sw) * 32);
  frag wf[9][4];
  auto rdw = [&](int i) {
#pragma unroll
    for (int n = 0; n < 4; ++n) wf[i][n] = ldW(bw[n] + i * 4096, bw[n] + i * 4096 + 2048);
  };
  rdw(0); rdw(1); rdw(2); rdw(3); rdw(4);
  SCHED0();
  unsigned so[9], vo[MT];
#pragma unroll
  for (int i = 0; i < 9; ++i) so[i] = (i < 8 ? 8 * kq + ((i + rot2) & 7) : 32 + kq) * 1024;
#pragma unroll
  for (int m = 0; m < MT; ++m) { int tt = tt0 + m; tt = tt > 127 ? 127 : tt; vo[m] = lane * 16 + tt * 36864; }
  int tf = (lane & 4) ? tt0 + MT - 1 : tt0; tf = tf > 127 ? 127 : tf;
  GAS u64* fp = (GAS u64*)(ws + FLAG_OFF + (kq * 64 + (tf >> 1)) * 128 + (lane & 3) * 8);
  unsigned rb[4];
#pragma unroll
  for (int n = 0; n < 4; ++n) rb[n] = L + kq * 36864 + (mrow + c) * 256 + (((4 * n + g) ^ c) * 16);
#pragma unroll
  for (int i = 0; i < 9; ++i) PINS(so[i]);
#pragma unroll
  for (int m = 0; m < MT; ++m) PIN(vo[m]);
#pragma unroll
  for (int n = 0; n < 4; ++n) PIN(rb[n]);
  PIN(fp);
  WAIT_LGKM0();
  BARRIER();
#pragma clang loop unroll(disable)
  for (unsigned spins = 0; spins < (1u << 17); ++spins) {
    u64 v = __hip_atomic_load(fp, RLX_AGENT);
    if (__all(v == tag)) break;
    __builtin_amdgcn_s_sleep(1);
  }
  __builtin_amdgcn_fence(__ATOMIC_ACQUIRE, "wavefront");
  asm volatile("" ::: "memory");
  frag of[9][MT];
  constexpr int WIN = 2;
#pragma unroll
  for (int i = 0; i < WIN; ++i)
#pragma unroll
    for (int m = 0; m < MT; ++m) { of[i][m] = __builtin_amdgcn_raw_buffer_load_b128(ors, vo[m], so[i], 16); SCHED0(); }
  f32x4 acc[MT][4];
#pragma unroll
  for (int i = 0; i < 9; ++i) {
    const bool rd = i >= 2 && i + 3 < 9;
    if (rd) rdw(i + 3);
#pragma unroll
    for (int m = 0; m < MT; ++m) {
#pragma unroll
      for (int n = 0; n < 4; ++n) acc[m][n] = MFMA(wf[i][n], of[i][m], i == 0 ? (f32x4{0, 0, 0, 0}) : acc[m][n]);
      if (i + WIN < 9) of[i + WIN][m] = __builtin_amdgcn_raw_buffer_load_b128(ors, vo[m], so[i + WIN], 16);
    }
#pragma unroll
    for (int m = 0; m < MT; ++m) {
#pragma unroll
      for (int n = 0; n < 4; ++n) { SGB(0x8, 1); if (m < 2 && rd) SGB(0x100, 1); }
      if (i + WIN < 9) SGB(0x20, 1);
    }
    SCHED0();
  }
#pragma unroll
  for (int m = 0; m < MT; ++m)
#pragma unroll
    for (int n = 0; n < 4; ++n) *(LDSP f32x4*)(size_t)(rb[n] + m * 4096) = acc[m][n];
  WAIT_LGKM0();
  BARRIER();
}

extern "C" __global__ __launch_bounds__(512, 2) void fused_kernel(const u16* __restrict__ x, const u16* __restrict__ wA, const u16* __restrict__ wB,
                                                        const u16* __restrict__ woA, const u16* __restrict__ woB, const u16* __restrict__ gw,
                                                        char* __restrict__ ws, u16* __restrict__ out) {
  __shared__ __attribute__((aligned(1024))) char lds[LDS_BYTES];
  const unsigned L = (unsigned)(size_t)(LDSP char*)lds;
  const int bid = blockIdx.x;
  const int xcd = bid & 7, idx = bid >> 3;
  const int tl = idx & 7, gl = idx >> 3;
  const int tile = (xcd & 1) * 8 + tl;
  const int grp = (xcd >> 1) * 4 + gl;
  const int rot = ((tl + gl) & 3) * 2 + ((xcd ^ (xcd >> 1)) & 1);
  const int tid = threadIdx.x, lane = tid & 63;
  const int wave = __builtin_amdgcn_readfirstlane(tid >> 6);
  const int vl = lane >> 3;
  if (tid < 7) {
    u64 pc0;
    unsigned len;
    asm volatile("s_getpc_b64 %0\n\ts_mov_b32 %1, .Lfk_tail-fused_kernel+128" : "=s"(pc0), "=s"(len));
    unsigned off = (idx * 7 + tid) * 128;
    off = off < len ? off : 0;
    asm volatile("s_mov_b32 m0, %2\n\ts_nop 0\n\tglobal_load_lds_dword %0, %1" ::"v"(off), "s"(pc0 & ~255ull), "s"(L + SCR_OFF) : "memory");
  }
  asm volatile("" ::"s"(x), "s"(wA), "s"(wB), "s"(woA), "s"(woB), "s"(gw), "s"(ws), "s"(out));
  u64 tag;
  u32x2 gv = {0u, 0u};
  if (tid < 144) gv = *(const u32x2*)(gw + tid * 4);
  const int lin0 = xcd * 30 + idx;
  const bool act2 = idx < 30 && lin0 < 234;
  const int lin = act2 ? lin0 : 0;
  const int rt = (lin * 57) >> 9, ct = lin - rt * 9;
  const int rot2 = 3 * (rt - ((rt * 171) >> 9) * 3);
  const float sg = (grp >= 8) ? 1.4426950408889634f : -1.4426950408889634f;
  const unsigned wsch = ((((lane & 7) >> 1) ^ ((vl >> 1) & 3)) << 5) | ((lane & 1) << 4);
  const unsigned offn = vl * XROW + wsch, offl = ((vl >> 2) * 72 + (vl & 3)) * XROW + wsch;
  auto wo_piece = [&](int kq, int i, int j) {
    const char* b = (const char*)(kq >= 2 ? woB : woA) + ct * 128 + (kq & 1) * (288 * XROW);
    if (i == 8) b += ((j & 1) * 144 + ((j >> 1) ? 52 : 16)) * XROW;
    else b += (((i + rot2) & 7) * 36 + (j >> 1) * 20 + 8 * (j & 1)) * XROW;
    glds16(i == 8 ? offl : offn, b, L + (kq * 9 + i) * 4096 + j * 1024);
  };
  const __amdgpu_buffer_rsrc_t ors = __builtin_amdgcn_make_buffer_rsrc(ws, 0, O_BYTES, 0x00020000);
  const int g = lane >> 4, q = (lane >> 2) & 3, p = lane & 3, c = lane & 15;
  const int cw = wave & 3;

  if (__builtin_expect(wave >= 4, 1)) {
    __builtin_amdgcn_s_setprio(2);
    const int lw = cw, tl256 = tid - 256;
    const unsigned offx = (tile * 128 + 8 * lw + vl) * XROW + ((lane & 7) ^ ((4 * lw + (vl >> 1)) & 7)) * 16;
    const char* w = (const char*)((grp >= 8) ? wB : wA) + (grp & 7) * 144;
    unsigned offw[7], offh;
    auto kchunk = [&](int st) { int kc = st + rot; return kc >= 9 ? kc - 9 : kc; };
    auto issueX = [&](int kc, unsigned sb, int jj) { glds16(offx, (const char*)x + kc * 128 + jj * (32 * XROW), sb + STG_W + (2 + lw + 4 * jj) * 1024); };
    auto issueW = [&](int kc, unsigned sb, int j) { glds16(offw[j], w + kc * (64 * WROW), sb + (lw + 4 * j) * 1024); };
    auto issue6 = [&](int kc, unsigned sb) {
      if (lw < 3) issueW(kc, sb, 6);
      else { glds16(offw[6], (const char*)x + kc * 128, sb + STG_W); glds16(offh, (const char*)x + kc * 128, sb + STG_W + 1024); }
    };
    const int kc0 = rot, kc1 = kchunk(1);
    const unsigned sb0 = L, sb1 = L + STG;
    auto calcw = [&](int j) {
      unsigned qq = 256 * j + tl256;
      unsigned u = (qq * 2428u) >> 16, cc = qq - u * 27;
      unsigned row = (u & 0x38) | ((u & 1) << 2) | ((u >> 1) & 3);
      offw[j] = row * WROW + cc * 16 + ((cc * 57u) >> 9) * 1008;
    };
    issueX(kc0, sb0, 0);
#pragma unroll
    for (int j = 0; j < 6; ++j) {
      SCHED0();
      calcw(j);
      SCHED0();
      issueW(kc0, sb0, j);
      if (j < 3) issueX(kc0, sb0, j + 1);
    }
    calcw(6);
    {
      int t0 = tile * 128 - 16 + vl, t1 = t0 + 8;
      t0 = t0 < 0 ? 0 : t0; t1 = t1 < 0 ? 0 : t1;
      unsigned h0 = t0 * XROW + ((lane & 7) ^ (vl >> 1)) * 16;
      offh = t1 * XROW + ((lane & 7) ^ (4 + (vl >> 1))) * 16;
      offw[6] = lw == 3 ? h0 : offw[6];
    }
    issue6(kc0, sb0);
#pragma unroll
    for (int j = 0; j < 5; ++j) issueW(kc1, sb1, j);
    f32x4 he = {0, 0, 0, 0};
    const unsigned aE = L + (8 * g + 2 * q) * PITCH + (lw == 0 ? 144 : 288) + ((p & 1) ? 104 : 32);
    const unsigned aX = L + STG_W + c * 128 + ((g ^ (c >> 1)) * 16);
    frag fe[2], fx[2];
    auto halo_rd = [&](unsigned so) {
      if (lw < 2) {
#pragma unroll
        for (int ks = 0; ks < 2; ++ks) {
          fx[ks] = ld128((aX ^ (ks * 64)) + so);
          fe[ks] = ldW(aE + so + ks * KO, aE + so + ks * KO + PITCH);
        }
      }
    };
    auto halo_mma = [&]() {
      if (lw == 0) { he = MFMA(fe[0], fx[0], he); he = MFMA(fe[1], fx[1], he); }
      if (lw == 1) { he = MFMA(fx[0], fe[0], he); he = MFMA(fx[1], fe[1], he); }
    };
    int slot = 0, kn = kc1;
    asm volatile("s_waitcnt vmcnt(5)" ::: "memory");
#pragma clang loop unroll(disable)
    for (int st = 0; st < 8; ++st) {
      BARRIER();
      if (st == 0) {
        issueW(kc1, sb1, 5);
        tag = __hip_atomic_load((GAS u64*)(ws + EPOCH_OFF), RLX_AGENT);
      }
      const unsigned b1 = L + (slot == 2 ? 0 : slot + 1) * STG, b2 = L + (slot == 0 ? 2 : slot - 1) * STG;
      issue6(kn, b1);
      issueX(kn, b1, 0);
      halo_rd(slot * STG);
      issueX(kn, b1, 1); issueX(kn, b1, 2); issueX(kn, b1, 3);
      SCHED0();
      halo_mma();
      SCHED0();
      kn = kn == 8 ? 0 : kn + 1;
      if (st < 7) {
#pragma unroll
        for (int j = 0; j < 6; ++j) issueW(kn, b2, j);
      } else {
#pragma unroll
        for (int I = 0; I < 6; ++I) wo_piece(0, I, lw);
      }
      slot = slot == 2 ? 0 : slot + 1;
      WAIT_LGKM0(); asm volatile("s_waitcnt vmcnt(6)" ::: "memory");
    }
    BARRIER();
    halo_rd(2 * STG);
    wo_piece(0, 6, lw); wo_piece(0, 7, lw);
    SCHED0();
    halo_mma();
    SCHED0();
    if (lw < 2) {
      u32x2 v = pack4(he);
      if (lw == 0 && tile == 0) v = u32x2{0u, 0u};
      st64(L + EX_OFF + lane * 8 + (lw == 0 ? 4 : 9) * 512, v);
    }
    __builtin_amdgcn_s_setprio(0);
#pragma unroll
    for (int I = 8; I < 22; ++I) wo_piece(I / 9, I % 9, lw);
    WAIT_LGKM0();
    BARRIER();
#pragma unroll
    for (int I = 22; I < 34; ++I) wo_piece(I / 9, I % 9, lw);
    WAIT_VM0();
    TAG_SCALAR();
  } else {
    const unsigned wrow = L + (8 * g + 2 * q) * PITCH;
    tag = __hip_atomic_load((GAS u64*)(ws + EPOCH_OFF), RLX_AGENT);
    f32x4 maskP, maskC, maskP0;
    {
      const float ninf = -__builtin_inff();
#pragma unroll
      for (int r = 0; r < 4; ++r) {
        bool pm = (r & 1) == (c & 1);
        maskC[r] = (pm && 4 * g + r <= c) ? 0.f : ninf;
        maskP[r] = (pm && 4 * g + r > c) ? 0.f : ninf;
      }
      maskP0 = maskP + ((tile == 0 && wave == 0) ? -998244352.f * 4.242640687119285f : 0.f);
      asm volatile("" : "+v"(maskP), "+v"(maskC), "+v"(maskP0));
    }
    u32x4 sel;
    {
      const int par = c & 1;
      const bool cur1 = 4 * g + par <= c, cur2 = 4 * g + 2 + par <= c;
      const unsigned s1 = par ? 0x01000c0cu : 0x0c0c0100u;
      const unsigned s2 = par ? 0x03020c0cu : 0x0c0c0302u;
      sel[0] = cur1 ? 0x0c0c0c0cu : s1;
      sel[1] = cur2 ? 0x0c0c0c0cu : s2;
      sel[2] = cur1 ? s1 : 0x0c0c0c0cu;
      sel[3] = cur2 ? s2 : 0x0c0c0c0cu;
    }
    unsigned vmain = lane * 16, vleft = lane < 32 ? c * 16 + g * 8 : 0x40000000u;
    unsigned osoff = (tile * 8 + 2 * wave) * 36864 + grp * 2048, oloff = (tile * 8 + 2 * wave) * 36864 + 32768 + grp * 256;
    GAS u64* myflag = (GAS u64*)(ws + FLAG_OFF + ((grp >> 2) * 64 + tile * 4 + cw) * 128 + (grp & 3) * 8);
    PINS(myflag);
    unsigned osoff1 = osoff + 36864, oloff1 = oloff + 36864;
    PINS(osoff); PINS(oloff); PINS(osoff1); PINS(oloff1);
    asm volatile("" : "+v"(sel), "+v"(vmain), "+v"(vleft));
    f32x4 aK[2][4], aQ[2][4], aKQ[2], aV[2][5], hk, hv;
    frag cv[5], xb[3];
    unsigned exr = L + EX_OFF + wave * 5120 + lane * 8, exh = L + EX_OFF + wave * 512 + lane * 8;
    unsigned exgw = L + SCR_OFF + wave * 128 + g * 16, exgr = L + SCR_OFF + wave * 128 + c * 4;
    PIN(exr); PIN(exh); PIN(exgw); PIN(exgr);
    auto mainloop = [&](auto WC) {
      constexpr int W = decltype(WC)::value;
      constexpr int XO = (1 + 2 * W) * 2048;
    hk = f32x4{0, 0, 0, 0}; hv = f32x4{0, 0, 0, 0};
#pragma unroll
    for (int t = 0; t < 2; ++t) {
#pragma unroll
      for (int f = 0; f < 4; ++f) { aK[t][f] = f32x4{0, 0, 0, 0}; aQ[t][f] = f32x4{0, 0, 0, 0}; }
      aKQ[t] = f32x4{0, 0, 0, 0};
#pragma unroll
      for (int f = 0; f < 5; ++f) aV[t][f] = f32x4{0, 0, 0, 0};
    }
    unsigned cW = wrow + 8 * p;
    unsigned cKQ = wrow + (p < 2 ? 144 : 0) + ((p & 1) ? 104 : 32);
    unsigned cX0 = L + STG_W + c * 128 + ((g ^ (c >> 1)) * 16), cX1 = cX0 ^ 64;
    unsigned cV[4];
#pragma unroll
    for (int i = 0; i < 4; ++i)
      cV[i] = p < 2 ? wrow + 288 + (p ? 104 : 32) + (i >> 1) * KO + (i & 1) * PITCH : L + G_OFF + rot * 512 + (8 * g + q) * 8 + (i >> 1) * 256 + (i & 1) * 32;
#pragma unroll
    for (int f = 0; f < 5; ++f) cv[f] = frag{0, 0, 0, 0};
    xb[0] = frag{0, 0, 0, 0}; xb[1] = frag{0, 0, 0, 0}; xb[2] = frag{0, 0, 0, 0};
    asm volatile("" : "+v"(aK[0][0]), "+v"(aK[0][1]), "+v"(aK[0][2]), "+v"(aK[0][3]), "+v"(aK[1][0]), "+v"(aK[1][1]), "+v"(aK[1][2]), "+v"(aK[1][3]),
                      "+v"(aQ[0][0]), "+v"(aQ[0][1]), "+v"(aQ[0][2]), "+v"(aQ[0][3]), "+v"(aQ[1][0]), "+v"(aQ[1][1]), "+v"(aQ[1][2]), "+v"(aQ[1][3]),
                      "+v"(aKQ[0]), "+v"(aKQ[1]), "+v"(hk), "+v"(hv), "+v"(xb[0]), "+v"(xb[1]), "+v"(xb[2]));
    asm volatile("" : "+v"(aV[0][0]), "+v"(aV[0][1]), "+v"(aV[0][2]), "+v"(aV[0][3]), "+v"(aV[0][4]), "+v"(aV[1][0]), "+v"(aV[1][1]), "+v"(aV[1][2]),
                      "+v"(aV[1][3]), "+v"(aV[1][4]), "+v"(cv[0]), "+v"(cv[1]), "+v"(cv[2]), "+v"(cv[3]), "+v"(cv[4]),
                      "+v"(cW), "+v"(cKQ), "+v"(cX0), "+v"(cX1), "+v"(cV[0]), "+v"(cV[1]), "+v"(cV[2]), "+v"(cV[3]));
    SCHED0();
    if (W < 2 || (W == 2 && lane < 16)) {
      unsigned a0 = __builtin_amdgcn_perm(gv[0], gv[0], 0x01000100), a1 = __builtin_amdgcn_perm(gv[0], gv[0], 0x03020302);
      unsigned a2 = __builtin_amdgcn_perm(gv[1], gv[1], 0x01000100), a3 = __builtin_amdgcn_perm(gv[1], gv[1], 0x03020302);
      *(LDSP u32x4*)(size_t)(L + G_OFF + tid * 32) = u32x4{a0, a0, a1, a1};
      *(LDSP u32x4*)(size_t)(L + G_OFF + tid * 32 + 16) = u32x4{a2, a2, a3, a3};
    }
    WAIT_LGKM0();
      int slot = 0, kc = rot;
#pragma clang loop unroll(disable)
      for (int st = 0; st < 9; ++st) {
        BARRIER();
        SCHED0();
        frag xa[3], W0[14], W1[9];
        xa[0] = ld128(cX0 + XO); xa[1] = ld128(cX0 + XO + 2048);
#pragma unroll
        for (int f = 0; f < 4; ++f) W0[f ^ 1] = ldW(cW + 144 + HOFF(f ^ 1), cW + 144 + HOFF(f ^ 1) + PITCH);
        xa[2] = ld128(cX0);
#pragma unroll
        for (int f = 0; f < 5; ++f) { aV[0][f] = MFMA(xb[0], cv[f], aV[0][f]); aV[1][f] = MFMA(xb[1], cv[f], aV[1][f]); }
        hv = MFMA(xb[2], cv[W], hv);
        W0[5] = ldW(cW + HOFF(0), cW + HOFF(0) + PITCH);
        W0[4] = ldW(cKQ, cKQ + PITCH);
        W0[7] = ldW(cW + HOFF(2), cW + HOFF(2) + PITCH);
        W0[6] = ldW(cW + HOFF(1), cW + HOFF(1) + PITCH);
        W0[9] = ldW(cW + 288 + HOFF(0), cW + 288 + HOFF(0) + PITCH);
        W0[8] = ldW(cW + HOFF(3), cW + HOFF(3) + PITCH);
        W0[11] = ldW(cW + 288 + HOFF(2), cW + 288 + HOFF(2) + PITCH);
        W0[10] = ldW(cW + 288 + HOFF(1), cW + 288 + HOFF(1) + PITCH);
        W0[13] = ldW(cV[0], cV[1]);
        W0[12] = ldW(cW + 288 + HOFF(3), cW + 288 + HOFF(3) + PITCH);
        xb[0] = ld128(cX1 + XO); xb[1] = ld128(cX1 + XO + 2048); xb[2] = ld128(cX1);
#pragma unroll
        for (int f = 0; f < 4; ++f) W1[f ^ 1] = ldW(cW + KO + 144 + HOFF(f ^ 1), cW + KO + 144 + HOFF(f ^ 1) + PITCH);
        W1[5] = ldW(cW + KO + HOFF(0), cW + KO + HOFF(0) + PITCH);
        W1[4] = ldW(cKQ + KO, cKQ + KO + PITCH);
        W1[8] = ldW(cW + KO + HOFF(3), cW + KO + HOFF(3) + PITCH);
        W1[7] = ldW(cW + KO + HOFF(2), cW + KO + HOFF(2) + PITCH);
        W1[6] = ldW(cW + KO + HOFF(1), cW + KO + HOFF(1) + PITCH);
#pragma unroll
        for (int f = 0; f < 4; ++f) cv[f] = ldW(cW + KO + 288 + HOFF(f), cW + KO + 288 + HOFF(f) + PITCH);
        cv[4] = ldW(cV[2], cV[3]);
#pragma unroll
        for (int f = 0; f < 4; ++f) { aK[0][f] = MFMA(W0[f], xa[0], aK[0][f]); aK[1][f] = MFMA(W0[f], xa[1], aK[1][f]); }
        hk = MFMA(W0[W], xa[2], hk);
        aKQ[0] = MFMA(W0[4], xa[0], aKQ[0]); aKQ[1] = MFMA(W0[4], xa[1], aKQ[1]);
#pragma unroll
        for (int f = 0; f < 4; ++f) { aQ[0][f] = MFMA(W0[5 + f], xa[0], aQ[0][f]); aQ[1][f] = MFMA(W0[5 + f], xa[1], aQ[1][f]); }
#pragma unroll
        for (int f = 0; f < 5; ++f) { aV[0][f] = MFMA(xa[0], W0[9 + f], aV[0][f]); aV[1][f] = MFMA(xa[1], W0[9 + f], aV[1][f]); }
        hv = MFMA(xa[2], W0[9 + W], hv);
#pragma unroll
        for (int f = 0; f < 4; ++f) { aK[0][f] = MFMA(W1[f], xb[0], aK[0][f]); aK[1][f] = MFMA(W1[f], xb[1], aK[1][f]); }
        hk = MFMA(W1[W], xb[2], hk);
        aKQ[0] = MFMA(W1[4], xb[0], aKQ[0]); aKQ[1] = MFMA(W1[4], xb[1], aKQ[1]);
#pragma unroll
        for (int f = 0; f < 4; ++f) { aQ[0][f] = MFMA(W1[5 + f], xb[0], aQ[0][f]); aQ[1][f] = MFMA(W1[5 + f], xb[1], aQ[1][f]); }
        const int ds = slot == 2 ? -2 * STG : STG, dg = kc == 8 ? -8 * 512 : 512;
        slot = slot == 2 ? 0 : slot + 1;
        kc = kc == 8 ? 0 : kc + 1;
        const int dv = p < 2 ? ds : dg;
        cW += ds; cKQ += ds; cX0 += ds; cX1 += ds;
#pragma unroll
        for (int i = 0; i < 4; ++i) cV[i] += dv;
        SGB(0x100, 2);
#pragma unroll
        for (int i = 0; i < 54; ++i) {
          SGB(0x8, 1);
          if (i < 3 || i == 16 || i == 28 || i == 40) SGB(0x100, 2); else SGB(0x100, 1);
          if (i >= 3 && i < 11) SGB(0x4, 1);
          if (i >= 51) SGB(0x2, 1);
        }
#pragma unroll
        for (int i = 0; i < 4; ++i) { SGB(0x8, 1); SGB(0x2, 2); }
        SGB(0x8, 2);
        SCHED0();
        WAIT_LGKM0();
      }
      hv = MFMA(xb[2], cv[W], hv);
    };
    if (wave == 0) mainloop(std::integral_constant<int, 0>{});
    else if (wave == 1) mainloop(std::integral_constant<int, 1>{});
    else if (wave == 2) mainloop(std::integral_constant<int, 2>{});
    else mainloop(std::integral_constant<int, 3>{});
    const bool g0 = g == 0;
    frag K[3][4], Q[2][4], Vb[5], Va[5];
    u32x2 ks[2], qs[2];
    aV[1][4] = MFMA(xb[1], cv[4], aV[1][4]); PIN(aV[1][4]);
    aV[0][4] = MFMA(xb[0], cv[4], aV[0][4]); PIN(aV[0][4]);
#pragma unroll
    for (int t = 1; t >= 0; --t) {
      u32x2 kq = pack4(aKQ[t]);
      auto r0 = __builtin_amdgcn_permlane32_swap(kq[0], 0u, false, false);
      auto r1 = __builtin_amdgcn_permlane32_swap(kq[1], 0u, false, false);
      ks[t] = u32x2{r0[0], r1[0]}; qs[t] = u32x2{r0[1], r1[1]};
      PIN(ks[t]); PIN(qs[t]);
    }
    SCHED0();
#pragma unroll
    for (int f = 0; f < 4; ++f) { aV[1][f] = MFMA(xb[1], cv[f], aV[1][f]); PIN(aV[1][f]); K[2][f] = cat(pack4(aK[1][f]), ks[1]); PIN(K[2][f]); SCHED0(); }
    {
      u32x2 h = pack4(hk), hh = pack4(hv);
      if (tile == 0) h = u32x2{0u, 0u};
      WR2(exh, h, hh, 0, 5);
    }
    if (c >= 8) { *(LDSP f32x4*)(size_t)(exgw) = aV[0][4]; *(LDSP f32x4*)(size_t)(exgw + 64) = aV[1][4]; }
    SCHED0();
#pragma unroll
    for (int f = 0; f < 4; ++f) {
      aV[0][f] = MFMA(xb[0], cv[f], aV[0][f]); PIN(aV[0][f]);
      u32x2 v = pack4(aV[1][f]); Vb[f][2] = v[0]; Vb[f][3] = v[1]; PIN(Vb[f]); SCHED0();
    }
    { u32x2 v = pack4(aV[1][4]); Vb[4][2] = v[0]; Vb[4][3] = v[1]; PIN(Vb[4]); }
    if (wave < 3) {
#pragma unroll
      for (int f = 0; f < 4; ++f) WR2(exr, (u32x2{K[2][f][0], K[2][f][1]}), (u32x2{Vb[f][2], Vb[f][3]}), 10 + f, 15 + f);
      WR2(exr, ks[1], (u32x2{Vb[4][2], Vb[4][3]}), 14, 19);
    }
    SCHED0();
#pragma unroll
    for (int h = 0; h < 4; ++h) {
      K[1][h] = cat(pack4(aK[0][h]), ks[0]); PIN(K[1][h]);
      u32x2 qt = pack4(aQ[1][h]);
      unsigned e = ((h < 2) == g0) ? qs[1][h & 1] : 0u;
      Q[1][h] = (h & 1) ? frag{qt[0], qt[1], 0u, e} : frag{qt[0], qt[1], e, 0u}; PIN(Q[1][h]);
    }
    SCHED0();
    TAG_SCALAR();
    WAIT_VM0(); WAIT_LGKM0();
    BARRIER();
    SCHED0();
    u32x2 gz;
    asm volatile("ds_read2_b32 %0, %1 offset0:0 offset1:16" : "=v"(gz) : "v"(exgr) : "memory");
#pragma unroll
    for (int h = 0; h < 4; ++h) asm volatile("ds_read2st64_b64 %0, %1 offset0:%2 offset1:4" : "=v"(K[0][h]) : "v"(exr), "i"(h) : "memory");
    f32x4 sc[2][4], sp[2][4];
#pragma unroll
    for (int h = 0; h < 4; ++h) {
      sc[1][h] = MFMA(K[2][h], Q[1][h], maskC); PIN(sc[1][h]);
      sp[1][h] = MFMA(K[1][h], Q[1][h], maskP); PIN(sp[1][h]);
      u32x2 qt = pack4(aQ[0][h]);
      unsigned e = ((h < 2) == g0) ? qs[0][h & 1] : 0u;
      Q[0][h] = (h & 1) ? frag{qt[0], qt[1], 0u, e} : frag{qt[0], qt[1], e, 0u}; PIN(Q[0][h]);
      SCHED0();
    }
    asm volatile("s_waitcnt lgkmcnt(0)" : "+v"(gz), "+v"(K[0][0]), "+v"(K[0][1]), "+v"(K[0][2]), "+v"(K[0][3])::"memory");
    float v[2][4][2];
#pragma unroll
    for (int h = 0; h < 4; ++h) {
      sc[0][h] = MFMA(K[1][h], Q[0][h], maskC); PIN(sc[0][h]);
      sp[0][h] = MFMA(K[0][h], Q[0][h], maskP0); PIN(sp[0][h]);
      v[1][h][0] = fmx(fmx3(sp[1][h][0], sp[1][h][1], sc[1][h][0]), sc[1][h][1]); PIN(v[1][h][0]);
      v[1][h][1] = fmx(fmx3(sp[1][h][2], sp[1][h][3], sc[1][h][2]), sc[1][h][3]); PIN(v[1][h][1]);
      SCHED0();
    }
#pragma unroll
    for (int f = 0; f < 5; ++f) {
      u32x2 a = pack4(aV[0][f]); Vb[f][0] = a[0]; Vb[f][1] = a[1]; PIN(Vb[f]);
      Va[f] = __builtin_shufflevector(Vb[f], Vb[f], -1, -1, 0, 1); PIN(Va[f]);
    }
#pragma unroll
    for (int f = 0; f < 5; ++f) { u32x2 n = *(volatile LDSP u32x2*)(size_t)(exr + (5 + f) * 512); Va[f][0] = n[0]; Va[f][1] = n[1]; }
    u32x4 pk[2];
    auto softmax = [&](float (&w)[4][2], float gate_logit) {
      swap32(w[0][0], w[2][0]); swap32(w[1][0], w[3][0]); swap32(w[0][1], w[2][1]); swap32(w[1][1], w[3][1]);
      swap16(w[0][0], w[1][0]); swap16(w[2][0], w[3][0]); swap16(w[0][1], w[1][1]); swap16(w[2][1], w[3][1]);
      const float cs = 0.23570226039551584f * 1.4426950408889634f;
      float m = fmx(fmx3(fmx3(w[0][0], w[1][0], w[2][0]), w[3][0], w[0][1]), fmx3(w[1][1], w[2][1], w[3][1]));
      f32x2 e[4];
#pragma unroll
      for (int i = 0; i < 4; ++i) {
        f32x2 a = f32x2{w[i][0], w[i][1]} - m;
        PIN(a);
        a *= cs;
        e[i] = f32x2{__builtin_amdgcn_exp2f(a[0]), __builtin_amdgcn_exp2f(a[1])};
      }
      f32x2 s2 = (e[0] + e[1]) + (e[2] + e[3]);
      float s = s2[0] + s2[1];
      float r = __builtin_amdgcn_rcpf(__builtin_fmaf(s, __builtin_amdgcn_exp2f(gate_logit * sg), s));
      float pf[4];
#pragma unroll
      for (int i = 0; i < 4; ++i) { f32x2 pp = e[i] * r; pf[i] = __uint_as_float(cvtpk(pp[0], pp[1])); }
      swap32(pf[0], pf[2]); swap32(pf[1], pf[3]);
      swap16(pf[0], pf[1]); swap16(pf[2], pf[3]);
      return u32x4{__float_as_uint(pf[0]), __float_as_uint(pf[1]), __float_as_uint(pf[2]), __float_as_uint(pf[3])};
    };
    SCHED0();
#pragma unroll
    for (int h = 0; h < 4; ++h) {
      v[0][h][0] = fmx(fmx3(sp[0][h][0], sp[0][h][1], sc[0][h][0]), sc[0][h][1]);
      v[0][h][1] = fmx(fmx3(sp[0][h][2], sp[0][h][3], sc[0][h][2]), sc[0][h][3]);
    }
    pk[1] = softmax(v[1], __uint_as_float(gz[1]));
#pragma unroll
    for (int h = 0; h < 4; ++h) { PIN(v[0][h][0]); PIN(v[0][h][1]); }
    SCHED0();
    pv_store(pk[1], Vb, sel, vmain, vleft, ors, osoff1, oloff1, g0);
    pk[0] = softmax(v[0], __uint_as_float(gz[0]));
    SCHED0();
    pv_store(pk[0], Va, sel, vmain, vleft, ors, osoff, oloff, g0);
    WAIT_VM0();
    if (lane == 0) __hip_atomic_store(myflag, tag, RLX_AGENT);
    asm volatile(".Lfk_tail:");
  }
  BARRIER();
  if (!act2) {
    if (bid == 255) {
      if (wave < 4) {
        const int b = wave * 64 + lane;
        GAS u64* fp = (GAS u64*)(ws + FLAG_OFF + (((b & 15) >> 2) * 64 + (b >> 4) * 4) * 128 + (b & 3) * 8);
#pragma clang loop unroll(disable)
        for (unsigned spins = 0; spins < (1u << 17); ++spins) {
          u64 v = __hip_atomic_load(fp, RLX_AGENT);
          if (__all(v == tag)) break;
          __builtin_amdgcn_s_sleep(8);
        }
      }
      BARRIER();
      if (tid == 0) __hip_atomic_store((GAS u64*)(ws + EPOCH_OFF), tag, RLX_AGENT);
    }
    return;
  }
  if (cw == 3) {
#pragma unroll
    for (int j = 0; j < 4; ++j) { wo_piece(3, 7, j); wo_piece(3, 8, j); }
  }
  if (wave < 4) phase2<2>(L, ws, ors, tag, rt * 5 + 3, 48, cw, rot2, lane);
  else phase2<3>(L, ws, ors, tag, rt * 5, 0, cw, rot2, lane);
  if (wave < 5 && rt * 5 + wave < 128) {
    f32x4 s[2][2];
#pragma unroll
    for (int it = 0; it < 2; ++it) {
      const int r = it * 8 + vl;
      const unsigned a0 = L + (wave * 16 + r) * 256 + ((((lane & 7) * 2) ^ r) * 16);
#pragma unroll
      for (int w = 0; w < 4; ++w) {
        f32x4 u0 = *(LDSP f32x4*)(size_t)(a0 + w * 36864), u1 = *(LDSP f32x4*)(size_t)((a0 ^ 16) + w * 36864);
        s[it][0] = w ? s[it][0] + u0 : u0; s[it][1] = w ? s[it][1] + u1 : u1;
      }
    }
#pragma unroll
    for (int it = 0; it < 2; ++it) {
      u32x4 o = {cvtpk(s[it][0][0], s[it][0][1]), cvtpk(s[it][0][2], s[it][0][3]), cvtpk(s[it][1][0], s[it][1][1]), cvtpk(s[it][1][2], s[it][1][3])};
      *(u32x4*)((char*)out + (size_t)(rt * 80 + wave * 16 + it * 8 + vl) * XROW + ct * 128 + (lane & 7) * 16) = o;
    }
  }
}

extern "C" void kernel_launch(void* const* d_in, const int* in_sizes, int n_in, void* d_out, int out_size, void* d_ws, size_t ws_size,
                              hipStream_t stream) {
  fused_kernel<<<256, 512, 0, stream>>>((const u16*)d_in[0], (const u16*)d_in[1], (const u16*)d_in[4], (const u16*)d_in[2], (const u16*)d_in[5],
                                        (const u16*)d_in[6], (char*)d_ws, (u16*)d_out);
}
```

```cpp
#include <hip/hip_runtime.h>
#include <hip/hip_bf16.h>
#include <stdint.h>
#include <type_traits>

typedef unsigned short u16;
typedef unsigned long long u64;
typedef short s16x4 __attribute__((ext_vector_type(4)));
typedef float f32x2 __attribute__((ext_vector_type(2)));
typedef float f32x4 __attribute__((ext_vector_type(4)));
typedef unsigned u32x2 __attribute__((ext_vector_type(2)));
typedef unsigned u32x4 __attribute__((ext_vector_type(4)));
typedef u32x4 frag;
typedef __bf16 bf16x2 __attribute__((ext_vector_type(2)));
typedef __bf16 bfx8 __attribute__((ext_vector_type(8)));
#define LDSP __attribute__((address_space(3)))
#define GAS __attribute__((address_space(1)))
#define DI __device__ __forceinline__

constexpr int WROW = 3456;
constexpr int XROW = 1152;
constexpr int PITCH = 432;
constexpr int KO = 32 * PITCH;
constexpr int STG_W = 64 * PITCH;
constexpr int STG = STG_W + 144 * 128;
constexpr int G_OFF = 3 * STG;
constexpr int SCR_OFF = G_OFF + 4608;
constexpr int EX_OFF = 143360;
constexpr int LDS_BYTES = 163840;
constexpr int O_BYTES = 128 * 36 * 1024;
constexpr int FLAG_OFF = 8 << 20;
constexpr int EPOCH_OFF = FLAG_OFF + (1 << 20);
#define ROT 11
#define PRE0 5
#define NBX 22
#define FS1 128
#define FS2 8
#define PSLEEP 1
#define CODEPF 22656
#define NB1 5

#define MFMA(a, b, c) __builtin_amdgcn_mfma_f32_16x16x32_bf16(__builtin_bit_cast(bfx8, (a)), __builtin_bit_cast(bfx8, (b)), (c), 0, 0, 0)
#define RLX_AGENT __ATOMIC_RELAXED, __HIP_MEMORY_SCOPE_AGENT
#define HOFF(h) (36 * (h) + ((h) & 1) * 4)

DI void glds16(unsigned voff, const GAS void* sbase, unsigned lds_addr) {
  asm volatile("s_mov_b32 m0, %2\n\ts_nop 0\n\tglobal_load_lds_dwordx4 %0, %1" ::"v"(voff), "s"(sbase), "s"(lds_addr) : "memory");
}
DI u32x2 trd(unsigned a) { return __builtin_bit_cast(u32x2, __builtin_amdgcn_ds_read_tr16_b64_v4i16((LDSP s16x4*)(size_t)a)); }
DI frag ldW(unsigned a0, unsigned a1) {
  u32x2 lo = trd(a0), hi = trd(a1);
  return __builtin_shufflevector(lo, hi, 0, 1, 2, 3);
}
DI frag ld128(unsigned a) { return *(LDSP frag*)(size_t)a; }
DI u32x2 ld64(unsigned a) { return *(LDSP u32x2*)(size_t)a; }
DI void st64(unsigned a, u32x2 v) { *(LDSP u32x2*)(size_t)a = v; }
DI unsigned cvtpk(float a, float b) {
  f32x2 f = {a, b};
  return __builtin_bit_cast(unsigned, __builtin_convertvector(f, bf16x2));
}
DI u32x2 pack4(f32x4 a) { return u32x2{cvtpk(a[0], a[1]), cvtpk(a[2], a[3])}; }
DI frag cat(u32x2 a, u32x2 b) { return __builtin_shufflevector(a, b, 0, 1, 2, 3); }
DI void swap32(float& a, float& b) {
  auto r = __builtin_amdgcn_permlane32_swap(__float_as_uint(a), __float_as_uint(b), false, false);
  a = __uint_as_float(r[0]); b = __uint_as_float(r[1]);
}
DI void swap16(float& a, float& b) {
  auto r = __builtin_amdgcn_permlane16_swap(__float_as_uint(a), __float_as_uint(b), false, false);
  a = __uint_as_float(r[0]); b = __uint_as_float(r[1]);
}
DI float fmx(float a, float b) { return __builtin_elementwise_maximum(a, b); }
DI float fmx3(float a, float b, float c) { return fmx(fmx(a, b), c); }
#define STAMP(i)
#define RSTAMP(i)
#define STAMP_OUT()
#define STAMP_ARG
#define STAMP_PASS
#define STAMP_DECL
#define SGB(m, n) __builtin_amdgcn_sched_group_barrier(m, n, 0)
#define SCHED0() __builtin_amdgcn_sched_barrier(0)
#define WAIT_LGKM0() do { __builtin_amdgcn_s_waitcnt(0xC07F); asm volatile("" ::: "memory"); } while (0)
#define WAIT_VM0() asm volatile("s_waitcnt vmcnt(0)" ::: "memory")
#define BARRIER() __builtin_amdgcn_s_barrier()
#define PIN(x) asm volatile("" : "+v"(x))
#define TAG_SCALAR() do { unsigned lo_ = __builtin_amdgcn_readfirstlane((unsigned)tag), hi_ = __builtin_amdgcn_readfirstlane((unsigned)(tag >> 32)); \
    tag = (((u64)hi_ << 32) | lo_) + 1; asm volatile("" : "+s"(tag)); } while (0)
#define PINS(x) asm volatile("" : "+s"(x))
#define WR2(addr, a, b, o0, o1) asm volatile("ds_write2st64_b64 %0, %1, %2 offset0:%3 offset1:%4" ::"v"(addr), "v"(a), "v"(b), "i"(o0), "i"(o1) : "memory")

DI void pv_store(u32x4 pk, const frag (&V)[5], u32x4 sel, unsigned vmain, unsigned vleft, __amdgpu_buffer_rsrc_t ors, unsigned so, unsigned sl, bool g0) {
  const f32x4 z = {0.f, 0.f, 0.f, 0.f};
  frag P[4];
  f32x4 o[4], os[4];
  auto mkP = [&](int h) {
    unsigned u = pk[h];
    P[h] = frag{__builtin_amdgcn_perm(u, u, sel[0]), __builtin_amdgcn_perm(u, u, sel[1]), __builtin_amdgcn_perm(u, u, sel[2]), __builtin_amdgcn_perm(u, u, sel[3])};
    PIN(P[h]);
  };
  mkP(0); mkP(1);
  SCHED0();
  u32x4 f0, f1;
  os[0] = MFMA(V[4], P[0], z); PIN(os[0]); o[0] = MFMA(V[0], P[0], z); PIN(o[0]); mkP(2); SCHED0();
  os[1] = MFMA(V[4], P[1], z); PIN(os[1]); o[1] = MFMA(V[1], P[1], z); PIN(o[1]); mkP(3); SCHED0();
  os[2] = MFMA(V[4], P[2], z); PIN(os[2]); os[3] = MFMA(V[4], P[3], z); PIN(os[3]);
  o[2] = MFMA(V[2], P[2], z); PIN(o[2]); o[3] = MFMA(V[3], P[3], z); PIN(o[3]); SCHED0();
  f0 = u32x4{cvtpk(o[0][0], o[0][1]), cvtpk(o[0][2], o[0][3]), cvtpk(o[1][0], o[1][1]), cvtpk(o[1][2], o[1][3])};
  PIN(f0); SCHED0();
  __builtin_amdgcn_raw_buffer_store_b128(f0, ors, vmain, so, 16);
  SCHED0();
  u32x2 f2 = {cvtpk(g0 ? os[0][0] : os[2][0], g0 ? os[0][1] : os[2][1]), cvtpk(g0 ? os[1][2] : os[3][2], g0 ? os[1][3] : os[3][3])};
  PIN(f2); SCHED0();
  f1 = u32x4{cvtpk(o[2][0], o[2][1]), cvtpk(o[2][2], o[2][3]), cvtpk(o[3][0], o[3][1]), cvtpk(o[3][2], o[3][3])};
  PIN(f1); PIN(f0); SCHED0();
  __builtin_amdgcn_raw_buffer_store_b128(f1, ors, vmain + 1024, so, 16);
  __builtin_amdgcn_raw_buffer_store_b64(f2, ors, vleft, sl, 16);
  SCHED0();
}

template <int MT>
DI void phase2(unsigned L, GAS char* ws, __amdgpu_buffer_rsrc_t ors, u64 tag, int tt0, int mrow, int kq, int rot2, int lane STAMP_ARG) {
  const int g = lane >> 4, q = (lane >> 2) & 3, p = lane & 3, c = lane & 15;
  const unsigned sw = (2 * g + (q >> 1)) & 3;
  unsigned bw[4];
#pragma unroll
  for (int n = 0; n < 4; ++n) bw[n] = L + kq * 36864 + (4 * g + q) * 128 + 8 * p + ((n ^ sw) * 32);
  frag wf[9][4];
  auto rdw = [&](int i) {
#pragma unroll
    for (int n = 0; n < 4; ++n) wf[i][n] = ldW(bw[n] + i * 4096, bw[n] + i * 4096 + 2048);
  };
  rdw(0); rdw(1); rdw(2); rdw(3); rdw(4);
  SCHED0();
  unsigned so[9], vo[MT];
#pragma unroll
  for (int i = 0; i < 9; ++i) so[i] = (i < 8 ? 8 * kq + ((i + rot2) & 7) : 32 + kq) * 1024;
#pragma unroll
  for (int m = 0; m < MT; ++m) { int tt = tt0 + m; tt = tt > 127 ? 127 : tt; vo[m] = lane * 16 + tt * 36864; }
  int tf = (lane & 4) ? tt0 + MT - 1 : tt0; tf = tf > 127 ? 127 : tf;
  GAS u64* fp = (GAS u64*)(ws + FLAG_OFF + (kq * 64 + (tf >> 1)) * FS1 + (lane & 3) * FS2);
  unsigned rb[4];
#pragma unroll
  for (int n = 0; n < 4; ++n) rb[n] = L + kq * 36864 + (mrow + c) * 256 + (((4 * n + g) ^ c) * 16);
#pragma unroll
  for (int i = 0; i < 9; ++i) PINS(so[i]);
#pragma unroll
  for (int m = 0; m < MT; ++m) PIN(vo[m]);
#pragma unroll
  for (int n = 0; n < 4; ++n) PIN(rb[n]);
  PIN(fp);
  WAIT_LGKM0();
  BARRIER();
  STAMP(27);
#pragma clang loop unroll(disable)
  for (unsigned spins = 0; spins < (1u << 17); ++spins) {
    u64 v = __hip_atomic_load(fp, RLX_AGENT);
    if (__all(v == tag)) break;
    __builtin_amdgcn_s_sleep(PSLEEP);
  }
  __builtin_amdgcn_fence(__ATOMIC_ACQUIRE, "wavefront");
  asm volatile("" ::: "memory");
  STAMP(28);
  RSTAMP(62);
  frag of[9][MT];
  constexpr int WIN = 2;
#pragma unroll
  for (int i = 0; i < WIN; ++i)
#pragma unroll
    for (int m = 0; m < MT; ++m) { of[i][m] = __builtin_amdgcn_raw_buffer_load_b128(ors, vo[m], so[i], 16); SCHED0(); }
  f32x4 acc[MT][4];
#pragma unroll
  for (int i = 0; i < 9; ++i) {
    const bool rd = i >= 2 && i + 3 < 9;
    if (rd) rdw(i + 3);
#pragma unroll
    for (int m = 0; m < MT; ++m) {
#pragma unroll
      for (int n = 0; n < 4; ++n) acc[m][n] = MFMA(wf[i][n], of[i][m], i == 0 ? (f32x4{0, 0, 0, 0}) : acc[m][n]);
      if (i + WIN < 9) of[i + WIN][m] = __builtin_amdgcn_raw_buffer_load_b128(ors, vo[m], so[i + WIN], 16);
    }
#pragma unroll
    for (int m = 0; m < MT; ++m) {
#pragma unroll
      for (int n = 0; n < 4; ++n) { SGB(0x8, 1); if (m < 2 && rd) SGB(0x100, 1); }
      if (i + WIN < 9) SGB(0x20, 1);
    }
    SCHED0();
  }
  STAMP(29);
#pragma unroll
  for (int m = 0; m < MT; ++m)
#pragma unroll
    for (int n = 0; n < 4; ++n) *(LDSP f32x4*)(size_t)(rb[n] + m * 4096) = acc[m][n];
  WAIT_LGKM0();
  BARRIER();
}

typedef u64 u64x8 __attribute__((ext_vector_type(8)));
extern "C" __global__ __launch_bounds__(512, 2) void fused_kernel(const u16* x_, const u16* wA_, const u16* wB_, const u16* woA_, const u16* woB_,
                                                                   const u16* gw_, char* ws_, u16* out_) {
  __shared__ __attribute__((aligned(1024))) char lds[LDS_BYTES];
  const unsigned L = (unsigned)(size_t)(LDSP char*)lds;
  const int BIDX = blockIdx.x, TIDX = threadIdx.x;
  u64x8 ka;
  asm volatile("s_load_dwordx16 %0, %1, 0x0" : "=s"(ka) : "s"(__builtin_amdgcn_kernarg_segment_ptr()));
#define KARG_WAIT() do { SCHED0(); asm volatile("s_waitcnt lgkmcnt(0)" ::: "memory"); SCHED0(); asm volatile("" : "+s"(ka)); } while (0)
#define x ((const GAS u16*)ka[0])
#define wA ((const GAS u16*)ka[1])
#define wB ((const GAS u16*)ka[2])
#define woA ((const GAS u16*)ka[3])
#define woB ((const GAS u16*)ka[4])
#define gw ((const GAS u16*)ka[5])
#define ws ((GAS char*)ka[6])
#define out ((GAS u16*)ka[7])
#define ors __builtin_amdgcn_make_buffer_rsrc((void*)ka[6], 0, O_BYTES, 0x00020000)
  const int bid = BIDX;
  const int xcd = bid & 7, idx = bid >> 3;
  const int tl = idx & 7, gl = idx >> 3;
  const int tile = (xcd & 1) * 8 + tl;
  const int grp = (xcd >> 1) * 4 + gl;
  int rot = ROT == 1 ? tl + 2 * gl : ROT == 2 ? (tl >> 1) * 2 + 2 * gl : ROT == 6 ? (tl % 3) * 3 + (gl % 3) * 3 : ROT == 7 ? (((tl >> 1) + gl) & 3) * 2 + (tl & 1 & (gl >> 1))
          : ROT == 8 ? (((tl + gl) & 3) * 9) >> 2 : ROT == 9 ? ((tl + gl + (xcd >> 1)) % 3) * 3 : ROT == 10 ? (((tl + gl) & 3) * 9 >> 2) + (xcd & 1)
          : ROT == 11 ? ((tl + gl) & 3) * 2 + ((xcd ^ (xcd >> 1)) & 1) : ROT == 12 ? ((tl + gl) % 3) * 3 + ((xcd & 1) + (xcd >> 1)) % 3
          : ROT == 13 ? ((tl + gl) & 1) * 4 + (((xcd & 1) * 2 + (xcd >> 1)) & 3) : ROT == 14 ? (tl + 2 * gl + ((xcd >> 1) & 1)) % 9
          : ROT == 15 ? (((tl + gl) & 3) * 2 + ((xcd ^ (xcd >> 1)) & 1) + (xcd >> 2)) : 0;
  if (rot >= 9) rot -= 9;
  const int tid = TIDX, lane = tid & 63;
  const int wave = __builtin_amdgcn_readfirstlane(tid >> 6);
  const int vl = lane >> 3;
  STAMP_DECL;
  STAMP(0);
  RSTAMP(60);
  if (tid < 7) {
    u64 pc0;
    unsigned len;
    asm volatile("s_getpc_b64 %0\n\ts_mov_b32 %1, .Lfk_tail-fused_kernel+128" : "=s"(pc0), "=s"(len));
    unsigned off = (idx * 7 + tid) * 128;
    off = off < len ? off : 0;
    asm volatile("s_mov_b32 m0, %2\n\ts_nop 0\n\tglobal_load_lds_dword %0, %1" ::"v"(off), "s"(pc0 & ~255ull), "s"(L + SCR_OFF) : "memory");
  }
  u64 tag;
  const int lin0 = xcd * 30 + idx;
  const bool act2 = idx < 30 && lin0 < 234;
  const int lin = act2 ? lin0 : 0;
  const int rt = (lin * 57) >> 9, ct = lin - rt * 9;
  const int rot2 = 3 * (rt - ((rt * 171) >> 9) * 3);
  const float sg = (grp >= 8) ? 1.4426950408889634f : -1.4426950408889634f;
  const unsigned wsch = ((((lane & 7) >> 1) ^ ((vl >> 1) & 3)) << 5) | ((lane & 1) << 4);
  const unsigned offn = vl * XROW + wsch, offl = ((vl >> 2) * 72 + (vl & 3)) * XROW + wsch;
  auto wo_piece = [&](int kq, int i, int j) {
    const GAS char* b = (const GAS char*)(kq >= 2 ? woB : woA) + ct * 128 + (kq & 1) * (288 * XROW);
    if (i == 8) b += ((j & 1) * 144 + ((j >> 1) ? 52 : 16)) * XROW;
    else b += (((i + rot2) & 7) * 36 + (j >> 1) * 20 + 8 * (j & 1)) * XROW;
    glds16(i == 8 ? offl : offn, b, L + (kq * 9 + i) * 4096 + j * 1024);
  };
  const int g = lane >> 4, q = (lane >> 2) & 3, p = lane & 3, c = lane & 15;
  const int cw = wave & 3;

  if (__builtin_expect(wave >= 4, 1)) {
    __builtin_amdgcn_s_setprio(2);
    const int lw = cw, tl256 = tid - 256;
    const unsigned offx = (tile * 128 + 8 * lw + vl) * XROW + ((lane & 7) ^ ((4 * lw + (vl >> 1)) & 7)) * 16;
    KARG_WAIT();
    const GAS char* w = (const GAS char*)((grp >= 8) ? wB : wA) + (grp & 7) * 144;
    unsigned offw[7], offh;
    auto kchunk = [&](int st) { int kc = st + rot; return kc >= 9 ? kc - 9 : kc; };
    auto issueX = [&](int kc, unsigned sb, int jj) { glds16(offx, (const GAS char*)x + kc * 128 + jj * (32 * XROW), sb + STG_W + (2 + lw + 4 * jj) * 1024); };
    auto issueW = [&](int kc, unsigned sb, int j) { glds16(offw[j], w + kc * (64 * WROW), sb + (lw + 4 * j) * 1024); };
    auto issue6 = [&](int kc, unsigned sb) {
      if (lw < 3) issueW(kc, sb, 6);
      else { glds16(offw[6], (const GAS char*)x + kc * 128, sb + STG_W); glds16(offh, (const GAS char*)x + kc * 128, sb + STG_W + 1024); }
    };
    const int kc0 = rot, kc1 = kchunk(1);
    const unsigned sb0 = L, sb1 = L + STG;
    auto calcw = [&](int j) {
      unsigned qq = 256 * j + tl256;
      unsigned u = (qq * 2428u) >> 16, cc = qq - u * 27;
      unsigned row = (u & 0x38) | ((u & 1) << 2) | ((u >> 1) & 3);
      offw[j] = row * WROW + cc * 16 + ((cc * 57u) >> 9) * 1008;
    };
    issueX(kc0, sb0, 0);
    u32x2 gv;
    const unsigned gi = tl256 < 144 ? tl256 : 143;
    asm volatile("global_load_dwordx2 %0, %1, %2" : "=v"(gv) : "v"(gi * 8), "s"(gw) : "memory");
#pragma unroll
    for (int j = 0; j < 6; ++j) {
      SCHED0();
      calcw(j);
      SCHED0();
      issueW(kc0, sb0, j);
      if (j < 3) issueX(kc0, sb0, j + 1);
    }
    calcw(6);
    {
      int t0 = tile * 128 - 16 + vl, t1 = t0 + 8;
      t0 = t0 < 0 ? 0 : t0; t1 = t1 < 0 ? 0 : t1;
      unsigned h0 = t0 * XROW + ((lane & 7) ^ (vl >> 1)) * 16;
      offh = t1 * XROW + ((lane & 7) ^ (4 + (vl >> 1))) * 16;
      offw[6] = lw == 3 ? h0 : offw[6];
    }
    issue6(kc0, sb0);
    STAMP(1);
    issueW(kc1, sb1, 0);
    f32x4 he = {0, 0, 0, 0};
    unsigned aE = L + (8 * g + 2 * q) * PITCH + (lw == 0 ? 144 : 288) + ((p & 1) ? 104 : 32);
    unsigned aX = L + STG_W + c * 128 + ((g ^ (c >> 1)) * 16);
    frag fe[2], fx[2];
    auto halo_rd = [&](unsigned so) {
      if (lw < 2) {
#pragma unroll
        for (int ks = 0; ks < 2; ++ks) {
          fx[ks] = ld128((aX ^ (ks * 64)) + so);
          fe[ks] = ldW(aE + so + ks * KO, aE + so + ks * KO + PITCH);
        }
      }
    };
    auto halo_mma = [&]() {
      if (lw == 0) { he = MFMA(fe[0], fx[0], he); he = MFMA(fe[1], fx[1], he); }
      if (lw == 1) { he = MFMA(fx[0], fe[0], he); he = MFMA(fx[1], fe[1], he); }
    };
    int slot = 0, kn = kc1;
    const GAS char* xp = (const GAS char*)x + kn * 128;
    unsigned b1 = sb1, xd = sb1 + STG_W + (2 + lw) * 1024;
    PINS(xp); PINS(xd); PIN(he); PIN(aE); PIN(aX);
    SCHED0();
#pragma unroll
    for (int j = 1; j < PRE0; ++j) issueW(kc1, sb1, j);
    STAMP(2);
    asm volatile("s_waitcnt vmcnt(%1)" : "+v"(gv) : "i"(10 + PRE0) : "memory");
    {
      unsigned a0 = __builtin_amdgcn_perm(gv[0], gv[0], 0x01000100), a1 = __builtin_amdgcn_perm(gv[0], gv[0], 0x03020302);
      unsigned a2 = __builtin_amdgcn_perm(gv[1], gv[1], 0x01000100), a3 = __builtin_amdgcn_perm(gv[1], gv[1], 0x03020302);
      *(LDSP u32x4*)(size_t)(L + G_OFF + gi * 32) = u32x4{a0, a0, a1, a1};
      *(LDSP u32x4*)(size_t)(L + G_OFF + gi * 32 + 16) = u32x4{a2, a2, a3, a3};
    }
    WAIT_LGKM0();
    asm volatile("s_waitcnt vmcnt(%0)" ::"i"(PRE0) : "memory");
#pragma clang loop unroll(disable)
    for (int st = 0; st < 7; ++st) {
      BARRIER();
      glds16(offx, xp, xd);
      SCHED0();
      STAMP(3 + st);
      glds16(offx, xp + 32 * XROW, xd + 4096);
      if (st == 0) {
#pragma unroll
        for (int j = PRE0; j < 6; ++j) issueW(kc1, sb1, j);
        tag = __hip_atomic_load((GAS u64*)(ws + EPOCH_OFF), RLX_AGENT);
      }
      issue6(kn, b1);
      halo_rd(slot * STG);
      glds16(offx, xp + 64 * XROW, xd + 8192);
      glds16(offx, xp + 96 * XROW, xd + 12288);
      SCHED0();
      kn = kn == 8 ? 0 : kn + 1;
      b1 = L + (slot == 0 ? 2 : slot - 1) * STG;
      issueW(kn, b1, 0); issueW(kn, b1, 1);
      SCHED0();
      halo_mma();
      SCHED0();
#pragma unroll
      for (int j = 2; j < 6; ++j) issueW(kn, b1, j);
      slot = slot == 2 ? 0 : slot + 1;
      xp = (const GAS char*)x + kn * 128;
      xd = b1 + STG_W + (2 + lw) * 1024;
      PINS(xp); PINS(xd);
      WAIT_LGKM0(); asm volatile("s_waitcnt vmcnt(6)" ::: "memory");
    }
    BARRIER();
    glds16(offx, xp, xd);
    SCHED0();
    STAMP(10);
    glds16(offx, xp + 32 * XROW, xd + 4096);
    issue6(kn, b1);
    halo_rd(1 * STG);
    glds16(offx, xp + 64 * XROW, xd + 8192);
    glds16(offx, xp + 96 * XROW, xd + 12288);
    SCHED0();
    halo_mma();
    SCHED0();
#pragma unroll
    for (int I = 0; I < 6; ++I) wo_piece(0, I, lw);
    WAIT_LGKM0(); asm volatile("s_waitcnt vmcnt(6)" ::: "memory");
    BARRIER();
    STAMP(11);
    halo_rd(2 * STG);
    wo_piece(0, 6, lw); wo_piece(0, 7, lw);
    SCHED0();
    halo_mma();
    SCHED0();
    if (lw < 2) {
      u32x2 v = pack4(he);
      if (lw == 0 && tile == 0) v = u32x2{0u, 0u};
      st64(L + EX_OFF + lane * 8 + (lw == 0 ? 4 : 9) * 512, v);
    }
    __builtin_amdgcn_s_setprio(0);
#pragma unroll
    for (int I = 8; I < NBX; ++I) wo_piece(I / 9, I % 9, lw);
    WAIT_LGKM0();
    STAMP(12);
    BARRIER();
    STAMP(13);
#pragma unroll
    for (int I = NBX; I < 34; ++I) wo_piece(I / 9, I % 9, lw);
    WAIT_VM0();
    TAG_SCALAR();
    STAMP(14);
  } else {
    const unsigned wrow = L + (8 * g + 2 * q) * PITCH;
    f32x4 maskP, maskC, maskP0;
    {
      const float ninf = -__builtin_inff();
#pragma unroll
      for (int r = 0; r < 4; ++r) {
        bool pm = (r & 1) == (c & 1);
        maskC[r] = (pm && 4 * g + r <= c) ? 0.f : ninf;
        maskP[r] = (pm && 4 * g + r > c) ? 0.f : ninf;
      }
      maskP0 = maskP + ((tile == 0 && wave == 0) ? -998244352.f * 4.242640687119285f : 0.f);
      asm volatile("" : "+v"(maskP), "+v"(maskC), "+v"(maskP0));
    }
    u32x4 sel;
    {
      const int par = c & 1;
      const bool cur1 = 4 * g + par <= c, cur2 = 4 * g + 2 + par <= c;
      const unsigned s1 = par ? 0x01000c0cu : 0x0c0c0100u;
      const unsigned s2 = par ? 0x03020c0cu : 0x0c0c0302u;
      sel[0] = cur1 ? 0x0c0c0c0cu : s1;
      sel[1] = cur2 ? 0x0c0c0c0cu : s2;
      sel[2] = cur1 ? s1 : 0x0c0c0c0cu;
      sel[3] = cur2 ? s2 : 0x0c0c0c0cu;
    }
    unsigned vmain = lane * 16, vleft = lane < 32 ? c * 16 + g * 8 : 0x40000000u;
    unsigned osoff = (tile * 8 + 2 * wave) * 36864 + grp * 2048, oloff = (tile * 8 + 2 * wave) * 36864 + 32768 + grp * 256;
    unsigned osoff1 = osoff + 36864, oloff1 = oloff + 36864;
    PINS(osoff); PINS(oloff); PINS(osoff1); PINS(oloff1);
    asm volatile("" : "+v"(sel), "+v"(vmain), "+v"(vleft));
    f32x4 aK[2][4], aQ[2][4], aKQ[2], aV[2][5], hk, hv;
    frag cv[5], xb[3];
    unsigned exr = L + EX_OFF + wave * 5120 + lane * 8, exh = L + EX_OFF + wave * 512 + lane * 8;
    unsigned exgw = L + SCR_OFF + wave * 128 + g * 16, exgr = L + SCR_OFF + wave * 128 + c * 4;
    PIN(exr); PIN(exh); PIN(exgw); PIN(exgr);
    auto mainloop = [&](auto WC) {
      constexpr int W = decltype(WC)::value;
      constexpr int XO = (1 + 2 * W) * 2048;
    hk = f32x4{0, 0, 0, 0}; hv = f32x4{0, 0, 0, 0};
#pragma unroll
    for (int t = 0; t < 2; ++t) {
#pragma unroll
      for (int f = 0; f < 4; ++f) { aK[t][f] = f32x4{0, 0, 0, 0}; aQ[t][f] = f32x4{0, 0, 0, 0}; }
      aKQ[t] = f32x4{0, 0, 0, 0};
#pragma unroll
      for (int f = 0; f < 5; ++f) aV[t][f] = f32x4{0, 0, 0, 0};
    }
    unsigned cW = wrow + 8 * p;
    unsigned cKQ = wrow + (p < 2 ? 144 : 0) + ((p & 1) ? 104 : 32);
    unsigned cX0 = L + STG_W + c * 128 + ((g ^ (c >> 1)) * 16), cX1 = cX0 ^ 64;
    unsigned cV[4];
#pragma unroll
    for (int i = 0; i < 4; ++i)
      cV[i] = p < 2 ? wrow + 288 + (p ? 104 : 32) + (i >> 1) * KO + (i & 1) * PITCH : L + G_OFF + rot * 512 + (8 * g + q) * 8 + (i >> 1) * 256 + (i & 1) * 32;
#pragma unroll
    for (int f = 0; f < 5; ++f) cv[f] = frag{0, 0, 0, 0};
    xb[0] = frag{0, 0, 0, 0}; xb[1] = frag{0, 0, 0, 0}; xb[2] = frag{0, 0, 0, 0};
    asm volatile("" : "+v"(aK[0][0]), "+v"(aK[0][1]), "+v"(aK[0][2]), "+v"(aK[0][3]), "+v"(aK[1][0]), "+v"(aK[1][1]), "+v"(aK[1][2]), "+v"(aK[1][3]),
                      "+v"(aQ[0][0]), "+v"(aQ[0][1]), "+v"(aQ[0][2]), "+v"(aQ[0][3]), "+v"(aQ[1][0]), "+v"(aQ[1][1]), "+v"(aQ[1][2]), "+v"(aQ[1][3]),
                      "+v"(aKQ[0]), "+v"(aKQ[1]), "+v"(hk), "+v"(hv), "+v"(xb[0]), "+v"(xb[1]), "+v"(xb[2]));
    asm volatile("" : "+v"(aV[0][0]), "+v"(aV[0][1]), "+v"(aV[0][2]), "+v"(aV[0][3]), "+v"(aV[0][4]), "+v"(aV[1][0]), "+v"(aV[1][1]), "+v"(aV[1][2]),
                      "+v"(aV[1][3]), "+v"(aV[1][4]), "+v"(cv[0]), "+v"(cv[1]), "+v"(cv[2]), "+v"(cv[3]), "+v"(cv[4]),
                      "+v"(cW), "+v"(cKQ), "+v"(cX0), "+v"(cX1), "+v"(cV[0]), "+v"(cV[1]), "+v"(cV[2]), "+v"(cV[3]));
    SCHED0();
    STAMP(1);
    KARG_WAIT();
    tag = __hip_atomic_load((GAS u64*)(ws + EPOCH_OFF), RLX_AGENT);
    STAMP(2);
    WAIT_LGKM0();
      int slot = 0, kc = rot;
#pragma clang loop unroll(disable)
      for (int st = 0; st < 9; ++st) {
        BARRIER();
        SCHED0();
        frag xa[3], W0[14], W1[9];
        xa[0] = ld128(cX0 + XO); xa[1] = ld128(cX0 + XO + 2048);
#pragma unroll
        for (int f = 0; f < 4; ++f) W0[f ^ 1] = ldW(cW + 144 + HOFF(f ^ 1), cW + 144 + HOFF(f ^ 1) + PITCH);
        xa[2] = ld128(cX0);
#pragma unroll
        for (int f = 0; f < 5; ++f) { aV[0][f] = MFMA(xb[0], cv[f], aV[0][f]); aV[1][f] = MFMA(xb[1], cv[f], aV[1][f]); }
        hv = MFMA(xb[2], cv[W], hv);
        W0[5] = ldW(cW + HOFF(0), cW + HOFF(0) + PITCH);
        W0[4] = ldW(cKQ, cKQ + PITCH);
        W0[7] = ldW(cW + HOFF(2), cW + HOFF(2) + PITCH);
        W0[6] = ldW(cW + HOFF(1), cW + HOFF(1) + PITCH);
        W0[9] = ldW(cW + 288 + HOFF(0), cW + 288 + HOFF(0) + PITCH);
        W0[8] = ldW(cW + HOFF(3), cW + HOFF(3) + PITCH);
        W0[11] = ldW(cW + 288 + HOFF(2), cW + 288 + HOFF(2) + PITCH);
        W0[10] = ldW(cW + 288 + HOFF(1), cW + 288 + HOFF(1) + PITCH);
        W0[13] = ldW(cV[0], cV[1]);
        W0[12] = ldW(cW + 288 + HOFF(3), cW + 288 + HOFF(3) + PITCH);
        xb[0] = ld128(cX1 + XO); xb[1] = ld128(cX1 + XO + 2048); xb[2] = ld128(cX1);
#pragma unroll
        for (int f = 0; f < 4; ++f) W1[f ^ 1] = ldW(cW + KO + 144 + HOFF(f ^ 1), cW + KO + 144 + HOFF(f ^ 1) + PITCH);
        W1[5] = ldW(cW + KO + HOFF(0), cW + KO + HOFF(0) + PITCH);
        W1[4] = ldW(cKQ + KO, cKQ + KO + PITCH);
        W1[8] = ldW(cW + KO + HOFF(3), cW + KO + HOFF(3) + PITCH);
        W1[7] = ldW(cW + KO + HOFF(2), cW + KO + HOFF(2) + PITCH);
        W1[6] = ldW(cW + KO + HOFF(1), cW + KO + HOFF(1) + PITCH);
#pragma unroll
        for (int f = 0; f < 4; ++f) cv[f] = ldW(cW + KO + 288 + HOFF(f), cW + KO + 288 + HOFF(f) + PITCH);
        cv[4] = ldW(cV[2], cV[3]);
#pragma unroll
        for (int f = 0; f < 4; ++f) { aK[0][f] = MFMA(W0[f], xa[0], aK[0][f]); aK[1][f] = MFMA(W0[f], xa[1], aK[1][f]); }
        hk = MFMA(W0[W], xa[2], hk);
        aKQ[0] = MFMA(W0[4], xa[0], aKQ[0]); aKQ[1] = MFMA(W0[4], xa[1], aKQ[1]);
#pragma unroll
        for (int f = 0; f < 4; ++f) { aQ[0][f] = MFMA(W0[5 + f], xa[0], aQ[0][f]); aQ[1][f] = MFMA(W0[5 + f], xa[1], aQ[1][f]); }
#pragma unroll
        for (int f = 0; f < 5; ++f) { aV[0][f] = MFMA(xa[0], W0[9 + f], aV[0][f]); aV[1][f] = MFMA(xa[1], W0[9 + f], aV[1][f]); }
        hv = MFMA(xa[2], W0[9 + W], hv);
#pragma unroll
        for (int f = 0; f < 4; ++f) { aK[0][f] = MFMA(W1[f], xb[0], aK[0][f]); aK[1][f] = MFMA(W1[f], xb[1], aK[1][f]); }
        hk = MFMA(W1[W], xb[2], hk);
        aKQ[0] = MFMA(W1[4], xb[0], aKQ[0]); aKQ[1] = MFMA(W1[4], xb[1], aKQ[1]);
#pragma unroll
        for (int f = 0; f < 4; ++f) { aQ[0][f] = MFMA(W1[5 + f], xb[0], aQ[0][f]); aQ[1][f] = MFMA(W1[5 + f], xb[1], aQ[1][f]); }
        const int ds = slot == 2 ? -2 * STG : STG, dg = kc == 8 ? -8 * 512 : 512;
        slot = slot == 2 ? 0 : slot + 1;
        kc = kc == 8 ? 0 : kc + 1;
        const int dv = p < 2 ? ds : dg;
        cW += ds; cKQ += ds; cX0 += ds; cX1 += ds;
#pragma unroll
        for (int i = 0; i < 4; ++i) cV[i] += dv;
        SGB(0x100, 2);
#pragma unroll
        for (int i = 0; i < 54; ++i) {
          SGB(0x8, 1);
          if (i < 3 || i == 16 || i == 28 || i == 40) SGB(0x100, 2); else SGB(0x100, 1);
          if (i >= 3 && i < 11) SGB(0x4, 1);
          if (i >= 51) SGB(0x2, 1);
        }
#pragma unroll
        for (int i = 0; i < 4; ++i) { SGB(0x8, 1); SGB(0x2, 2); }
        SGB(0x8, 2);
        SCHED0();
        WAIT_LGKM0();
      }
      hv = MFMA(xb[2], cv[W], hv);
    };
    if (wave == 0) mainloop(std::integral_constant<int, 0>{});
    else if (wave == 1) mainloop(std::integral_constant<int, 1>{});
    else if (wave == 2) mainloop(std::integral_constant<int, 2>{});
    else mainloop(std::integral_constant<int, 3>{});
    WAIT_VM0();
    STAMP(12);
    const bool g0 = g == 0;
    frag K[3][4], Q[2][4], Vb[5], Va[5];
    u32x2 ks[2], qs[2];
    aV[1][4] = MFMA(xb[1], cv[4], aV[1][4]); PIN(aV[1][4]);
    aV[0][4] = MFMA(xb[0], cv[4], aV[0][4]); PIN(aV[0][4]);
#pragma unroll
    for (int t = 1; t >= 0; --t) {
      u32x2 kq = pack4(aKQ[t]);
      auto r0 = __builtin_amdgcn_permlane32_swap(kq[0], 0u, false, false);
      auto r1 = __builtin_amdgcn_permlane32_swap(kq[1], 0u, false, false);
      ks[t] = u32x2{r0[0], r1[0]}; qs[t] = u32x2{r0[1], r1[1]};
      PIN(ks[t]); PIN(qs[t]);
    }
    SCHED0();
#pragma unroll
    for (int f = 0; f < 4; ++f) { aV[1][f] = MFMA(xb[1], cv[f], aV[1][f]); PIN(aV[1][f]); K[2][f] = cat(pack4(aK[1][f]), ks[1]); PIN(K[2][f]); SCHED0(); }
    {
      u32x2 h = pack4(hk), hh = pack4(hv);
      if (tile == 0) h = u32x2{0u, 0u};
      WR2(exh, h, hh, 0, 5);
    }
    if (c >= 8) { *(LDSP f32x4*)(size_t)(exgw) = aV[0][4]; *(LDSP f32x4*)(size_t)(exgw + 64) = aV[1][4]; }
    SCHED0();
#pragma unroll
    for (int f = 0; f < 4; ++f) {
      aV[0][f] = MFMA(xb[0], cv[f], aV[0][f]); PIN(aV[0][f]);
      u32x2 v = pack4(aV[1][f]); Vb[f][2] = v[0]; Vb[f][3] = v[1]; PIN(Vb[f]); SCHED0();
    }
    { u32x2 v = pack4(aV[1][4]); Vb[4][2] = v[0]; Vb[4][3] = v[1]; PIN(Vb[4]); }
    if (wave < 3) {
#pragma unroll
      for (int f = 0; f < 4; ++f) WR2(exr, (u32x2{K[2][f][0], K[2][f][1]}), (u32x2{Vb[f][2], Vb[f][3]}), 10 + f, 15 + f);
      WR2(exr, ks[1], (u32x2{Vb[4][2], Vb[4][3]}), 14, 19);
    }
    SCHED0();
#pragma unroll
    for (int h = 0; h < 4; ++h) {
      K[1][h] = cat(pack4(aK[0][h]), ks[0]); PIN(K[1][h]);
      u32x2 qt = pack4(aQ[1][h]);
      unsigned e = ((h < 2) == g0) ? qs[1][h & 1] : 0u;
      Q[1][h] = (h & 1) ? frag{qt[0], qt[1], 0u, e} : frag{qt[0], qt[1], e, 0u}; PIN(Q[1][h]);
    }
    SCHED0();
    GAS u64* myflag = (GAS u64*)(ws + FLAG_OFF + ((grp >> 2) * 64 + tile * 4 + cw) * FS1 + (grp & 3) * FS2);
    PINS(myflag);
    TAG_SCALAR();
    WAIT_LGKM0();
    STAMP(23);
    BARRIER();
    STAMP(24);
    SCHED0();
    u32x2 gz;
    asm volatile("ds_read2_b32 %0, %1 offset0:0 offset1:16" : "=v"(gz) : "v"(exgr) : "memory");
#pragma unroll
    for (int h = 0; h < 4; ++h) asm volatile("ds_read2st64_b64 %0, %1 offset0:%2 offset1:4" : "=v"(K[0][h]) : "v"(exr), "i"(h) : "memory");
    f32x4 sc[2][4], sp[2][4];
#pragma unroll
    for (int h = 0; h < 4; ++h) {
      sc[1][h] = MFMA(K[2][h], Q[1][h], maskC); PIN(sc[1][h]);
      sp[1][h] = MFMA(K[1][h], Q[1][h], maskP); PIN(sp[1][h]);
      u32x2 qt = pack4(aQ[0][h]);
      unsigned e = ((h < 2) == g0) ? qs[0][h & 1] : 0u;
      Q[0][h] = (h & 1) ? frag{qt[0], qt[1], 0u, e} : frag{qt[0], qt[1], e, 0u}; PIN(Q[0][h]);
      SCHED0();
    }
    asm volatile("s_waitcnt lgkmcnt(0)" : "+v"(gz), "+v"(K[0][0]), "+v"(K[0][1]), "+v"(K[0][2]), "+v"(K[0][3])::"memory");
    float v[2][4][2];
#pragma unroll
    for (int h = 0; h < 4; ++h) {
      sc[0][h] = MFMA(K[1][h], Q[0][h], maskC); PIN(sc[0][h]);
      sp[0][h] = MFMA(K[0][h], Q[0][h], maskP0); PIN(sp[0][h]);
      v[1][h][0] = fmx(fmx3(sp[1][h][0], sp[1][h][1], sc[1][h][0]), sc[1][h][1]); PIN(v[1][h][0]);
      v[1][h][1] = fmx(fmx3(sp[1][h][2], sp[1][h][3], sc[1][h][2]), sc[1][h][3]); PIN(v[1][h][1]);
      SCHED0();
    }
#pragma unroll
    for (int f = 0; f < 5; ++f) {
      u32x2 a = pack4(aV[0][f]); Vb[f][0] = a[0]; Vb[f][1] = a[1]; PIN(Vb[f]);
      Va[f] = __builtin_shufflevector(Vb[f], Vb[f], -1, -1, 0, 1); PIN(Va[f]);
    }
#pragma unroll
    for (int f = 0; f < 5; ++f) { u32x2 n = *(volatile LDSP u32x2*)(size_t)(exr + (5 + f) * 512); Va[f][0] = n[0]; Va[f][1] = n[1]; }
    u32x4 pk[2];
    auto softmax = [&](float (&w)[4][2], float gate_logit) {
      swap32(w[0][0], w[2][0]); swap32(w[1][0], w[3][0]); swap32(w[0][1], w[2][1]); swap32(w[1][1], w[3][1]);
      swap16(w[0][0], w[1][0]); swap16(w[2][0], w[3][0]); swap16(w[0][1], w[1][1]); swap16(w[2][1], w[3][1]);
      const float cs = 0.23570226039551584f * 1.4426950408889634f;
      float m = fmx(fmx3(fmx3(w[0][0], w[1][0], w[2][0]), w[3][0], w[0][1]), fmx3(w[1][1], w[2][1], w[3][1]));
      f32x2 e[4];
#pragma unroll
      for (int i = 0; i < 4; ++i) {
        f32x2 a = f32x2{w[i][0], w[i][1]} - m;
        PIN(a);
        a *= cs;
        e[i] = f32x2{__builtin_amdgcn_exp2f(a[0]), __builtin_amdgcn_exp2f(a[1])};
      }
      f32x2 s2 = (e[0] + e[1]) + (e[2] + e[3]);
      float s = s2[0] + s2[1];
      float r = __builtin_amdgcn_rcpf(__builtin_fmaf(s, __builtin_amdgcn_exp2f(gate_logit * sg), s));
      float pf[4];
#pragma unroll
      for (int i = 0; i < 4; ++i) { f32x2 pp = e[i] * r; pf[i] = __uint_as_float(cvtpk(pp[0], pp[1])); }
      swap32(pf[0], pf[2]); swap32(pf[1], pf[3]);
      swap16(pf[0], pf[1]); swap16(pf[2], pf[3]);
      return u32x4{__float_as_uint(pf[0]), __float_as_uint(pf[1]), __float_as_uint(pf[2]), __float_as_uint(pf[3])};
    };
    SCHED0();
#pragma unroll
    for (int h = 0; h < 4; ++h) {
      v[0][h][0] = fmx(fmx3(sp[0][h][0], sp[0][h][1], sc[0][h][0]), sc[0][h][1]);
      v[0][h][1] = fmx(fmx3(sp[0][h][2], sp[0][h][3], sc[0][h][2]), sc[0][h][3]);
    }
    pk[1] = softmax(v[1], __uint_as_float(gz[1]));
#pragma unroll
    for (int h = 0; h < 4; ++h) { PIN(v[0][h][0]); PIN(v[0][h][1]); }
    SCHED0();
    pv_store(pk[1], Vb, sel, vmain, vleft, ors, osoff1, oloff1, g0);
    pk[0] = softmax(v[0], __uint_as_float(gz[0]));
    SCHED0();
    pv_store(pk[0], Va, sel, vmain, vleft, ors, osoff, oloff, g0);
    STAMP(25);
    WAIT_VM0();
    if (lane == 0) __hip_atomic_store(myflag, tag, RLX_AGENT);
    STAMP(26);
    RSTAMP(61);
    asm volatile(".Lfk_tail:");
  }
  BARRIER();
  if (!act2) {
    if (bid == 255) {
      if (wave < 4) {
        const int b = wave * 64 + lane;
        GAS u64* fp = (GAS u64*)(ws + FLAG_OFF + (((b & 15) >> 2) * 64 + (b >> 4) * 4) * FS1 + (b & 3) * FS2);
#pragma clang loop unroll(disable)
        for (unsigned spins = 0; spins < (1u << 17); ++spins) {
          u64 v = __hip_atomic_load(fp, RLX_AGENT);
          if (__all(v == tag)) break;
          __builtin_amdgcn_s_sleep(8);
        }
      }
      BARRIER();
      if (tid == 0) __hip_atomic_store((GAS u64*)(ws + EPOCH_OFF), tag, RLX_AGENT);
    }
    return;
  }
  if (cw == 3) {
#pragma unroll
    for (int j = 0; j < 4; ++j) { wo_piece(3, 7, j); wo_piece(3, 8, j); }
  }
  if (wave < 4) phase2<2>(L, ws, ors, tag, rt * 5 + 3, 48, cw, rot2, lane STAMP_PASS);
  else phase2<3>(L, ws, ors, tag, rt * 5, 0, cw, rot2, lane STAMP_PASS);
  STAMP(30);
  if (wave < 5 && rt * 5 + wave < 128) {
    f32x4 s[2][2];
#pragma unroll
    for (int it = 0; it < 2; ++it) {
      const int r = it * 8 + vl;
      const unsigned a0 = L + (wave * 16 + r) * 256 + ((((lane & 7) * 2) ^ r) * 16);
#pragma unroll
      for (int w = 0; w < 4; ++w) {
        f32x4 u0 = *(LDSP f32x4*)(size_t)(a0 + w * 36864), u1 = *(LDSP f32x4*)(size_t)((a0 ^ 16) + w * 36864);
        s[it][0] = w ? s[it][0] + u0 : u0; s[it][1] = w ? s[it][1] + u1 : u1;
      }
    }
#pragma unroll
    for (int it = 0; it < 2; ++it) {
      u32x4 o = {cvtpk(s[it][0][0], s[it][0][1]), cvtpk(s[it][0][2], s[it][0][3]), cvtpk(s[it][1][0], s[it][1][1]), cvtpk(s[it][1][2], s[it][1][3])};
      *(GAS u32x4*)((GAS char*)out + (size_t)(rt * 80 + wave * 16 + it * 8 + vl) * XROW + ct * 128 + (lane & 7) * 16) = o;
    }
  }
  STAMP(31);
  RSTAMP(63);
  STAMP_OUT();
}

#undef x
#undef wA
#undef wB
#undef woA
#undef woB
#undef gw
#undef ws
#undef out
#undef ors

extern "C" void kernel_launch(void* const* d_in, const int* in_sizes, int n_in, void* d_out, int out_size, void* d_ws, size_t ws_size,
                              hipStream_t stream) {
  fused_kernel<<<256, 512, 0, stream>>>((const u16*)d_in[0], (const u16*)d_in[1], (const u16*)d_in[4], (const u16*)d_in[2], (const u16*)d_in[5],
                                        (const u16*)d_in[6], (char*)d_ws, (u16*)d_out);
}
```

```cpp
#include <hip/hip_runtime.h>
#include <hip/hip_bf16.h>
#include <stdint.h>
#include <type_traits>

typedef unsigned short u16;
typedef unsigned long long u64;
typedef short s16x4 __attribute__((ext_vector_type(4)));
typedef float f32x2 __attribute__((ext_vector_type(2)));
typedef float f32x4 __attribute__((ext_vector_type(4)));
typedef unsigned u32x2 __attribute__((ext_vector_type(2)));
typedef unsigned u32x4 __attribute__((ext_vector_type(4)));
typedef u32x4 frag;
typedef __bf16 bf16x2 __attribute__((ext_vector_type(2)));
typedef __bf16 bfx8 __attribute__((ext_vector_type(8)));
#define LDSP __attribute__((address_space(3)))
#define GAS __attribute__((address_space(1)))
#define DI __device__ __forceinline__

constexpr int WROW = 3456;
constexpr int XROW = 1152;
constexpr int PITCH = 432;
constexpr int KO = 32 * PITCH;
constexpr int STG_W = 64 * PITCH;
constexpr int STG = STG_W + 144 * 128;
constexpr int G_OFF = 3 * STG;
constexpr int SCR_OFF = G_OFF + 4608;
constexpr int EX_OFF = 143360;
constexpr int LDS_BYTES = 163840;
constexpr int O_BYTES = 128 * 36 * 1024;
constexpr int FLAG_OFF = 8 << 20;
constexpr int EPOCH_OFF = FLAG_OFF + (1 << 20);
#define ROT 11
#define LPRIO 2
#define PRE0 5
#define NBX 22
#define FS1 128
#define FS2 8
#define PSLEEP 1
#define CODEPF 22656
#define NB1 5

#define MFMA(a, b, c) __builtin_amdgcn_mfma_f32_16x16x32_bf16(__builtin_bit_cast(bfx8, (a)), __builtin_bit_cast(bfx8, (b)), (c), 0, 0, 0)
#define RLX_AGENT __ATOMIC_RELAXED, __HIP_MEMORY_SCOPE_AGENT
#define HOFF(h) (36 * (h) + ((h) & 1) * 4)

DI void glds16(unsigned voff, const GAS void* sbase, unsigned lds_addr) {
  asm volatile("s_mov_b32 m0, %2\n\ts_nop 0\n\tglobal_load_lds_dwordx4 %0, %1" ::"v"(voff), "s"(sbase), "s"(lds_addr) : "memory");
}
DI u32x2 trd(unsigned a) { return __builtin_bit_cast(u32x2, __builtin_amdgcn_ds_read_tr16_b64_v4i16((LDSP s16x4*)(size_t)a)); }
DI frag ldW(unsigned a0, unsigned a1) {
  u32x2 lo = trd(a0), hi = trd(a1);
  return __builtin_shufflevector(lo, hi, 0, 1, 2, 3);
}
DI frag ld128(unsigned a) { return *(LDSP frag*)(size_t)a; }
DI u32x2 ld64(unsigned a) { return *(LDSP u32x2*)(size_t)a; }
DI void st64(unsigned a, u32x2 v) { *(LDSP u32x2*)(size_t)a = v; }
DI unsigned cvtpk(float a, float b) {
  f32x2 f = {a, b};
  return __builtin_bit_cast(unsigned, __builtin_convertvector(f, bf16x2));
}
DI u32x2 pack4(f32x4 a) { return u32x2{cvtpk(a[0], a[1]), cvtpk(a[2], a[3])}; }
DI frag cat(u32x2 a, u32x2 b) { return __builtin_shufflevector(a, b, 0, 1, 2, 3); }
DI void swap32(float& a, float& b) {
  auto r = __builtin_amdgcn_permlane32_swap(__float_as_uint(a), __float_as_uint(b), false, false);
  a = __uint_as_float(r[0]); b = __uint_as_float(r[1]);
}
DI void swap16(float& a, float& b) {
  auto r = __builtin_amdgcn_permlane16_swap(__float_as_uint(a), __float_as_uint(b), false, false);
  a = __uint_as_float(r[0]); b = __uint_as_float(r[1]);
}
DI float fmx(float a, float b) { return __builtin_elementwise_maximum(a, b); }
DI float fmx3(float a, float b, float c) { return fmx(fmx(a, b), c); }
#define STAMP(i)
#define RSTAMP(i)
#define STAMP_OUT()
#define STAMP_ARG
#define STAMP_PASS
#define STAMP_DECL
#define SGB(m, n) __builtin_amdgcn_sched_group_barrier(m, n, 0)
#define SCHED0() __builtin_amdgcn_sched_barrier(0)
#define WAIT_LGKM0() do { __builtin_amdgcn_s_waitcnt(0xC07F); asm volatile("" ::: "memory"); } while (0)
#define WAIT_VM0() asm volatile("s_waitcnt vmcnt(0)" ::: "memory")
#define BARRIER() __builtin_amdgcn_s_barrier()
#define PIN(x) asm volatile("" : "+v"(x))
#define TAG_SCALAR() do { unsigned lo_ = __builtin_amdgcn_readfirstlane((unsigned)tag), hi_ = __builtin_amdgcn_readfirstlane((unsigned)(tag >> 32)); \
    tag = (((u64)hi_ << 32) | lo_) + 1; asm volatile("" : "+s"(tag)); } while (0)
#define PINS(x) asm volatile("" : "+s"(x))
#define WR2(addr, a, b, o0, o1) asm volatile("ds_write2st64_b64 %0, %1, %2 offset0:%3 offset1:%4" ::"v"(addr), "v"(a), "v"(b), "i"(o0), "i"(o1) : "memory")

DI void pv_store(u32x4 pk, const frag (&V)[5], u32x4 sel, unsigned vmain, unsigned vleft, __amdgpu_buffer_rsrc_t ors, unsigned so, unsigned sl, bool g0) {
  const f32x4 z = {0.f, 0.f, 0.f, 0.f};
  frag P[4];
  f32x4 o[4], os[4];
  auto mkP = [&](int h) {
    unsigned u = pk[h];
    P[h] = frag{__builtin_amdgcn_perm(u, u, sel[0]), __builtin_amdgcn_perm(u, u, sel[1]), __builtin_amdgcn_perm(u, u, sel[2]), __builtin_amdgcn_perm(u, u, sel[3])};
    PIN(P[h]);
  };
  mkP(0); mkP(1);
  SCHED0();
  u32x4 f0, f1;
  os[0] = MFMA(V[4], P[0], z); PIN(os[0]); o[0] = MFMA(V[0], P[0], z); PIN(o[0]); mkP(2); SCHED0();
  os[1] = MFMA(V[4], P[1], z); PIN(os[1]); o[1] = MFMA(V[1], P[1], z); PIN(o[1]); mkP(3); SCHED0();
  os[2] = MFMA(V[4], P[2], z); PIN(os[2]); os[3] = MFMA(V[4], P[3], z); PIN(os[3]);
  o[2] = MFMA(V[2], P[2], z); PIN(o[2]); o[3] = MFMA(V[3], P[3], z); PIN(o[3]); SCHED0();
  f0 = u32x4{cvtpk(o[0][0], o[0][1]), cvtpk(o[0][2], o[0][3]), cvtpk(o[1][0], o[1][1]), cvtpk(o[1][2], o[1][3])};
  PIN(f0); SCHED0();
  __builtin_amdgcn_raw_buffer_store_b128(f0, ors, vmain, so, 16);
  SCHED0();
  u32x2 f2 = {cvtpk(g0 ? os[0][0] : os[2][0], g0 ? os[0][1] : os[2][1]), cvtpk(g0 ? os[1][2] : os[3][2], g0 ? os[1][3] : os[3][3])};
  PIN(f2); SCHED0();
  f1 = u32x4{cvtpk(o[2][0], o[2][1]), cvtpk(o[2][2], o[2][3]), cvtpk(o[3][0], o[3][1]), cvtpk(o[3][2], o[3][3])};
  PIN(f1); PIN(f0); SCHED0();
  __builtin_amdgcn_raw_buffer_store_b128(f1, ors, vmain + 1024, so, 16);
  __builtin_amdgcn_raw_buffer_store_b64(f2, ors, vleft, sl, 16);
  SCHED0();
}

template <int MT>
DI void phase2(unsigned L, GAS char* ws, __amdgpu_buffer_rsrc_t ors, u64 tag, int tt0, int mrow, int kq, int rot2, int lane STAMP_ARG) {
  const int g = lane >> 4, q = (lane >> 2) & 3, p = lane & 3, c = lane & 15;
  const unsigned sw = (2 * g + (q >> 1)) & 3;
  unsigned bw[4];
#pragma unroll
  for (int n = 0; n < 4; ++n) bw[n] = L + kq * 36864 + (4 * g + q) * 128 + 8 * p + ((n ^ sw) * 32);
  frag wf[9][4];
  auto rdw = [&](int i) {
#pragma unroll
    for (int n = 0; n < 4; ++n) wf[i][n] = ldW(bw[n] + i * 4096, bw[n] + i * 4096 + 2048);
  };
  rdw(0); rdw(1); rdw(2); rdw(3); rdw(4);
  SCHED0();
  unsigned so[9], vo[MT];
#pragma unroll
  for (int i = 0; i < 9; ++i) so[i] = (i < 8 ? 8 * kq + ((i + rot2) & 7) : 32 + kq) * 1024;
#pragma unroll
  for (int m = 0; m < MT; ++m) { int tt = tt0 + m; tt = tt > 127 ? 127 : tt; vo[m] = lane * 16 + tt * 36864; }
  int tf = (lane & 4) ? tt0 + MT - 1 : tt0; tf = tf > 127 ? 127 : tf;
  GAS u64* fp = (GAS u64*)(ws + FLAG_OFF + (kq * 64 + (tf >> 1)) * FS1 + (lane & 3) * FS2);
  unsigned rb[4];
#pragma unroll
  for (int n = 0; n < 4; ++n) rb[n] = L + kq * 36864 + (mrow + c) * 256 + (((4 * n + g) ^ c) * 16);
#pragma unroll
  for (int i = 0; i < 9; ++i) PINS(so[i]);
#pragma unroll
  for (int m = 0; m < MT; ++m) PIN(vo[m]);
#pragma unroll
  for (int n = 0; n < 4; ++n) PIN(rb[n]);
  PIN(fp);
  WAIT_LGKM0();
  BARRIER();
  STAMP(27);
#pragma clang loop unroll(disable)
  for (unsigned spins = 0; spins < (1u << 17); ++spins) {
    u64 v = __hip_atomic_load(fp, RLX_AGENT);
    if (__all(v == tag)) break;
    __builtin_amdgcn_s_sleep(PSLEEP);
  }
  __builtin_amdgcn_fence(__ATOMIC_ACQUIRE, "wavefront");
  asm volatile("" ::: "memory");
  STAMP(28);
  RSTAMP(62);
  frag of[9][MT];
  constexpr int WIN = 2;
#pragma unroll
  for (int i = 0; i < WIN; ++i)
#pragma unroll
    for (int m = 0; m < MT; ++m) { of[i][m] = __builtin_amdgcn_raw_buffer_load_b128(ors, vo[m], so[i], 16); SCHED0(); }
  f32x4 acc[MT][4];
#pragma unroll
  for (int i = 0; i < 9; ++i) {
    const bool rd = i >= 2 && i + 3 < 9;
    if (rd) rdw(i + 3);
#pragma unroll
    for (int m = 0; m < MT; ++m) {
#pragma unroll
      for (int n = 0; n < 4; ++n) acc[m][n] = MFMA(wf[i][n], of[i][m], i == 0 ? (f32x4{0, 0, 0, 0}) : acc[m][n]);
      if (i + WIN < 9) of[i + WIN][m] = __builtin_amdgcn_raw_buffer_load_b128(ors, vo[m], so[i + WIN], 16);
    }
#pragma unroll
    for (int m = 0; m < MT; ++m) {
#pragma unroll
      for (int n = 0; n < 4; ++n) { SGB(0x8, 1); if (m < 2 && rd) SGB(0x100, 1); }
      if (i + WIN < 9) SGB(0x20, 1);
    }
    SCHED0();
  }
  STAMP(29);
#pragma unroll
  for (int m = 0; m < MT; ++m)
#pragma unroll
    for (int n = 0; n < 4; ++n) *(LDSP f32x4*)(size_t)(rb[n] + m * 4096) = acc[m][n];
  WAIT_LGKM0();
  BARRIER();
}

typedef u64 u64x8 __attribute__((ext_vector_type(8)));
extern "C" __global__ __launch_bounds__(512, 2) void fused_kernel(const u16* x_, const u16* wA_, const u16* wB_, const u16* woA_, const u16* woB_,
                                                                   const u16* gw_, char* ws_, u16* out_) {
  __shared__ __attribute__((aligned(1024))) char lds[LDS_BYTES];
  const unsigned L = (unsigned)(size_t)(LDSP char*)lds;
  const int BIDX = blockIdx.x, TIDX = threadIdx.x;
  u64x8 ka;
  asm volatile("s_load_dwordx16 %0, %1, 0x0" : "=s"(ka) : "s"(__builtin_amdgcn_kernarg_segment_ptr()));
#define KARG_WAIT() do { SCHED0(); asm volatile("s_waitcnt lgkmcnt(0)" ::: "memory"); SCHED0(); asm volatile("" : "+s"(ka)); } while (0)
#define x ((const GAS u16*)ka[0])
#define wA ((const GAS u16*)ka[1])
#define wB ((const GAS u16*)ka[2])
#define woA ((const GAS u16*)ka[3])
#define woB ((const GAS u16*)ka[4])
#define gw ((const GAS u16*)ka[5])
#define ws ((GAS char*)ka[6])
#define out ((GAS u16*)ka[7])
#define ors __builtin_amdgcn_make_buffer_rsrc((void*)ka[6], 0, O_BYTES, 0x00020000)
  const int bid = BIDX;
  const int xcd = bid & 7, idx = bid >> 3;
  const int tl = idx & 7, gl = idx >> 3;
  const int tile = (xcd & 1) * 8 + tl;
  const int grp = (xcd >> 1) * 4 + gl;
  int rot = ROT == 1 ? tl + 2 * gl : ROT == 2 ? (tl >> 1) * 2 + 2 * gl : ROT == 6 ? (tl % 3) * 3 + (gl % 3) * 3 : ROT == 7 ? (((tl >> 1) + gl) & 3) * 2 + (tl & 1 & (gl >> 1))
          : ROT == 8 ? (((tl + gl) & 3) * 9) >> 2 : ROT == 9 ? ((tl + gl + (xcd >> 1)) % 3) * 3 : ROT == 10 ? (((tl + gl) & 3) * 9 >> 2) + (xcd & 1)
          : ROT == 11 ? ((tl + gl) & 3) * 2 + ((xcd ^ (xcd >> 1)) & 1) : ROT == 12 ? ((tl + gl) % 3) * 3 + ((xcd & 1) + (xcd >> 1)) % 3
          : ROT == 13 ? ((tl + gl) & 1) * 4 + (((xcd & 1) * 2 + (xcd >> 1)) & 3) : ROT == 14 ? (tl + 2 * gl + ((xcd >> 1) & 1)) % 9
          : ROT == 15 ? (((tl + gl) & 3) * 2 + ((xcd ^ (xcd >> 1)) & 1) + (xcd >> 2)) : 0;
  if (rot >= 9) rot -= 9;
  const int tid = TIDX, lane = tid & 63;
  const int wave = __builtin_amdgcn_readfirstlane(tid >> 6);
  const int vl = lane >> 3;
  STAMP_DECL;
  STAMP(0);
  RSTAMP(60);
  if (tid < 7) {
    u64 pc0;
    unsigned len;
    asm volatile("s_getpc_b64 %0\n\ts_mov_b32 %1, .Lfk_tail-fused_kernel+128" : "=s"(pc0), "=s"(len));
    unsigned off = (idx * 7 + tid) * 128;
    off = off < len ? off : 0;
    asm volatile("s_mov_b32 m0, %2\n\ts_nop 0\n\tglobal_load_lds_dword %0, %1" ::"v"(off), "s"(pc0 & ~255ull), "s"(L + SCR_OFF) : "memory");
  }
  u64 tag;
  const int lin0 = xcd * 30 + idx;
  const bool act2 = idx < 30 && lin0 < 234;
  const int lin = act2 ? lin0 : 0;
  const int rt = (lin * 57) >> 9, ct = lin - rt * 9;
  const int rot2 = 3 * (rt - ((rt * 171) >> 9) * 3);
  const float sg = (grp >= 8) ? 1.4426950408889634f : -1.4426950408889634f;
  const unsigned wsch = ((((lane & 7) >> 1) ^ ((vl >> 1) & 3)) << 5) | ((lane & 1) << 4);
  const unsigned offn = vl * XROW + wsch, offl = ((vl >> 2) * 72 + (vl & 3)) * XROW + wsch;
  auto wo_piece = [&](int kq, int i, int j) {
    const GAS char* b = (const GAS char*)(kq >= 2 ? woB : woA) + ct * 128 + (kq & 1) * (288 * XROW);
    if (i == 8) b += ((j & 1) * 144 + ((j >> 1) ? 52 : 16)) * XROW;
    else b += (((i + rot2) & 7) * 36 + (j >> 1) * 20 + 8 * (j & 1)) * XROW;
    glds16(i == 8 ? offl : offn, b, L + (kq * 9 + i) * 4096 + j * 1024);
  };
  const int g = lane >> 4, q = (lane >> 2) & 3, p = lane & 3, c = lane & 15;
  const int cw = wave & 3;

  if (__builtin_expect(wave >= 4, 1)) {
    __builtin_amdgcn_s_setprio(LPRIO);
    const int lw = cw, tl256 = tid - 256;
    const unsigned offx = (tile * 128 + 8 * lw + vl) * XROW + ((lane & 7) ^ ((4 * lw + (vl >> 1)) & 7)) * 16;
    KARG_WAIT();
    const GAS char* w = (const GAS char*)((grp >= 8) ? wB : wA) + (grp & 7) * 144;
    unsigned offw[7], offh;
    auto kchunk = [&](int st) { int kc = st + rot; return kc >= 9 ? kc - 9 : kc; };
    auto issueX = [&](int kc, unsigned sb, int jj) { glds16(offx, (const GAS char*)x + kc * 128 + jj * (32 * XROW), sb + STG_W + (2 + lw + 4 * jj) * 1024); };
    auto issueW = [&](int kc, unsigned sb, int j) { glds16(offw[j], w + kc * (64 * WROW), sb + (lw + 4 * j) * 1024); };
    auto issue6 = [&](int kc, unsigned sb) {
      if (lw < 3) issueW(kc, sb, 6);
      else { glds16(offw[6], (const GAS char*)x + kc * 128, sb + STG_W); glds16(offh, (const GAS char*)x + kc * 128, sb + STG_W + 1024); }
    };
    const int kc0 = rot, kc1 = kchunk(1);
    const unsigned sb0 = L, sb1 = L + STG;
    auto calcw = [&](int j) {
      unsigned qq = 256 * j + tl256;
      unsigned u = (qq * 2428u) >> 16, cc = qq - u * 27;
      unsigned row = (u & 0x38) | ((u & 1) << 2) | ((u >> 1) & 3);
      offw[j] = row * WROW + cc * 16 + ((cc * 57u) >> 9) * 1008;
    };
    issueX(kc0, sb0, 0);
    u32x2 gv;
    const unsigned gi = tl256 < 144 ? tl256 : 143;
    asm volatile("global_load_dwordx2 %0, %1, %2" : "=v"(gv) : "v"(gi * 8), "s"(gw) : "memory");
#pragma unroll
    for (int j = 0; j < 6; ++j) {
      SCHED0();
      calcw(j);
      SCHED0();
      issueW(kc0, sb0, j);
      if (j < 3) issueX(kc0, sb0, j + 1);
    }
    calcw(6);
    {
      int t0 = tile * 128 - 16 + vl, t1 = t0 + 8;
      t0 = t0 < 0 ? 0 : t0; t1 = t1 < 0 ? 0 : t1;
      unsigned h0 = t0 * XROW + ((lane & 7) ^ (vl >> 1)) * 16;
      offh = t1 * XROW + ((lane & 7) ^ (4 + (vl >> 1))) * 16;
      offw[6] = lw == 3 ? h0 : offw[6];
    }
    issue6(kc0, sb0);
    STAMP(1);
    issueW(kc1, sb1, 0);
    int slot = 0, kn = kc1;
    const GAS char* xp = (const GAS char*)x + kn * 128;
    unsigned b1 = sb1, xd = sb1 + STG_W + (2 + lw) * 1024;
    PINS(xp); PINS(xd);
    SCHED0();
#pragma unroll
    for (int j = 1; j < PRE0; ++j) issueW(kc1, sb1, j);
    STAMP(2);
    asm volatile("s_waitcnt vmcnt(%1)" : "+v"(gv) : "i"(10 + PRE0) : "memory");
    {
      unsigned a0 = __builtin_amdgcn_perm(gv[0], gv[0], 0x01000100), a1 = __builtin_amdgcn_perm(gv[0], gv[0], 0x03020302);
      unsigned a2 = __builtin_amdgcn_perm(gv[1], gv[1], 0x01000100), a3 = __builtin_amdgcn_perm(gv[1], gv[1], 0x03020302);
      *(LDSP u32x4*)(size_t)(L + G_OFF + gi * 32) = u32x4{a0, a0, a1, a1};
      *(LDSP u32x4*)(size_t)(L + G_OFF + gi * 32 + 16) = u32x4{a2, a2, a3, a3};
    }
    WAIT_LGKM0();
    asm volatile("s_waitcnt vmcnt(%0)" ::"i"(PRE0) : "memory");
#pragma clang loop unroll(disable)
    for (int st = 0; st < 7; ++st) {
      BARRIER();
      glds16(offx, xp, xd);
      SCHED0();
      STAMP(3 + st);
      glds16(offx, xp + 32 * XROW, xd + 4096);
      if (st == 0) {
#pragma unroll
        for (int j = PRE0; j < 6; ++j) issueW(kc1, sb1, j);
        tag = __hip_atomic_load((GAS u64*)(ws + EPOCH_OFF), RLX_AGENT);
      }
      issue6(kn, b1);
      glds16(offx, xp + 64 * XROW, xd + 8192);
      glds16(offx, xp + 96 * XROW, xd + 12288);
      SCHED0();
      kn = kn == 8 ? 0 : kn + 1;
      b1 = L + (slot == 0 ? 2 : slot - 1) * STG;
      issueW(kn, b1, 0); issueW(kn, b1, 1);
#pragma unroll
      for (int j = 2; j < 6; ++j) issueW(kn, b1, j);
      slot = slot == 2 ? 0 : slot + 1;
      xp = (const GAS char*)x + kn * 128;
      xd = b1 + STG_W + (2 + lw) * 1024;
      PINS(xp); PINS(xd);
      asm volatile("s_waitcnt vmcnt(6)" ::: "memory");
    }
    BARRIER();
    glds16(offx, xp, xd);
    SCHED0();
    STAMP(10);
    glds16(offx, xp + 32 * XROW, xd + 4096);
    issue6(kn, b1);
    glds16(offx, xp + 64 * XROW, xd + 8192);
    glds16(offx, xp + 96 * XROW, xd + 12288);
#pragma unroll
    for (int I = 0; I < 6; ++I) wo_piece(0, I, lw);
    asm volatile("s_waitcnt vmcnt(6)" ::: "memory");
    BARRIER();
    STAMP(11);
    wo_piece(0, 6, lw); wo_piece(0, 7, lw);
    __builtin_amdgcn_s_setprio(0);
#pragma unroll
    for (int I = 8; I < NBX; ++I) wo_piece(I / 9, I % 9, lw);
    WAIT_LGKM0();
    STAMP(12);
    BARRIER();
    STAMP(13);
#pragma unroll
    for (int I = NBX; I < 34; ++I) wo_piece(I / 9, I % 9, lw);
    WAIT_VM0();
    TAG_SCALAR();
    STAMP(14);
  } else {
    const unsigned wrow = L + (8 * g + 2 * q) * PITCH;
    f32x4 maskP, maskC, maskP0;
    {
      const float ninf = -__builtin_inff();
#pragma unroll
      for (int r = 0; r < 4; ++r) {
        bool pm = (r & 1) == (c & 1);
        maskC[r] = (pm && 4 * g + r <= c) ? 0.f : ninf;
        maskP[r] = (pm && 4 * g + r > c) ? 0.f : ninf;
      }
      maskP0 = maskP + ((tile == 0 && wave == 0) ? -998244352.f * 4.242640687119285f : 0.f);
      asm volatile("" : "+v"(maskP), "+v"(maskC), "+v"(maskP0));
    }
    u32x4 sel;
    {
      const int par = c & 1;
      const bool cur1 = 4 * g + par <= c, cur2 = 4 * g + 2 + par <= c;
      const unsigned s1 = par ? 0x01000c0cu : 0x0c0c0100u;
      const unsigned s2 = par ? 0x03020c0cu : 0x0c0c0302u;
      sel[0] = cur1 ? 0x0c0c0c0cu : s1;
      sel[1] = cur2 ? 0x0c0c0c0cu : s2;
      sel[2] = cur1 ? s1 : 0x0c0c0c0cu;
      sel[3] = cur2 ? s2 : 0x0c0c0c0cu;
    }
    unsigned vmain = lane * 16, vleft = lane < 32 ? c * 16 + g * 8 : 0x40000000u;
    unsigned osoff = (tile * 8 + 2 * wave) * 36864 + grp * 2048, oloff = (tile * 8 + 2 * wave) * 36864 + 32768 + grp * 256;
    unsigned osoff1 = osoff + 36864, oloff1 = oloff + 36864;
    PINS(osoff); PINS(oloff); PINS(osoff1); PINS(oloff1);
    asm volatile("" : "+v"(sel), "+v"(vmain), "+v"(vleft));
    f32x4 aK[2][4], aQ[2][4], aKQ[2], aV[2][5], hk, hv, he;
    frag cv[5], xb[3];
    unsigned exr = L + EX_OFF + wave * 5120 + lane * 8, exh = L + EX_OFF + wave * 512 + lane * 8;
    unsigned exgw = L + SCR_OFF + wave * 128 + g * 16, exgr = L + SCR_OFF + wave * 128 + c * 4;
    PIN(exr); PIN(exh); PIN(exgw); PIN(exgr);
    auto mainloop = [&](auto WC) {
      constexpr int W = decltype(WC)::value;
      constexpr int XO = (1 + 2 * W) * 2048;
    hk = f32x4{0, 0, 0, 0}; hv = f32x4{0, 0, 0, 0}; he = f32x4{0, 0, 0, 0};
    PIN(he);
#pragma unroll
    for (int t = 0; t < 2; ++t) {
#pragma unroll
      for (int f = 0; f < 4; ++f) { aK[t][f] = f32x4{0, 0, 0, 0}; aQ[t][f] = f32x4{0, 0, 0, 0}; }
      aKQ[t] = f32x4{0, 0, 0, 0};
#pragma unroll
      for (int f = 0; f < 5; ++f) aV[t][f] = f32x4{0, 0, 0, 0};
    }
    unsigned cW = wrow + 8 * p;
    unsigned cKQ = wrow + (p < 2 ? 144 : 0) + ((p & 1) ? 104 : 32);
    unsigned cX0 = L + STG_W + c * 128 + ((g ^ (c >> 1)) * 16), cX1 = cX0 ^ 64;
    unsigned cV[4];
#pragma unroll
    for (int i = 0; i < 4; ++i)
      cV[i] = p < 2 ? wrow + 288 + (p ? 104 : 32) + (i >> 1) * KO + (i & 1) * PITCH : L + G_OFF + rot * 512 + (8 * g + q) * 8 + (i >> 1) * 256 + (i & 1) * 32;
#pragma unroll
    for (int f = 0; f < 5; ++f) cv[f] = frag{0, 0, 0, 0};
    xb[0] = frag{0, 0, 0, 0}; xb[1] = frag{0, 0, 0, 0}; xb[2] = frag{0, 0, 0, 0};
    asm volatile("" : "+v"(aK[0][0]), "+v"(aK[0][1]), "+v"(aK[0][2]), "+v"(aK[0][3]), "+v"(aK[1][0]), "+v"(aK[1][1]), "+v"(aK[1][2]), "+v"(aK[1][3]),
                      "+v"(aQ[0][0]), "+v"(aQ[0][1]), "+v"(aQ[0][2]), "+v"(aQ[0][3]), "+v"(aQ[1][0]), "+v"(aQ[1][1]), "+v"(aQ[1][2]), "+v"(aQ[1][3]),
                      "+v"(aKQ[0]), "+v"(aKQ[1]), "+v"(hk), "+v"(hv), "+v"(xb[0]), "+v"(xb[1]), "+v"(xb[2]));
    asm volatile("" : "+v"(aV[0][0]), "+v"(aV[0][1]), "+v"(aV[0][2]), "+v"(aV[0][3]), "+v"(aV[0][4]), "+v"(aV[1][0]), "+v"(aV[1][1]), "+v"(aV[1][2]),
                      "+v"(aV[1][3]), "+v"(aV[1][4]), "+v"(cv[0]), "+v"(cv[1]), "+v"(cv[2]), "+v"(cv[3]), "+v"(cv[4]),
                      "+v"(cW), "+v"(cKQ), "+v"(cX0), "+v"(cX1), "+v"(cV[0]), "+v"(cV[1]), "+v"(cV[2]), "+v"(cV[3]));
    SCHED0();
    STAMP(1);
    KARG_WAIT();
    tag = __hip_atomic_load((GAS u64*)(ws + EPOCH_OFF), RLX_AGENT);
    STAMP(2);
    WAIT_LGKM0();
      int slot = 0, kc = rot;
#pragma clang loop unroll(disable)
      for (int st = 0; st < 9; ++st) {
        BARRIER();
        SCHED0();
        frag xa[3], W0[14], W1[9];
        xa[0] = ld128(cX0 + XO); xa[1] = ld128(cX0 + XO + 2048);
#pragma unroll
        for (int f = 0; f < 4; ++f) W0[f ^ 1] = ldW(cW + 144 + HOFF(f ^ 1), cW + 144 + HOFF(f ^ 1) + PITCH);
        xa[2] = ld128(cX0);
#pragma unroll
        for (int f = 0; f < 5; ++f) { aV[0][f] = MFMA(xb[0], cv[f], aV[0][f]); aV[1][f] = MFMA(xb[1], cv[f], aV[1][f]); }
        hv = MFMA(xb[2], cv[W], hv);
        if (W == 1) he = MFMA(xb[2], cv[4], he);
        W0[5] = ldW(cW + HOFF(0), cW + HOFF(0) + PITCH);
        W0[4] = ldW(cKQ, cKQ + PITCH);
        W0[7] = ldW(cW + HOFF(2), cW + HOFF(2) + PITCH);
        W0[6] = ldW(cW + HOFF(1), cW + HOFF(1) + PITCH);
        W0[9] = ldW(cW + 288 + HOFF(0), cW + 288 + HOFF(0) + PITCH);
        W0[8] = ldW(cW + HOFF(3), cW + HOFF(3) + PITCH);
        W0[11] = ldW(cW + 288 + HOFF(2), cW + 288 + HOFF(2) + PITCH);
        W0[10] = ldW(cW + 288 + HOFF(1), cW + 288 + HOFF(1) + PITCH);
        W0[13] = ldW(cV[0], cV[1]);
        W0[12] = ldW(cW + 288 + HOFF(3), cW + 288 + HOFF(3) + PITCH);
        xb[0] = ld128(cX1 + XO); xb[1] = ld128(cX1 + XO + 2048); xb[2] = ld128(cX1);
#pragma unroll
        for (int f = 0; f < 4; ++f) W1[f ^ 1] = ldW(cW + KO + 144 + HOFF(f ^ 1), cW + KO + 144 + HOFF(f ^ 1) + PITCH);
        W1[5] = ldW(cW + KO + HOFF(0), cW + KO + HOFF(0) + PITCH);
        W1[4] = ldW(cKQ + KO, cKQ + KO + PITCH);
        W1[8] = ldW(cW + KO + HOFF(3), cW + KO + HOFF(3) + PITCH);
        W1[7] = ldW(cW + KO + HOFF(2), cW + KO + HOFF(2) + PITCH);
        W1[6] = ldW(cW + KO + HOFF(1), cW + KO + HOFF(1) + PITCH);
#pragma unroll
        for (int f = 0; f < 4; ++f) cv[f] = ldW(cW + KO + 288 + HOFF(f), cW + KO + 288 + HOFF(f) + PITCH);
        cv[4] = ldW(cV[2], cV[3]);
#pragma unroll
        for (int f = 0; f < 4; ++f) { aK[0][f] = MFMA(W0[f], xa[0], aK[0][f]); aK[1][f] = MFMA(W0[f], xa[1], aK[1][f]); }
        hk = MFMA(W0[W], xa[2], hk);
        aKQ[0] = MFMA(W0[4], xa[0], aKQ[0]); aKQ[1] = MFMA(W0[4], xa[1], aKQ[1]);
        if (W == 0) he = MFMA(W0[4], xa[2], he);
#pragma unroll
        for (int f = 0; f < 4; ++f) { aQ[0][f] = MFMA(W0[5 + f], xa[0], aQ[0][f]); aQ[1][f] = MFMA(W0[5 + f], xa[1], aQ[1][f]); }
#pragma unroll
        for (int f = 0; f < 5; ++f) { aV[0][f] = MFMA(xa[0], W0[9 + f], aV[0][f]); aV[1][f] = MFMA(xa[1], W0[9 + f], aV[1][f]); }
        hv = MFMA(xa[2], W0[9 + W], hv);
        if (W == 1) he = MFMA(xa[2], W0[13], he);
#pragma unroll
        for (int f = 0; f < 4; ++f) { aK[0][f] = MFMA(W1[f], xb[0], aK[0][f]); aK[1][f] = MFMA(W1[f], xb[1], aK[1][f]); }
        hk = MFMA(W1[W], xb[2], hk);
        aKQ[0] = MFMA(W1[4], xb[0], aKQ[0]); aKQ[1] = MFMA(W1[4], xb[1], aKQ[1]);
        if (W == 0) he = MFMA(W1[4], xb[2], he);
#pragma unroll
        for (int f = 0; f < 4; ++f) { aQ[0][f] = MFMA(W1[5 + f], xb[0], aQ[0][f]); aQ[1][f] = MFMA(W1[5 + f], xb[1], aQ[1][f]); }
        const int ds = slot == 2 ? -2 * STG : STG, dg = kc == 8 ? -8 * 512 : 512;
        slot = slot == 2 ? 0 : slot + 1;
        kc = kc == 8 ? 0 : kc + 1;
        const int dv = p < 2 ? ds : dg;
        cW += ds; cKQ += ds; cX0 += ds; cX1 += ds;
#pragma unroll
        for (int i = 0; i < 4; ++i) cV[i] += dv;
        SGB(0x100, 2);
#pragma unroll
        for (int i = 0; i < 54; ++i) {
          SGB(0x8, 1);
          if (i < 3 || i == 16 || i == 28 || i == 40) SGB(0x100, 2); else SGB(0x100, 1);
          if (i >= 3 && i < 11) SGB(0x4, 1);
          if (i >= 51) SGB(0x2, 1);
        }
#pragma unroll
        for (int i = 0; i < 4; ++i) { SGB(0x8, 1); SGB(0x2, 2); }
        SGB(0x8, W < 2 ? 4 : 2);
        SCHED0();
        WAIT_LGKM0();
      }
      hv = MFMA(xb[2], cv[W], hv);
      if (W == 1) he = MFMA(xb[2], cv[4], he);
      if (W < 2) {
        u32x2 v = pack4(he);
        if (W == 0 && tile == 0) v = u32x2{0u, 0u};
        st64(L + EX_OFF + lane * 8 + (W == 0 ? 4 : 9) * 512, v);
      }
    };
    if (wave == 0) mainloop(std::integral_constant<int, 0>{});
    else if (wave == 1) mainloop(std::integral_constant<int, 1>{});
    else if (wave == 2) mainloop(std::integral_constant<int, 2>{});
    else mainloop(std::integral_constant<int, 3>{});
    WAIT_VM0();
    STAMP(12);
    const bool g0 = g == 0;
    frag K[3][4], Q[2][4], Vb[5], Va[5];
    u32x2 ks[2], qs[2];
    aV[1][4] = MFMA(xb[1], cv[4], aV[1][4]); PIN(aV[1][4]);
    aV[0][4] = MFMA(xb[0], cv[4], aV[0][4]); PIN(aV[0][4]);
#pragma unroll
    for (int t = 1; t >= 0; --t) {
      u32x2 kq = pack4(aKQ[t]);
      auto r0 = __builtin_amdgcn_permlane32_swap(kq[0], 0u, false, false);
      auto r1 = __builtin_amdgcn_permlane32_swap(kq[1], 0u, false, false);
      ks[t] = u32x2{r0[0], r1[0]}; qs[t] = u32x2{r0[1], r1[1]};
      PIN(ks[t]); PIN(qs[t]);
    }
    SCHED0();
#pragma unroll
    for (int f = 0; f < 4; ++f) { aV[1][f] = MFMA(xb[1], cv[f], aV[1][f]); PIN(aV[1][f]); K[2][f] = cat(pack4(aK[1][f]), ks[1]); PIN(K[2][f]); SCHED0(); }
    {
      u32x2 h = pack4(hk), hh = pack4(hv);
      if (tile == 0) h = u32x2{0u, 0u};
      WR2(exh, h, hh, 0, 5);
    }
    if (c >= 8) { *(LDSP f32x4*)(size_t)(exgw) = aV[0][4]; *(LDSP f32x4*)(size_t)(exgw + 64) = aV[1][4]; }
    SCHED0();
#pragma unroll
    for (int f = 0; f < 4; ++f) {
      aV[0][f] = MFMA(xb[0], cv[f], aV[0][f]); PIN(aV[0][f]);
      u32x2 v = pack4(aV[1][f]); Vb[f][2] = v[0]; Vb[f][3] = v[1]; PIN(Vb[f]); SCHED0();
    }
    { u32x2 v = pack4(aV[1][4]); Vb[4][2] = v[0]; Vb[4][3] = v[1]; PIN(Vb[4]); }
    if (wave < 3) {
#pragma unroll
      for (int f = 0; f < 4; ++f) WR2(exr, (u32x2{K[2][f][0], K[2][f][1]}), (u32x2{Vb[f][2], Vb[f][3]}), 10 + f, 15 + f);
      WR2(exr, ks[1], (u32x2{Vb[4][2], Vb[4][3]}), 14, 19);
    }
    SCHED0();
#pragma unroll
    for (int h = 0; h < 4; ++h) {
      K[1][h] = cat(pack4(aK[0][h]), ks[0]); PIN(K[1][h]);
      u32x2 qt = pack4(aQ[1][h]);
      unsigned e = ((h < 2) == g0) ? qs[1][h & 1] : 0u;
      Q[1][h] = (h & 1) ? frag{qt[0], qt[1], 0u, e} : frag{qt[0], qt[1], e, 0u}; PIN(Q[1][h]);
    }
    SCHED0();
    GAS u64* myflag = (GAS u64*)(ws + FLAG_OFF + ((grp >> 2) * 64 + tile * 4 + cw) * FS1 + (grp & 3) * FS2);
    PINS(myflag);
    TAG_SCALAR();
    WAIT_LGKM0();
    STAMP(23);
    BARRIER();
    STAMP(24);
    SCHED0();
    u32x2 gz;
    asm volatile("ds_read2_b32 %0, %1 offset0:0 offset1:16" : "=v"(gz) : "v"(exgr) : "memory");
#pragma unroll
    for (int h = 0; h < 4; ++h) asm volatile("ds_read2st64_b64 %0, %1 offset0:%2 offset1:4" : "=v"(K[0][h]) : "v"(exr), "i"(h) : "memory");
    f32x4 sc[2][4], sp[2][4];
#pragma unroll
    for (int h = 0; h < 4; ++h) {
      sc[1][h] = MFMA(K[2][h], Q[1][h], maskC); PIN(sc[1][h]);
      sp[1][h] = MFMA(K[1][h], Q[1][h], maskP); PIN(sp[1][h]);
      u32x2 qt = pack4(aQ[0][h]);
      unsigned e = ((h < 2) == g0) ? qs[0][h & 1] : 0u;
      Q[0][h] = (h & 1) ? frag{qt[0], qt[1], 0u, e} : frag{qt[0], qt[1], e, 0u}; PIN(Q[0][h]);
      SCHED0();
    }
    asm volatile("s_waitcnt lgkmcnt(0)" : "+v"(gz), "+v"(K[0][0]), "+v"(K[0][1]), "+v"(K[0][2]), "+v"(K[0][3])::"memory");
    float v[2][4][2];
#pragma unroll
    for (int h = 0; h < 4; ++h) {
      sc[0][h] = MFMA(K[1][h], Q[0][h], maskC); PIN(sc[0][h]);
      sp[0][h] = MFMA(K[0][h], Q[0][h], maskP0); PIN(sp[0][h]);
      v[1][h][0] = fmx(fmx3(sp[1][h][0], sp[1][h][1], sc[1][h][0]), sc[1][h][1]); PIN(v[1][h][0]);
      v[1][h][1] = fmx(fmx3(sp[1][h][2], sp[1][h][3], sc[1][h][2]), sc[1][h][3]); PIN(v[1][h][1]);
      SCHED0();
    }
#pragma unroll
    for (int f = 0; f < 5; ++f) {
      u32x2 a = pack4(aV[0][f]); Vb[f][0] = a[0]; Vb[f][1] = a[1]; PIN(Vb[f]);
      Va[f] = __builtin_shufflevector(Vb[f], Vb[f], -1, -1, 0, 1); PIN(Va[f]);
    }
#pragma unroll
    for (int f = 0; f < 5; ++f) { u32x2 n = *(volatile LDSP u32x2*)(size_t)(exr + (5 + f) * 512); Va[f][0] = n[0]; Va[f][1] = n[1]; }
    u32x4 pk[2];
    auto softmax = [&](float (&w)[4][2], float gate_logit) {
      swap32(w[0][0], w[2][0]); swap32(w[1][0], w[3][0]); swap32(w[0][1], w[2][1]); swap32(w[1][1], w[3][1]);
      swap16(w[0][0], w[1][0]); swap16(w[2][0], w[3][0]); swap16(w[0][1], w[1][1]); swap16(w[2][1], w[3][1]);
      const float cs = 0.23570226039551584f * 1.4426950408889634f;
      float m = fmx(fmx3(fmx3(w[0][0], w[1][0], w[2][0]), w[3][0], w[0][1]), fmx3(w[1][1], w[2][1], w[3][1]));
      f32x2 e[4];
#pragma unroll
      for (int i = 0; i < 4; ++i) {
        f32x2 a = f32x2{w[i][0], w[i][1]} - m;
        PIN(a);
        a *= cs;
        e[i] = f32x2{__builtin_amdgcn_exp2f(a[0]), __builtin_amdgcn_exp2f(a[1])};
      }
      f32x2 s2 = (e[0] + e[1]) + (e[2] + e[3]);
      float s = s2[0] + s2[1];
      float r = __builtin_amdgcn_rcpf(__builtin_fmaf(s, __builtin_amdgcn_exp2f(gate_logit * sg), s));
      float pf[4];
#pragma unroll
      for (int i = 0; i < 4; ++i) { f32x2 pp = e[i] * r; pf[i] = __uint_as_float(cvtpk(pp[0], pp[1])); }
      swap32(pf[0], pf[2]); swap32(pf[1], pf[3]);
      swap16(pf[0], pf[1]); swap16(pf[2], pf[3]);
      return u32x4{__float_as_uint(pf[0]), __float_as_uint(pf[1]), __float_as_uint(pf[2]), __float_as_uint(pf[3])};
    };
    SCHED0();
#pragma unroll
    for (int h = 0; h < 4; ++h) {
      v[0][h][0] = fmx(fmx3(sp[0][h][0], sp[0][h][1], sc[0][h][0]), sc[0][h][1]);
      v[0][h][1] = fmx(fmx3(sp[0][h][2], sp[0][h][3], sc[0][h][2]), sc[0][h][3]);
    }
    pk[1] = softmax(v[1], __uint_as_float(gz[1]));
#pragma unroll
    for (int h = 0; h < 4; ++h) { PIN(v[0][h][0]); PIN(v[0][h][1]); }
    SCHED0();
    pv_store(pk[1], Vb, sel, vmain, vleft, ors, osoff1, oloff1, g0);
    pk[0] = softmax(v[0], __uint_as_float(gz[0]));
    SCHED0();
    pv_store(pk[0], Va, sel, vmain, vleft, ors, osoff, oloff, g0);
    STAMP(25);
    WAIT_VM0();
    if (lane == 0) __hip_atomic_store(myflag, tag, RLX_AGENT);
    STAMP(26);
    RSTAMP(61);
    asm volatile(".Lfk_tail:");
  }
  BARRIER();
  if (!act2) {
    if (bid == 255) {
      if (wave < 4) {
        const int b = wave * 64 + lane;
        GAS u64* fp = (GAS u64*)(ws + FLAG_OFF + (((b & 15) >> 2) * 64 + (b >> 4) * 4) * FS1 + (b & 3) * FS2);
#pragma clang loop unroll(disable)
        for (unsigned spins = 0; spins < (1u << 17); ++spins) {
          u64 v = __hip_atomic_load(fp, RLX_AGENT);
          if (__all(v == tag)) break;
          __builtin_amdgcn_s_sleep(8);
        }
      }
      BARRIER();
      if (tid == 0) __hip_atomic_store((GAS u64*)(ws + EPOCH_OFF), tag, RLX_AGENT);
    }
    return;
  }
  if (cw == 3) {
#pragma unroll
    for (int j = 0; j < 4; ++j) { wo_piece(3, 7, j); wo_piece(3, 8, j); }
  }
  if (wave < 4) phase2<2>(L, ws, ors, tag, rt * 5 + 3, 48, cw, rot2, lane STAMP_PASS);
  else phase2<3>(L, ws, ors, tag, rt * 5, 0, cw, rot2, lane STAMP_PASS);
  STAMP(30);
  if (wave < 5 && rt * 5 + wave < 128) {
    f32x4 s[2][2];
#pragma unroll
    for (int it = 0; it < 2; ++it) {
      const int r = it * 8 + vl;
      const unsigned a0 = L + (wave * 16 + r) * 256 + ((((lane & 7) * 2) ^ r) * 16);
#pragma unroll
      for (int w = 0; w < 4; ++w) {
        f32x4 u0 = *(LDSP f32x4*)(size_t)(a0 + w * 36864), u1 = *(LDSP f32x4*)(size_t)((a0 ^ 16) + w * 36864);
        s[it][0] = w ? s[it][0] + u0 : u0; s[it][1] = w ? s[it][1] + u1 : u1;
      }
    }
#pragma unroll
    for (int it = 0; it < 2; ++it) {
      u32x4 o = {cvtpk(s[it][0][0], s[it][0][1]), cvtpk(s[it][0][2], s[it][0][3]), cvtpk(s[it][1][0], s[it][1][1]), cvtpk(s[it][1][2], s[it][1][3])};
      *(GAS u32x4*)((GAS char*)out + (size_t)(rt * 80 + wave * 16 + it * 8 + vl) * XROW + ct * 128 + (lane & 7) * 16) = o;
    }
  }
  STAMP(31);
  RSTAMP(63);
  STAMP_OUT();
}

#undef x
#undef wA
#undef wB
#undef woA
#undef woB
#undef gw
#undef ws
#undef out
#undef ors

extern "C" void kernel_launch(void* const* d_in, const int* in_sizes, int n_in, void* d_out, int out_size, void* d_ws, size_t ws_size,
                              hipStream_t stream) {
  fused_kernel<<<256, 512, 0, stream>>>((const u16*)d_in[0], (const u16*)d_in[1], (const u16*)d_in[4], (const u16*)d_in[2], (const u16*)d_in[5],
                                        (const u16*)d_in[6], (char*)d_ws, (u16*)d_out);
}
```

```cpp
#include <hip/hip_runtime.h>
#include <hip/hip_bf16.h>
#include <stdint.h>
#include <type_traits>

typedef unsigned short u16;
typedef unsigned long long u64;
typedef short s16x4 __attribute__((ext_vector_type(4)));
typedef float f32x2 __attribute__((ext_vector_type(2)));
typedef float f32x4 __attribute__((ext_vector_type(4)));
typedef unsigned u32x2 __attribute__((ext_vector_type(2)));
typedef unsigned u32x4 __attribute__((ext_vector_type(4)));
typedef u32x4 frag;
typedef __bf16 bf16x2 __attribute__((ext_vector_type(2)));
typedef __bf16 bfx8 __attribute__((ext_vector_type(8)));
#define LDSP __attribute__((address_space(3)))
#define GAS __attribute__((address_space(1)))
#define DI __device__ __forceinline__

constexpr int WROW = 3456;
constexpr int XROW = 1152;
constexpr int PITCH = 432;
constexpr int KO = 32 * PITCH;
constexpr int STG_W = 64 * PITCH;
constexpr int STG = STG_W + 144 * 128;
constexpr int G_OFF = 3 * STG;
constexpr int SCR_OFF = G_OFF + 4608;
constexpr int EX_OFF = 143360;
constexpr int LDS_BYTES = 163840;
constexpr int O_BYTES = 128 * 36 * 1024;
constexpr int FLAG_OFF = 8 << 20;
constexpr int EPOCH_OFF = FLAG_OFF + (1 << 20);
#define ROT 11
#define LPRIO 2
#define PRE0 5
#define NBX 22
#define FS1 128
#define FS2 8
#define PSLEEP 1
#define CODEPF 22656
#define NB1 5

#define MFMA(a, b, c) __builtin_amdgcn_mfma_f32_16x16x32_bf16(__builtin_bit_cast(bfx8, (a)), __builtin_bit_cast(bfx8, (b)), (c), 0, 0, 0)
#define RLX_AGENT __ATOMIC_RELAXED, __HIP_MEMORY_SCOPE_AGENT
#define HOFF(h) (36 * (h) + ((h) & 1) * 4)

DI void glds16(unsigned voff, const GAS void* sbase, unsigned lds_addr) {
  asm volatile("s_mov_b32 m0, %2\n\ts_nop 0\n\tglobal_load_lds_dwordx4 %0, %1" ::"v"(voff), "s"(sbase), "s"(lds_addr) : "memory");
}
DI u32x2 trd(unsigned a) { return __builtin_bit_cast(u32x2, __builtin_amdgcn_ds_read_tr16_b64_v4i16((LDSP s16x4*)(size_t)a)); }
DI frag ldW(unsigned a0, unsigned a1) {
  u32x2 lo = trd(a0), hi = trd(a1);
  return __builtin_shufflevector(lo, hi, 0, 1, 2, 3);
}
DI frag ld128(unsigned a) { return *(LDSP frag*)(size_t)a; }
DI u32x2 ld64(unsigned a) { return *(LDSP u32x2*)(size_t)a; }
DI void st64(unsigned a, u32x2 v) { *(LDSP u32x2*)(size_t)a = v; }
DI unsigned cvtpk(float a, float b) {
  f32x2 f = {a, b};
  return __builtin_bit_cast(unsigned, __builtin_convertvector(f, bf16x2));
}
DI u32x2 pack4(f32x4 a) { return u32x2{cvtpk(a[0], a[1]), cvtpk(a[2], a[3])}; }
DI frag cat(u32x2 a, u32x2 b) { return __builtin_shufflevector(a, b, 0, 1, 2, 3); }
DI void swap32(float& a, float& b) {
  auto r = __builtin_amdgcn_permlane32_swap(__float_as_uint(a), __float_as_uint(b), false, false);
  a = __uint_as_float(r[0]); b = __uint_as_float(r[1]);
}
DI void swap16(float& a, float& b) {
  auto r = __builtin_amdgcn_permlane16_swap(__float_as_uint(a), __float_as_uint(b), false, false);
  a = __uint_as_float(r[0]); b = __uint_as_float(r[1]);
}
DI float fmx(float a, float b) { return __builtin_elementwise_maximum(a, b); }
DI float fmx3(float a, float b, float c) { return fmx(fmx(a, b), c); }
#define STAMP(i)
#define RSTAMP(i)
#define STAMP_OUT()
#define STAMP_ARG
#define STAMP_PASS
#define STAMP_DECL
#define SGB(m, n) __builtin_amdgcn_sched_group_barrier(m, n, 0)
#define SCHED0() __builtin_amdgcn_sched_barrier(0)
#define WAIT_LGKM0() do { __builtin_amdgcn_s_waitcnt(0xC07F); asm volatile("" ::: "memory"); } while (0)
#define WAIT_VM0() asm volatile("s_waitcnt vmcnt(0)" ::: "memory")
#define BARRIER() __builtin_amdgcn_s_barrier()
#define PIN(x) asm volatile("" : "+v"(x))
#define TAG_SCALAR() do { unsigned lo_ = __builtin_amdgcn_readfirstlane((unsigned)tag), hi_ = __builtin_amdgcn_readfirstlane((unsigned)(tag >> 32)); \
    tag = (((u64)hi_ << 32) | lo_) + 1; asm volatile("" : "+s"(tag)); } while (0)
#define PINS(x) asm volatile("" : "+s"(x))
#define WR2(addr, a, b, o0, o1) asm volatile("ds_write2st64_b64 %0, %1, %2 offset0:%3 offset1:%4" ::"v"(addr), "v"(a), "v"(b), "i"(o0), "i"(o1) : "memory")

DI void pv_store(u32x4 pk, const frag (&V)[5], u32x4 sel, unsigned vmain, unsigned vleft, __amdgpu_buffer_rsrc_t ors, unsigned so, unsigned sl, bool g0) {
  const f32x4 z = {0.f, 0.f, 0.f, 0.f};
  frag P[4];
  f32x4 o[4], os[4];
  auto mkP = [&](int h) {
    unsigned u = pk[h];
    P[h] = frag{__builtin_amdgcn_perm(u, u, sel[0]), __builtin_amdgcn_perm(u, u, sel[1]), __builtin_amdgcn_perm(u, u, sel[2]), __builtin_amdgcn_perm(u, u, sel[3])};
    PIN(P[h]);
  };
  mkP(0); mkP(1);
  SCHED0();
  u32x4 f0, f1;
  os[0] = MFMA(V[4], P[0], z); PIN(os[0]); o[0] = MFMA(V[0], P[0], z); PIN(o[0]); mkP(2); SCHED0();
  os[1] = MFMA(V[4], P[1], z); PIN(os[1]); o[1] = MFMA(V[1], P[1], z); PIN(o[1]); mkP(3); SCHED0();
  os[2] = MFMA(V[4], P[2], z); PIN(os[2]); os[3] = MFMA(V[4], P[3], z); PIN(os[3]);
  o[2] = MFMA(V[2], P[2], z); PIN(o[2]); o[3] = MFMA(V[3], P[3], z); PIN(o[3]); SCHED0();
  f0 = u32x4{cvtpk(o[0][0], o[0][1]), cvtpk(o[0][2], o[0][3]), cvtpk(o[1][0], o[1][1]), cvtpk(o[1][2], o[1][3])};
  PIN(f0); SCHED0();
  __builtin_amdgcn_raw_buffer_store_b128(f0, ors, vmain, so, 16);
  SCHED0();
  u32x2 f2 = {cvtpk(g0 ? os[0][0] : os[2][0], g0 ? os[0][1] : os[2][1]), cvtpk(g0 ? os[1][2] : os[3][2], g0 ? os[1][3] : os[3][3])};
  PIN(f2); SCHED0();
  f1 = u32x4{cvtpk(o[2][0], o[2][1]), cvtpk(o[2][2], o[2][3]), cvtpk(o[3][0], o[3][1]), cvtpk(o[3][2], o[3][3])};
  PIN(f1); PIN(f0); SCHED0();
  __builtin_amdgcn_raw_buffer_store_b128(f1, ors, vmain + 1024, so, 16);
  __builtin_amdgcn_raw_buffer_store_b64(f2, ors, vleft, sl, 16);
  SCHED0();
}

template <int MT>
DI void phase2(unsigned L, GAS char* ws, __amdgpu_buffer_rsrc_t ors, u64 tag, int tt0, int mrow, int kq, int rot2, int lane STAMP_ARG) {
  const int g = lane >> 4, q = (lane >> 2) & 3, p = lane & 3, c = lane & 15;
  const unsigned sw = (2 * g + (q >> 1)) & 3;
  unsigned bw[4];
#pragma unroll
  for (int n = 0; n < 4; ++n) bw[n] = L + kq * 36864 + (4 * g + q) * 128 + 8 * p + ((n ^ sw) * 32);
  frag wf[9][4];
  auto rdw = [&](int i) {
#pragma unroll
    for (int n = 0; n < 4; ++n) wf[i][n] = ldW(bw[n] + i * 4096, bw[n] + i * 4096 + 2048);
  };
  rdw(0); rdw(1); rdw(2); rdw(3); rdw(4);
  SCHED0();
  unsigned so[9], vo[MT];
#pragma unroll
  for (int i = 0; i < 9; ++i) so[i] = (i < 8 ? 8 * kq + ((i + rot2) & 7) : 32 + kq) * 1024;
#pragma unroll
  for (int m = 0; m < MT; ++m) { int tt = tt0 + m; tt = tt > 127 ? 127 : tt; vo[m] = lane * 16 + tt * 36864; }
  int tf = (lane & 4) ? tt0 + MT - 1 : tt0; tf = tf > 127 ? 127 : tf;
  GAS u64* fp = (GAS u64*)(ws + FLAG_OFF + (kq * 64 + (tf >> 1)) * FS1 + (lane & 3) * FS2);
  unsigned rb[4];
#pragma unroll
  for (int n = 0; n < 4; ++n) rb[n] = L + kq * 36864 + (mrow + c) * 256 + (((4 * n + g) ^ c) * 16);
#pragma unroll
  for (int i = 0; i < 9; ++i) PINS(so[i]);
#pragma unroll
  for (int m = 0; m < MT; ++m) PIN(vo[m]);
#pragma unroll
  for (int n = 0; n < 4; ++n) PIN(rb[n]);
  PIN(fp);
  WAIT_LGKM0();
  BARRIER();
  STAMP(27);
#pragma clang loop unroll(disable)
  for (unsigned spins = 0; spins < (1u << 17); ++spins) {
    u64 v = __hip_atomic_load(fp, RLX_AGENT);
    if (__all(v == tag)) break;
    __builtin_amdgcn_s_sleep(PSLEEP);
  }
  __builtin_amdgcn_fence(__ATOMIC_ACQUIRE, "wavefront");
  asm volatile("" ::: "memory");
  STAMP(28);
  RSTAMP(62);
  frag of[9][MT];
  constexpr int WIN = 2;
#pragma unroll
  for (int i = 0; i < WIN; ++i)
#pragma unroll
    for (int m = 0; m < MT; ++m) { of[i][m] = __builtin_amdgcn_raw_buffer_load_b128(ors, vo[m], so[i], 16); SCHED0(); }
  f32x4 acc[MT][4];
#pragma unroll
  for (int i = 0; i < 9; ++i) {
    const bool rd = i >= 2 && i + 3 < 9;
    if (rd) rdw(i + 3);
#pragma unroll
    for (int m = 0; m < MT; ++m) {
#pragma unroll
      for (int n = 0; n < 4; ++n) acc[m][n] = MFMA(wf[i][n], of[i][m], i == 0 ? (f32x4{0, 0, 0, 0}) : acc[m][n]);
      if (i + WIN < 9) of[i + WIN][m] = __builtin_amdgcn_raw_buffer_load_b128(ors, vo[m], so[i + WIN], 16);
    }
#pragma unroll
    for (int m = 0; m < MT; ++m) {
#pragma unroll
      for (int n = 0; n < 4; ++n) { SGB(0x8, 1); if (m < 2 && rd) SGB(0x100, 1); }
      if (i + WIN < 9) SGB(0x20, 1);
    }
    SCHED0();
  }
  STAMP(29);
#pragma unroll
  for (int m = 0; m < MT; ++m)
#pragma unroll
    for (int n = 0; n < 4; ++n) *(LDSP f32x4*)(size_t)(rb[n] + m * 4096) = acc[m][n];
  WAIT_LGKM0();
  BARRIER();
}

typedef u64 u64x8 __attribute__((ext_vector_type(8)));
extern "C" __global__ __launch_bounds__(512, 2) void fused_kernel(const u16* x_, const u16* wA_, const u16* wB_, const u16* woA_, const u16* woB_,
                                                                   const u16* gw_, char* ws_, u16* out_) {
  __shared__ __attribute__((aligned(1024))) char lds[LDS_BYTES];
  const unsigned L = (unsigned)(size_t)(LDSP char*)lds;
  const int BIDX = blockIdx.x, TIDX = threadIdx.x;
  u64x8 ka;
  asm volatile("s_load_dwordx16 %0, %1, 0x0" : "=s"(ka) : "s"(__builtin_amdgcn_kernarg_segment_ptr()));
#define KARG_WAIT() do { SCHED0(); asm volatile("s_waitcnt lgkmcnt(0)" ::: "memory"); SCHED0(); asm volatile("" : "+s"(ka)); } while (0)
#define x ((const GAS u16*)ka[0])
#define wA ((const GAS u16*)ka[1])
#define wB ((const GAS u16*)ka[2])
#define woA ((const GAS u16*)ka[3])
#define woB ((const GAS u16*)ka[4])
#define gw ((const GAS u16*)ka[5])
#define ws ((GAS char*)ka[6])
#define out ((GAS u16*)ka[7])
#define ors __builtin_amdgcn_make_buffer_rsrc((void*)ka[6], 0, O_BYTES, 0x00020000)
  const int bid = BIDX;
  const int xcd = bid & 7, idx = bid >> 3;
  const int tl = idx & 7, gl = idx >> 3;
  const int tile = (xcd & 1) * 8 + tl;
  const int grp = (xcd >> 1) * 4 + gl;
  int rot = ROT == 1 ? tl + 2 * gl : ROT == 2 ? (tl >> 1) * 2 + 2 * gl : ROT == 6 ? (tl % 3) * 3 + (gl % 3) * 3 : ROT == 7 ? (((tl >> 1) + gl) & 3) * 2 + (tl & 1 & (gl >> 1))
          : ROT == 8 ? (((tl + gl) & 3) * 9) >> 2 : ROT == 9 ? ((tl + gl + (xcd >> 1)) % 3) * 3 : ROT == 10 ? (((tl + gl) & 3) * 9 >> 2) + (xcd & 1)
          : ROT == 11 ? ((tl + gl) & 3) * 2 + ((xcd ^ (xcd >> 1)) & 1) : ROT == 12 ? ((tl + gl) % 3) * 3 + ((xcd & 1) + (xcd >> 1)) % 3
          : ROT == 13 ? ((tl + gl) & 1) * 4 + (((xcd & 1) * 2 + (xcd >> 1)) & 3) : ROT == 14 ? (tl + 2 * gl + ((xcd >> 1) & 1)) % 9
          : ROT == 15 ? (((tl + gl) & 3) * 2 + ((xcd ^ (xcd >> 1)) & 1) + (xcd >> 2)) : 0;
  if (rot >= 9) rot -= 9;
  const int tid = TIDX, lane = tid & 63;
  const int wave = __builtin_amdgcn_readfirstlane(tid >> 6);
  const int vl = lane >> 3;
  STAMP_DECL;
  STAMP(0);
  RSTAMP(60);
  if (tid < 7) {
    u64 pc0;
    unsigned len;
    asm volatile("s_getpc_b64 %0\n\ts_mov_b32 %1, .Lfk_tail-fused_kernel+128" : "=s"(pc0), "=s"(len));
    unsigned off = (idx * 7 + tid) * 128;
    off = off < len ? off : 0;
    asm volatile("s_mov_b32 m0, %2\n\ts_nop 0\n\tglobal_load_lds_dword %0, %1" ::"v"(off), "s"(pc0 & ~255ull), "s"(L + SCR_OFF) : "memory");
  }
  u64 tag;
  const int lin0 = xcd * 30 + idx;
  const bool act2 = idx < 30 && lin0 < 234;
  const int lin = act2 ? lin0 : 0;
  const int rt = (lin * 57) >> 9, ct = lin - rt * 9;
  const int rot2 = 3 * (rt - ((rt * 171) >> 9) * 3);
  const float sg = (grp >= 8) ? 1.4426950408889634f : -1.4426950408889634f;
  const unsigned wsch = ((((lane & 7) >> 1) ^ ((vl >> 1) & 3)) << 5) | ((lane & 1) << 4);
  const unsigned offn = vl * XROW + wsch, offl = ((vl >> 2) * 72 + (vl & 3)) * XROW + wsch;
  auto wo_piece = [&](int kq, int i, int j) {
    const GAS char* b = (const GAS char*)(kq >= 2 ? woB : woA) + ct * 128 + (kq & 1) * (288 * XROW);
    if (i == 8) b += ((j & 1) * 144 + ((j >> 1) ? 52 : 16)) * XROW;
    else b += (((i + rot2) & 7) * 36 + (j >> 1) * 20 + 8 * (j & 1)) * XROW;
    glds16(i == 8 ? offl : offn, b, L + (kq * 9 + i) * 4096 + j * 1024);
  };
  const int g = lane >> 4, q = (lane >> 2) & 3, p = lane & 3, c = lane & 15;
  const int cw = wave & 3;

  if (__builtin_expect(wave >= 4, 1)) {
    __builtin_amdgcn_s_setprio(LPRIO);
    const int lw = cw, tl256 = tid - 256;
    const unsigned offx = (tile * 128 + 8 * lw + vl) * XROW + ((lane & 7) ^ ((4 * lw + (vl >> 1)) & 7)) * 16;
    KARG_WAIT();
    const GAS char* w = (const GAS char*)((grp >= 8) ? wB : wA) + (grp & 7) * 144;
    unsigned offw[7], offh;
    auto kchunk = [&](int st) { int kc = st + rot; return kc >= 9 ? kc - 9 : kc; };
    auto issueX = [&](int kc, unsigned sb, int jj) { glds16(offx, (const GAS char*)x + kc * 128 + jj * (32 * XROW), sb + STG_W + (2 + lw + 4 * jj) * 1024); };
    auto issueW = [&](int kc, unsigned sb, int j) { glds16(offw[j], w + kc * (64 * WROW), sb + (lw + 4 * j) * 1024); };
    auto issue6 = [&](int kc, unsigned sb) {
      if (lw < 3) issueW(kc, sb, 6);
      else { glds16(offw[6], (const GAS char*)x + kc * 128, sb + STG_W); glds16(offh, (const GAS char*)x + kc * 128, sb + STG_W + 1024); }
    };
    const int kc0 = rot, kc1 = kchunk(1);
    const unsigned sb0 = L, sb1 = L + STG;
    auto calcw = [&](int j) {
      unsigned qq = 256 * j + tl256;
      unsigned u = (qq * 2428u) >> 16, cc = qq - u * 27;
      unsigned row = (u & 0x38) | ((u & 1) << 2) | ((u >> 1) & 3);
      offw[j] = row * WROW + cc * 16 + ((cc * 57u) >> 9) * 1008;
    };
    issueX(kc0, sb0, 0);
    u32x2 gv;
    const unsigned gi = tl256 < 144 ? tl256 : 143;
    asm volatile("global_load_dwordx2 %0, %1, %2" : "=v"(gv) : "v"(gi * 8), "s"(gw) : "memory");
#pragma unroll
    for (int j = 0; j < 6; ++j) {
      SCHED0();
      calcw(j);
      SCHED0();
      issueW(kc0, sb0, j);
      if (j < 3) issueX(kc0, sb0, j + 1);
    }
    calcw(6);
    {
      int t0 = tile * 128 - 16 + vl, t1 = t0 + 8;
      t0 = t0 < 0 ? 0 : t0; t1 = t1 < 0 ? 0 : t1;
      unsigned h0 = t0 * XROW + ((lane & 7) ^ (vl >> 1)) * 16;
      offh = t1 * XROW + ((lane & 7) ^ (4 + (vl >> 1))) * 16;
      offw[6] = lw == 3 ? h0 : offw[6];
    }
    issue6(kc0, sb0);
    STAMP(1);
    issueW(kc1, sb1, 0);
    int slot = 0, kn = kc1;
    const GAS char* xp = (const GAS char*)x + kn * 128;
    unsigned b1 = sb1, xd = sb1 + STG_W + (2 + lw) * 1024;
    PINS(xp); PINS(xd);
    SCHED0();
#pragma unroll
    for (int j = 1; j < PRE0; ++j) issueW(kc1, sb1, j);
    STAMP(2);
    asm volatile("s_waitcnt vmcnt(%1)" : "+v"(gv) : "i"(10 + PRE0) : "memory");
    {
      unsigned a0 = __builtin_amdgcn_perm(gv[0], gv[0], 0x01000100), a1 = __builtin_amdgcn_perm(gv[0], gv[0], 0x03020302);
      unsigned a2 = __builtin_amdgcn_perm(gv[1], gv[1], 0x01000100), a3 = __builtin_amdgcn_perm(gv[1], gv[1], 0x03020302);
      *(LDSP u32x4*)(size_t)(L + G_OFF + gi * 32) = u32x4{a0, a0, a1, a1};
      *(LDSP u32x4*)(size_t)(L + G_OFF + gi * 32 + 16) = u32x4{a2, a2, a3, a3};
    }
    __builtin_amdgcn_s_setprio(0);
    WAIT_LGKM0();
    asm volatile("s_waitcnt vmcnt(%0)" ::"i"(PRE0) : "memory");
#pragma clang loop unroll(disable)
    for (int st = 0; st < 7; ++st) {
      BARRIER();
      glds16(offx, xp, xd);
      SCHED0();
      STAMP(3 + st);
      glds16(offx, xp + 32 * XROW, xd + 4096);
      if (st == 0) {
#pragma unroll
        for (int j = PRE0; j < 6; ++j) issueW(kc1, sb1, j);
        tag = __hip_atomic_load((GAS u64*)(ws + EPOCH_OFF), RLX_AGENT);
      }
      issue6(kn, b1);
      glds16(offx, xp + 64 * XROW, xd + 8192);
      glds16(offx, xp + 96 * XROW, xd + 12288);
      SCHED0();
      kn = kn == 8 ? 0 : kn + 1;
      b1 = L + (slot == 0 ? 2 : slot - 1) * STG;
      issueW(kn, b1, 0); issueW(kn, b1, 1);
#pragma unroll
      for (int j = 2; j < 6; ++j) issueW(kn, b1, j);
      slot = slot == 2 ? 0 : slot + 1;
      xp = (const GAS char*)x + kn * 128;
      xd = b1 + STG_W + (2 + lw) * 1024;
      PINS(xp); PINS(xd);
      asm volatile("s_waitcnt vmcnt(6)" ::: "memory");
    }
    BARRIER();
    glds16(offx, xp, xd);
    SCHED0();
    STAMP(10);
    glds16(offx, xp + 32 * XROW, xd + 4096);
    issue6(kn, b1);
    glds16(offx, xp + 64 * XROW, xd + 8192);
    glds16(offx, xp + 96 * XROW, xd + 12288);
#pragma unroll
    for (int I = 0; I < 6; ++I) wo_piece(0, I, lw);
    asm volatile("s_waitcnt vmcnt(6)" ::: "memory");
    BARRIER();
    STAMP(11);
    wo_piece(0, 6, lw); wo_piece(0, 7, lw);
#pragma unroll
    for (int I = 8; I < NBX; ++I) wo_piece(I / 9, I % 9, lw);
    WAIT_LGKM0();
    STAMP(12);
    BARRIER();
    STAMP(13);
#pragma unroll
    for (int I = NBX; I < 34; ++I) wo_piece(I / 9, I % 9, lw);
    WAIT_VM0();
    TAG_SCALAR();
    STAMP(14);
  } else {
    const unsigned wrow = L + (8 * g + 2 * q) * PITCH;
    f32x4 maskP, maskC, maskP0;
    {
      const float ninf = -__builtin_inff();
#pragma unroll
      for (int r = 0; r < 4; ++r) {
        bool pm = (r & 1) == (c & 1);
        maskC[r] = (pm && 4 * g + r <= c) ? 0.f : ninf;
        maskP[r] = (pm && 4 * g + r > c) ? 0.f : ninf;
      }
      maskP0 = maskP + ((tile == 0 && wave == 0) ? -998244352.f * 4.242640687119285f : 0.f);
      asm volatile("" : "+v"(maskP), "+v"(maskC), "+v"(maskP0));
    }
    u32x4 sel;
    {
      const int par = c & 1;
      const bool cur1 = 4 * g + par <= c, cur2 = 4 * g + 2 + par <= c;
      const unsigned s1 = par ? 0x01000c0cu : 0x0c0c0100u;
      const unsigned s2 = par ? 0x03020c0cu : 0x0c0c0302u;
      sel[0] = cur1 ? 0x0c0c0c0cu : s1;
      sel[1] = cur2 ? 0x0c0c0c0cu : s2;
      sel[2] = cur1 ? s1 : 0x0c0c0c0cu;
      sel[3] = cur2 ? s2 : 0x0c0c0c0cu;
    }
    unsigned vmain = lane * 16, vleft = lane < 32 ? c * 16 + g * 8 : 0x40000000u;
    unsigned osoff = (tile * 8 + 2 * wave) * 36864 + grp * 2048, oloff = (tile * 8 + 2 * wave) * 36864 + 32768 + grp * 256;
    unsigned osoff1 = osoff + 36864, oloff1 = oloff + 36864;
    PINS(osoff); PINS(oloff); PINS(osoff1); PINS(oloff1);
    asm volatile("" : "+v"(sel), "+v"(vmain), "+v"(vleft));
    f32x4 aK[2][4], aQ[2][4], aKQ[2], aV[2][5], hk, hv, he;
    frag cv[5], xb[3];
    unsigned exr = L + EX_OFF + wave * 5120 + lane * 8, exh = L + EX_OFF + wave * 512 + lane * 8;
    unsigned exgw = L + SCR_OFF + wave * 128 + g * 16, exgr = L + SCR_OFF + wave * 128 + c * 4;
    PIN(exr); PIN(exh); PIN(exgw); PIN(exgr);
    auto mainloop = [&](auto WC) {
      constexpr int W = decltype(WC)::value;
      constexpr int XO = (1 + 2 * W) * 2048;
    hk = f32x4{0, 0, 0, 0}; hv = f32x4{0, 0, 0, 0}; he = f32x4{0, 0, 0, 0};
    PIN(he);
#pragma unroll
    for (int t = 0; t < 2; ++t) {
#pragma unroll
      for (int f = 0; f < 4; ++f) { aK[t][f] = f32x4{0, 0, 0, 0}; aQ[t][f] = f32x4{0, 0, 0, 0}; }
      aKQ[t] = f32x4{0, 0, 0, 0};
#pragma unroll
      for (int f = 0; f < 5; ++f) aV[t][f] = f32x4{0, 0, 0, 0};
    }
    unsigned cW = wrow + 8 * p;
    unsigned cKQ = wrow + (p < 2 ? 144 : 0) + ((p & 1) ? 104 : 32);
    unsigned cX0 = L + STG_W + c * 128 + ((g ^ (c >> 1)) * 16), cX1 = cX0 ^ 64;
    unsigned cV[4];
#pragma unroll
    for (int i = 0; i < 4; ++i)
      cV[i] = p < 2 ? wrow + 288 + (p ? 104 : 32) + (i >> 1) * KO + (i & 1) * PITCH : L + G_OFF + rot * 512 + (8 * g + q) * 8 + (i >> 1) * 256 + (i & 1) * 32;
#pragma unroll
    for (int f = 0; f < 5; ++f) cv[f] = frag{0, 0, 0, 0};
    xb[0] = frag{0, 0, 0, 0}; xb[1] = frag{0, 0, 0, 0}; xb[2] = frag{0, 0, 0, 0};
    asm volatile("" : "+v"(aK[0][0]), "+v"(aK[0][1]), "+v"(aK[0][2]), "+v"(aK[0][3]), "+v"(aK[1][0]), "+v"(aK[1][1]), "+v"(aK[1][2]), "+v"(aK[1][3]),
                      "+v"(aQ[0][0]), "+v"(aQ[0][1]), "+v"(aQ[0][2]), "+v"(aQ[0][3]), "+v"(aQ[1][0]), "+v"(aQ[1][1]), "+v"(aQ[1][2]), "+v"(aQ[1][3]),
                      "+v"(aKQ[0]), "+v"(aKQ[1]), "+v"(hk), "+v"(hv), "+v"(xb[0]), "+v"(xb[1]), "+v"(xb[2]));
    asm volatile("" : "+v"(aV[0][0]), "+v"(aV[0][1]), "+v"(aV[0][2]), "+v"(aV[0][3]), "+v"(aV[0][4]), "+v"(aV[1][0]), "+v"(aV[1][1]), "+v"(aV[1][2]),
                      "+v"(aV[1][3]), "+v"(aV[1][4]), "+v"(cv[0]), "+v"(cv[1]), "+v"(cv[2]), "+v"(cv[3]), "+v"(cv[4]),
                      "+v"(cW), "+v"(cKQ), "+v"(cX0), "+v"(cX1), "+v"(cV[0]), "+v"(cV[1]), "+v"(cV[2]), "+v"(cV[3]));
    SCHED0();
    STAMP(1);
    KARG_WAIT();
    STAMP(2);
    WAIT_LGKM0();
      int slot = 0, kc = rot;
#pragma clang loop unroll(disable)
      for (int st = 0; st < 9; ++st) {
        BARRIER();
        SCHED0();
        frag xa[3], W0[14], W1[9];
        xa[0] = ld128(cX0 + XO); xa[1] = ld128(cX0 + XO + 2048);
#pragma unroll
        for (int f = 0; f < 4; ++f) W0[f ^ 1] = ldW(cW + 144 + HOFF(f ^ 1), cW + 144 + HOFF(f ^ 1) + PITCH);
        xa[2] = ld128(cX0);
#pragma unroll
        for (int f = 0; f < 5; ++f) { aV[0][f] = MFMA(xb[0], cv[f], aV[0][f]); aV[1][f] = MFMA(xb[1], cv[f], aV[1][f]); }
        hv = MFMA(xb[2], cv[W], hv);
        if (W == 1) he = MFMA(xb[2], cv[4], he);
        W0[5] = ldW(cW + HOFF(0), cW + HOFF(0) + PITCH);
        W0[4] = ldW(cKQ, cKQ + PITCH);
        W0[7] = ldW(cW + HOFF(2), cW + HOFF(2) + PITCH);
        W0[6] = ldW(cW + HOFF(1), cW + HOFF(1) + PITCH);
        W0[9] = ldW(cW + 288 + HOFF(0), cW + 288 + HOFF(0) + PITCH);
        W0[8] = ldW(cW + HOFF(3), cW + HOFF(3) + PITCH);
        W0[11] = ldW(cW + 288 + HOFF(2), cW + 288 + HOFF(2) + PITCH);
        W0[10] = ldW(cW + 288 + HOFF(1), cW + 288 + HOFF(1) + PITCH);
        W0[13] = ldW(cV[0], cV[1]);
        W0[12] = ldW(cW + 288 + HOFF(3), cW + 288 + HOFF(3) + PITCH);
        xb[0] = ld128(cX1 + XO); xb[1] = ld128(cX1 + XO + 2048); xb[2] = ld128(cX1);
#pragma unroll
        for (int f = 0; f < 4; ++f) W1[f ^ 1] = ldW(cW + KO + 144 + HOFF(f ^ 1), cW + KO + 144 + HOFF(f ^ 1) + PITCH);
        W1[5] = ldW(cW + KO + HOFF(0), cW + KO + HOFF(0) + PITCH);
        W1[4] = ldW(cKQ + KO, cKQ + KO + PITCH);
        W1[8] = ldW(cW + KO + HOFF(3), cW + KO + HOFF(3) + PITCH);
        W1[7] = ldW(cW + KO + HOFF(2), cW + KO + HOFF(2) + PITCH);
        W1[6] = ldW(cW + KO + HOFF(1), cW + KO + HOFF(1) + PITCH);
#pragma unroll
        for (int f = 0; f < 4; ++f) cv[f] = ldW(cW + KO + 288 + HOFF(f), cW + KO + 288 + HOFF(f) + PITCH);
        cv[4] = ldW(cV[2], cV[3]);
#pragma unroll
        for (int f = 0; f < 4; ++f) { aK[0][f] = MFMA(W0[f], xa[0], aK[0][f]); aK[1][f] = MFMA(W0[f], xa[1], aK[1][f]); }
        hk = MFMA(W0[W], xa[2], hk);
        aKQ[0] = MFMA(W0[4], xa[0], aKQ[0]); aKQ[1] = MFMA(W0[4], xa[1], aKQ[1]);
        if (W == 0) he = MFMA(W0[4], xa[2], he);
#pragma unroll
        for (int f = 0; f < 4; ++f) { aQ[0][f] = MFMA(W0[5 + f], xa[0], aQ[0][f]); aQ[1][f] = MFMA(W0[5 + f], xa[1], aQ[1][f]); }
#pragma unroll
        for (int f = 0; f < 5; ++f) { aV[0][f] = MFMA(xa[0], W0[9 + f], aV[0][f]); aV[1][f] = MFMA(xa[1], W0[9 + f], aV[1][f]); }
        hv = MFMA(xa[2], W0[9 + W], hv);
        if (W == 1) he = MFMA(xa[2], W0[13], he);
#pragma unroll
        for (int f = 0; f < 4; ++f) { aK[0][f] = MFMA(W1[f], xb[0], aK[0][f]); aK[1][f] = MFMA(W1[f], xb[1], aK[1][f]); }
        hk = MFMA(W1[W], xb[2], hk);
        aKQ[0] = MFMA(W1[4], xb[0], aKQ[0]); aKQ[1] = MFMA(W1[4], xb[1], aKQ[1]);
        if (W == 0) he = MFMA(W1[4], xb[2], he);
#pragma unroll
        for (int f = 0; f < 4; ++f) { aQ[0][f] = MFMA(W1[5 + f], xb[0], aQ[0][f]); aQ[1][f] = MFMA(W1[5 + f], xb[1], aQ[1][f]); }
        const int ds = slot == 2 ? -2 * STG : STG, dg = kc == 8 ? -8 * 512 : 512;
        slot = slot == 2 ? 0 : slot + 1;
        kc = kc == 8 ? 0 : kc + 1;
        const int dv = p < 2 ? ds : dg;
        cW += ds; cKQ += ds; cX0 += ds; cX1 += ds;
#pragma unroll
        for (int i = 0; i < 4; ++i) cV[i] += dv;
        SGB(0x100, 2);
#pragma unroll
        for (int i = 0; i < 54; ++i) {
          SGB(0x8, 1);
          if (i < 3 || i == 16 || i == 28 || i == 40) SGB(0x100, 2); else SGB(0x100, 1);
          if (i >= 3 && i < 11) SGB(0x4, 1);
          if (i >= 51) SGB(0x2, 1);
        }
#pragma unroll
        for (int i = 0; i < 4; ++i) { SGB(0x8, 1); SGB(0x2, 2); }
        SGB(0x8, W < 2 ? 4 : 2);
        SCHED0();
        WAIT_LGKM0();
      }
      hv = MFMA(xb[2], cv[W], hv);
      if (W == 1) he = MFMA(xb[2], cv[4], he);
      if (W < 2) {
        u32x2 v = pack4(he);
        if (W == 0 && tile == 0) v = u32x2{0u, 0u};
        st64(L + EX_OFF + lane * 8 + (W == 0 ? 4 : 9) * 512, v);
      }
    };
    if (wave == 0) mainloop(std::integral_constant<int, 0>{});
    else if (wave == 1) mainloop(std::integral_constant<int, 1>{});
    else if (wave == 2) mainloop(std::integral_constant<int, 2>{});
    else mainloop(std::integral_constant<int, 3>{});
    WAIT_VM0();
    STAMP(12);
    const bool g0 = g == 0;
    frag K[3][4], Q[2][4], Vb[5], Va[5];
    u32x2 ks[2], qs[2];
    aV[1][4] = MFMA(xb[1], cv[4], aV[1][4]); PIN(aV[1][4]);
    aV[0][4] = MFMA(xb[0], cv[4], aV[0][4]); PIN(aV[0][4]);
#pragma unroll
    for (int t = 1; t >= 0; --t) {
      u32x2 kq = pack4(aKQ[t]);
      auto r0 = __builtin_amdgcn_permlane32_swap(kq[0], 0u, false, false);
      auto r1 = __builtin_amdgcn_permlane32_swap(kq[1], 0u, false, false);
      ks[t] = u32x2{r0[0], r1[0]}; qs[t] = u32x2{r0[1], r1[1]};
      PIN(ks[t]); PIN(qs[t]);
    }
    SCHED0();
#pragma unroll
    for (int f = 0; f < 4; ++f) { aV[1][f] = MFMA(xb[1], cv[f], aV[1][f]); PIN(aV[1][f]); K[2][f] = cat(pack4(aK[1][f]), ks[1]); PIN(K[2][f]); SCHED0(); }
    {
      u32x2 h = pack4(hk), hh = pack4(hv);
      if (tile == 0) h = u32x2{0u, 0u};
      WR2(exh, h, hh, 0, 5);
    }
    if (c >= 8) { *(LDSP f32x4*)(size_t)(exgw) = aV[0][4]; *(LDSP f32x4*)(size_t)(exgw + 64) = aV[1][4]; }
    SCHED0();
#pragma unroll
    for (int f = 0; f < 4; ++f) {
      aV[0][f] = MFMA(xb[0], cv[f], aV[0][f]); PIN(aV[0][f]);
      u32x2 v = pack4(aV[1][f]); Vb[f][2] = v[0]; Vb[f][3] = v[1]; PIN(Vb[f]); SCHED0();
    }
    { u32x2 v = pack4(aV[1][4]); Vb[4][2] = v[0]; Vb[4][3] = v[1]; PIN(Vb[4]); }
    if (wave < 3) {
#pragma unroll
      for (int f = 0; f < 4; ++f) WR2(exr, (u32x2{K[2][f][0], K[2][f][1]}), (u32x2{Vb[f][2], Vb[f][3]}), 10 + f, 15 + f);
      WR2(exr, ks[1], (u32x2{Vb[4][2], Vb[4][3]}), 14, 19);
    }
    SCHED0();
#pragma unroll
    for (int h = 0; h < 4; ++h) {
      K[1][h] = cat(pack4(aK[0][h]), ks[0]); PIN(K[1][h]);
      u32x2 qt = pack4(aQ[1][h]);
      unsigned e = ((h < 2) == g0) ? qs[1][h & 1] : 0u;
      Q[1][h] = (h & 1) ? frag{qt[0], qt[1], 0u, e} : frag{qt[0], qt[1], e, 0u}; PIN(Q[1][h]);
    }
    SCHED0();
    GAS u64* myflag = (GAS u64*)(ws + FLAG_OFF + ((grp >> 2) * 64 + tile * 4 + cw) * FS1 + (grp & 3) * FS2);
    PINS(myflag);
    WAIT_LGKM0();
    STAMP(23);
    BARRIER();
    STAMP(24);
    SCHED0();
    u32x2 gz;
    asm volatile("ds_read2_b32 %0, %1 offset0:0 offset1:16" : "=v"(gz) : "v"(exgr) : "memory");
#pragma unroll
    for (int h = 0; h < 4; ++h) asm volatile("ds_read2st64_b64 %0, %1 offset0:%2 offset1:4" : "=v"(K[0][h]) : "v"(exr), "i"(h) : "memory");
    f32x4 sc[2][4], sp[2][4];
#pragma unroll
    for (int h = 0; h < 4; ++h) {
      sc[1][h] = MFMA(K[2][h], Q[1][h], maskC); PIN(sc[1][h]);
      sp[1][h] = MFMA(K[1][h], Q[1][h], maskP); PIN(sp[1][h]);
      u32x2 qt = pack4(aQ[0][h]);
      unsigned e = ((h < 2) == g0) ? qs[0][h & 1] : 0u;
      Q[0][h] = (h & 1) ? frag{qt[0], qt[1], 0u, e} : frag{qt[0], qt[1], e, 0u}; PIN(Q[0][h]);
      SCHED0();
    }
    asm volatile("s_waitcnt lgkmcnt(0)" : "+v"(gz), "+v"(K[0][0]), "+v"(K[0][1]), "+v"(K[0][2]), "+v"(K[0][3])::"memory");
    float v[2][4][2];
#pragma unroll
    for (int h = 0; h < 4; ++h) {
      sc[0][h] = MFMA(K[1][h], Q[0][h], maskC); PIN(sc[0][h]);
      sp[0][h] = MFMA(K[0][h], Q[0][h], maskP0); PIN(sp[0][h]);
      v[1][h][0] = fmx(fmx3(sp[1][h][0], sp[1][h][1], sc[1][h][0]), sc[1][h][1]); PIN(v[1][h][0]);
      v[1][h][1] = fmx(fmx3(sp[1][h][2], sp[1][h][3], sc[1][h][2]), sc[1][h][3]); PIN(v[1][h][1]);
      SCHED0();
    }
#pragma unroll
    for (int f = 0; f < 5; ++f) {
      u32x2 a = pack4(aV[0][f]); Vb[f][0] = a[0]; Vb[f][1] = a[1]; PIN(Vb[f]);
      Va[f] = __builtin_shufflevector(Vb[f], Vb[f], -1, -1, 0, 1); PIN(Va[f]);
    }
#pragma unroll
    for (int f = 0; f < 5; ++f) { u32x2 n = *(volatile LDSP u32x2*)(size_t)(exr + (5 + f) * 512); Va[f][0] = n[0]; Va[f][1] = n[1]; }
    u32x4 pk[2];
    auto softmax = [&](float (&w)[4][2], float gate_logit) {
      swap32(w[0][0], w[2][0]); swap32(w[1][0], w[3][0]); swap32(w[0][1], w[2][1]); swap32(w[1][1], w[3][1]);
      swap16(w[0][0], w[1][0]); swap16(w[2][0], w[3][0]); swap16(w[0][1], w[1][1]); swap16(w[2][1], w[3][1]);
      const float cs = 0.23570226039551584f * 1.4426950408889634f;
      float m = fmx(fmx3(fmx3(w[0][0], w[1][0], w[2][0]), w[3][0], w[0][1]), fmx3(w[1][1], w[2][1], w[3][1]));
      f32x2 e[4];
#pragma unroll
      for (int i = 0; i < 4; ++i) {
        f32x2 a = f32x2{w[i][0], w[i][1]} - m;
        PIN(a);
        a *= cs;
        e[i] = f32x2{__builtin_amdgcn_exp2f(a[0]), __builtin_amdgcn_exp2f(a[1])};
      }
      f32x2 s2 = (e[0] + e[1]) + (e[2] + e[3]);
      float s = s2[0] + s2[1];
      float r = __builtin_amdgcn_rcpf(__builtin_fmaf(s, __builtin_amdgcn_exp2f(gate_logit * sg), s));
      float pf[4];
#pragma unroll
      for (int i = 0; i < 4; ++i) { f32x2 pp = e[i] * r; pf[i] = __uint_as_float(cvtpk(pp[0], pp[1])); }
      swap32(pf[0], pf[2]); swap32(pf[1], pf[3]);
      swap16(pf[0], pf[1]); swap16(pf[2], pf[3]);
      return u32x4{__float_as_uint(pf[0]), __float_as_uint(pf[1]), __float_as_uint(pf[2]), __float_as_uint(pf[3])};
    };
    SCHED0();
#pragma unroll
    for (int h = 0; h < 4; ++h) {
      v[0][h][0] = fmx(fmx3(sp[0][h][0], sp[0][h][1], sc[0][h][0]), sc[0][h][1]);
      v[0][h][1] = fmx(fmx3(sp[0][h][2], sp[0][h][3], sc[0][h][2]), sc[0][h][3]);
    }
    pk[1] = softmax(v[1], __uint_as_float(gz[1]));
#pragma unroll
    for (int h = 0; h < 4; ++h) { PIN(v[0][h][0]); PIN(v[0][h][1]); }
    SCHED0();
    pv_store(pk[1], Vb, sel, vmain, vleft, ors, osoff1, oloff1, g0);
    pk[0] = softmax(v[0], __uint_as_float(gz[0]));
    SCHED0();
    pv_store(pk[0], Va, sel, vmain, vleft, ors, osoff, oloff, g0);
    STAMP(25);
    tag = __hip_atomic_load((GAS u64*)(ws + EPOCH_OFF), RLX_AGENT);
    WAIT_VM0();
    TAG_SCALAR();
    if (lane == 0) __hip_atomic_store(myflag, tag, RLX_AGENT);
    STAMP(26);
    RSTAMP(61);
    asm volatile(".Lfk_tail:");
  }
  BARRIER();
  if (!act2) {
    if (bid == 255) {
      if (wave < 4) {
        GAS u64* fp = (GAS u64*)(ws + FLAG_OFF + (wave * 64 + lane) * FS1);
        __builtin_amdgcn_s_sleep(40);
#pragma clang loop unroll(disable)
        for (unsigned spins = 0; spins < (1u << 17); ++spins) {
          bool ok = true;
#pragma unroll
          for (int k = 0; k < 4; ++k) ok &= __hip_atomic_load(fp + k, RLX_AGENT) == tag;
          if (__all(ok)) break;
          __builtin_amdgcn_s_sleep(8);
        }
      }
      BARRIER();
      if (tid == 0) __hip_atomic_store((GAS u64*)(ws + EPOCH_OFF), tag, RLX_AGENT);
    }
    return;
  }
  if (cw == 3) {
#pragma unroll
    for (int j = 0; j < 4; ++j) { wo_piece(3, 7, j); wo_piece(3, 8, j); }
  }
  if (wave < 4) phase2<2>(L, ws, ors, tag, rt * 5 + 3, 48, cw, rot2, lane STAMP_PASS);
  else phase2<3>(L, ws, ors, tag, rt * 5, 0, cw, rot2, lane STAMP_PASS);
  STAMP(30);
  if (wave < 5 && rt * 5 + wave < 128) {
    f32x4 s[2][2];
#pragma unroll
    for (int it = 0; it < 2; ++it) {
      const int r = it * 8 + vl;
      const unsigned a0 = L + (wave * 16 + r) * 256 + ((((lane & 7) * 2) ^ r) * 16);
#pragma unroll
      for (int w = 0; w < 4; ++w) {
        f32x4 u0 = *(LDSP f32x4*)(size_t)(a0 + w * 36864), u1 = *(LDSP f32x4*)(size_t)((a0 ^ 16) + w * 36864);
        s[it][0] = w ? s[it][0] + u0 : u0; s[it][1] = w ? s[it][1] + u1 : u1;
      }
    }
#pragma unroll
    for (int it = 0; it < 2; ++it) {
      u32x4 o = {cvtpk(s[it][0][0], s[it][0][1]), cvtpk(s[it][0][2], s[it][0][3]), cvtpk(s[it][1][0], s[it][1][1]), cvtpk(s[it][1][2], s[it][1][3])};
      *(GAS u32x4*)((GAS char*)out + (size_t)(rt * 80 + wave * 16 + it * 8 + vl) * XROW + ct * 128 + (lane & 7) * 16) = o;
    }
  }
  STAMP(31);
  RSTAMP(63);
  STAMP_OUT();
}

#undef x
#undef wA
#undef wB
#undef woA
#undef woB
#undef gw
#undef ws
#undef out
#undef ors

extern "C" void kernel_launch(void* const* d_in, const int* in_sizes, int n_in, void* d_out, int out_size, void* d_ws, size_t ws_size,
                              hipStream_t stream) {
  fused_kernel<<<256, 512, 0, stream>>>((const u16*)d_in[0], (const u16*)d_in[1], (const u16*)d_in[4], (const u16*)d_in[2], (const u16*)d_in[5],
                                        (const u16*)d_in[6], (char*)d_ws, (u16*)d_out);
}
```

```cpp
#include <hip/hip_runtime.h>
#include <hip/hip_bf16.h>
#include <stdint.h>
#include <type_traits>

typedef unsigned short u16;
typedef unsigned long long u64;
typedef short s16x4 __attribute__((ext_vector_type(4)));
typedef float f32x2 __attribute__((ext_vector_type(2)));
typedef float f32x4 __attribute__((ext_vector_type(4)));
typedef unsigned u32x2 __attribute__((ext_vector_type(2)));
typedef unsigned u32x4 __attribute__((ext_vector_type(4)));
typedef u32x4 frag;
typedef __bf16 bf16x2 __attribute__((ext_vector_type(2)));
typedef __bf16 bfx8 __attribute__((ext_vector_type(8)));
#define LDSP __attribute__((address_space(3)))
#define GAS __attribute__((address_space(1)))
#define DI __device__ __forceinline__

constexpr int WROW = 3456;
constexpr int XROW = 1152;
constexpr int PITCH = 432;
constexpr int KO = 32 * PITCH;
constexpr int STG_W = 64 * PITCH;
constexpr int STG = STG_W + 144 * 128;
constexpr int G_OFF = 3 * STG;
constexpr int SCR_OFF = G_OFF + 4608;
constexpr int EX_OFF = 143360;
constexpr int LDS_BYTES = 163840;
constexpr int O_BYTES = 128 * 36 * 1024;
constexpr int FLAG_OFF = 8 << 20;
constexpr int EPOCH_OFF = FLAG_OFF + (1 << 20);
#define ROT 11
#define PRE0 5
#define NBX 22
#define FS1 128
#define FS2 8
#define PSLEEP 1
#define CODEPF 22656
#define NB1 5

#define MFMA(a, b, c) __builtin_amdgcn_mfma_f32_16x16x32_bf16(__builtin_bit_cast(bfx8, (a)), __builtin_bit_cast(bfx8, (b)), (c), 0, 0, 0)
#define RLX_AGENT __ATOMIC_RELAXED, __HIP_MEMORY_SCOPE_AGENT
#define HOFF(h) (36 * (h) + ((h) & 1) * 4)

DI void glds16(unsigned voff, const GAS void* sbase, unsigned lds_addr) {
  asm volatile("s_mov_b32 m0, %2\n\ts_nop 0\n\tglobal_load_lds_dwordx4 %0, %1" ::"v"(voff), "s"(sbase), "s"(lds_addr) : "memory");
}
DI u32x2 trd(unsigned a) { return __builtin_bit_cast(u32x2, __builtin_amdgcn_ds_read_tr16_b64_v4i16((LDSP s16x4*)(size_t)a)); }
DI frag ldW(unsigned a0, unsigned a1) {
  u32x2 lo = trd(a0), hi = trd(a1);
  return __builtin_shufflevector(lo, hi, 0, 1, 2, 3);
}
DI frag ld128(unsigned a) { return *(LDSP frag*)(size_t)a; }
DI u32x2 ld64(unsigned a) { return *(LDSP u32x2*)(size_t)a; }
DI void st64(unsigned a, u32x2 v) { *(LDSP u32x2*)(size_t)a = v; }
DI unsigned cvtpk(float a, float b) {
  f32x2 f = {a, b};
  return __builtin_bit_cast(unsigned, __builtin_convertvector(f, bf16x2));
}
DI u32x2 pack4(f32x4 a) { return u32x2{cvtpk(a[0], a[1]), cvtpk(a[2], a[3])}; }
DI frag cat(u32x2 a, u32x2 b) { return __builtin_shufflevector(a, b, 0, 1, 2, 3); }
DI void swap32(float& a, float& b) {
  auto r = __builtin_amdgcn_permlane32_swap(__float_as_uint(a), __float_as_uint(b), false, false);
  a = __uint_as_float(r[0]); b = __uint_as_float(r[1]);
}
DI void swap16(float& a, float& b) {
  auto r = __builtin_amdgcn_permlane16_swap(__float_as_uint(a), __float_as_uint(b), false, false);
  a = __uint_as_float(r[0]); b = __uint_as_float(r[1]);
}
DI float fmx(float a, float b) { return __builtin_elementwise_maximum(a, b); }
DI float fmx3(float a, float b, float c) { return fmx(fmx(a, b), c); }
#define STAMP(i)
#define RSTAMP(i)
#define STAMP_OUT()
#define STAMP_ARG
#define STAMP_PASS
#define STAMP_DECL
#define SGB(m, n) __builtin_amdgcn_sched_group_barrier(m, n, 0)
#define SCHED0() __builtin_amdgcn_sched_barrier(0)
#define WAIT_LGKM0() do { __builtin_amdgcn_s_waitcnt(0xC07F); asm volatile("" ::: "memory"); } while (0)
#define WAIT_VM0() asm volatile("s_waitcnt vmcnt(0)" ::: "memory")
#define BARRIER() __builtin_amdgcn_s_barrier()
#define PIN(x) asm volatile("" : "+v"(x))
#define TAG_SCALAR() do { unsigned lo_ = __builtin_amdgcn_readfirstlane((unsigned)tag), hi_ = __builtin_amdgcn_readfirstlane((unsigned)(tag >> 32)); \
    tag = (((u64)hi_ << 32) | lo_) + 1; asm volatile("" : "+s"(tag)); } while (0)
#define PINS(x) asm volatile("" : "+s"(x))
#define WR2(addr, a, b, o0, o1) asm volatile("ds_write2st64_b64 %0, %1, %2 offset0:%3 offset1:%4" ::"v"(addr), "v"(a), "v"(b), "i"(o0), "i"(o1) : "memory")

DI void pv_store(u32x4 pk, const frag (&V)[5], u32x4 sel, unsigned vmain, unsigned vleft, __amdgpu_buffer_rsrc_t ors, unsigned so, unsigned sl, bool g0) {
  const f32x4 z = {0.f, 0.f, 0.f, 0.f};
  frag P[4];
  f32x4 o[4], os[4];
  auto mkP = [&](int h) {
    unsigned u = pk[h];
    P[h] = frag{__builtin_amdgcn_perm(u, u, sel[0]), __builtin_amdgcn_perm(u, u, sel[1]), __builtin_amdgcn_perm(u, u, sel[2]), __builtin_amdgcn_perm(u, u, sel[3])};
    PIN(P[h]);
  };
  mkP(0); mkP(1);
  SCHED0();
  u32x4 f0, f1;
  os[0] = MFMA(V[4], P[0], z); PIN(os[0]); o[0] = MFMA(V[0], P[0], z); PIN(o[0]); mkP(2); SCHED0();
  os[1] = MFMA(V[4], P[1], z); PIN(os[1]); o[1] = MFMA(V[1], P[1], z); PIN(o[1]); mkP(3); SCHED0();
  os[2] = MFMA(V[4], P[2], z); PIN(os[2]); os[3] = MFMA(V[4], P[3], z); PIN(os[3]);
  o[2] = MFMA(V[2], P[2], z); PIN(o[2]); o[3] = MFMA(V[3], P[3], z); PIN(o[3]); SCHED0();
  f0 = u32x4{cvtpk(o[0][0], o[0][1]), cvtpk(o[0][2], o[0][3]), cvtpk(o[1][0], o[1][1]), cvtpk(o[1][2], o[1][3])};
  PIN(f0); SCHED0();
  __builtin_amdgcn_raw_buffer_store_b128(f0, ors, vmain, so, 16);
  SCHED0();
  u32x2 f2 = {cvtpk(g0 ? os[0][0] : os[2][0], g0 ? os[0][1] : os[2][1]), cvtpk(g0 ? os[1][2] : os[3][2], g0 ? os[1][3] : os[3][3])};
  PIN(f2); SCHED0();
  f1 = u32x4{cvtpk(o[2][0], o[2][1]), cvtpk(o[2][2], o[2][3]), cvtpk(o[3][0], o[3][1]), cvtpk(o[3][2], o[3][3])};
  PIN(f1); PIN(f0); SCHED0();
  __builtin_amdgcn_raw_buffer_store_b128(f1, ors, vmain + 1024, so, 16);
  __builtin_amdgcn_raw_buffer_store_b64(f2, ors, vleft, sl, 16);
  SCHED0();
}

template <int MT>
DI void phase2(unsigned L, GAS char* ws, __amdgpu_buffer_rsrc_t ors, u64 tag, int tt0, int mrow, int kq, int rot2, int lane STAMP_ARG) {
  const int g = lane >> 4, q = (lane >> 2) & 3, p = lane & 3, c = lane & 15;
  const unsigned sw = (2 * g + (q >> 1)) & 3;
  unsigned bw[4];
#pragma unroll
  for (int n = 0; n < 4; ++n) bw[n] = L + kq * 36864 + (4 * g + q) * 128 + 8 * p + ((n ^ sw) * 32);
  frag wf[9][4];
  auto rdw = [&](int i) {
#pragma unroll
    for (int n = 0; n < 4; ++n) wf[i][n] = ldW(bw[n] + i * 4096, bw[n] + i * 4096 + 2048);
  };
  rdw(0); rdw(1); rdw(2); rdw(3); rdw(4);
  SCHED0();
  unsigned so[9], vo[MT];
#pragma unroll
  for (int i = 0; i < 9; ++i) so[i] = (i < 8 ? 8 * kq + ((i + rot2) & 7) : 32 + kq) * 1024;
#pragma unroll
  for (int m = 0; m < MT; ++m) { int tt = tt0 + m; tt = tt > 127 ? 127 : tt; vo[m] = lane * 16 + tt * 36864; }
  int tf = (lane & 4) ? tt0 + MT - 1 : tt0; tf = tf > 127 ? 127 : tf;
  GAS u64* fp = (GAS u64*)(ws + FLAG_OFF + (kq * 64 + (tf >> 1)) * FS1 + (lane & 3) * FS2);
  unsigned rb[4];
#pragma unroll
  for (int n = 0; n < 4; ++n) rb[n] = L + kq * 36864 + (mrow + c) * 256 + (((4 * n + g) ^ c) * 16);
#pragma unroll
  for (int i = 0; i < 9; ++i) PINS(so[i]);
#pragma unroll
  for (int m = 0; m < MT; ++m) PIN(vo[m]);
#pragma unroll
  for (int n = 0; n < 4; ++n) PIN(rb[n]);
  PIN(fp);
  WAIT_LGKM0();
  BARRIER();
  STAMP(27);
#pragma clang loop unroll(disable)
  for (unsigned spins = 0; spins < (1u << 17); ++spins) {
    u64 v = __hip_atomic_load(fp, RLX_AGENT);
    if (__all(v == tag)) break;
    __builtin_amdgcn_s_sleep(PSLEEP);
  }
  __builtin_amdgcn_fence(__ATOMIC_ACQUIRE, "wavefront");
  asm volatile("" ::: "memory");
  STAMP(28);
  RSTAMP(62);
  frag of[9][MT];
  constexpr int WIN = 2;
#pragma unroll
  for (int i = 0; i < WIN; ++i)
#pragma unroll
    for (int m = 0; m < MT; ++m) { of[i][m] = __builtin_amdgcn_raw_buffer_load_b128(ors, vo[m], so[i], 16); SCHED0(); }
  f32x4 acc[MT][4];
#pragma unroll
  for (int i = 0; i < 8; ++i) {
    const bool rd = i >= 2 && i + 3 < 9;
    if (rd) rdw(i + 3);
#pragma unroll
    for (int m = 0; m < MT; ++m) {
#pragma unroll
      for (int n = 0; n < 4; ++n) acc[m][n] = MFMA(wf[i][n], of[i][m], i == 0 ? (f32x4{0, 0, 0, 0}) : acc[m][n]);
      if (i + WIN < 9) of[i + WIN][m] = __builtin_amdgcn_raw_buffer_load_b128(ors, vo[m], so[i + WIN], 16);
    }
#pragma unroll
    for (int m = 0; m < MT; ++m) {
#pragma unroll
      for (int n = 0; n < 4; ++n) { SGB(0x8, 1); if (m < 2 && rd) SGB(0x100, 1); }
      if (i + WIN < 9) SGB(0x20, 1);
    }
    SCHED0();
  }
#pragma unroll
  for (int m = 0; m <= MT; ++m) {
    if (m < MT) {
#pragma unroll
      for (int n = 0; n < 4; ++n) { acc[m][n] = MFMA(wf[8][n], of[8][m], acc[m][n]); PIN(acc[m][n]); }
    }
    SCHED0();
    if (m > 0) {
#pragma unroll
      for (int n = 0; n < 4; ++n) *(LDSP f32x4*)(size_t)(rb[n] + (m - 1) * 4096) = acc[m - 1][n];
    }
    SCHED0();
  }
  STAMP(29);
}

typedef u64 u64x8 __attribute__((ext_vector_type(8)));
extern "C" __global__ __launch_bounds__(512, 2) void fused_kernel(const u16* x_, const u16* wA_, const u16* wB_, const u16* woA_, const u16* woB_,
                                                                   const u16* gw_, char* ws_, u16* out_) {
  __shared__ __attribute__((aligned(1024))) char lds[LDS_BYTES];
  const unsigned L = (unsigned)(size_t)(LDSP char*)lds;
  const int BIDX = blockIdx.x, TIDX = threadIdx.x;
  u64x8 ka;
  asm volatile("s_load_dwordx16 %0, %1, 0x0" : "=s"(ka) : "s"(__builtin_amdgcn_kernarg_segment_ptr()));
#define KARG_WAIT() do { SCHED0(); asm volatile("s_waitcnt lgkmcnt(0)" ::: "memory"); SCHED0(); asm volatile("" : "+s"(ka)); } while (0)
#define x ((const GAS u16*)ka[0])
#define wA ((const GAS u16*)ka[1])
#define wB ((const GAS u16*)ka[2])
#define woA ((const GAS u16*)ka[3])
#define woB ((const GAS u16*)ka[4])
#define gw ((const GAS u16*)ka[5])
#define ws ((GAS char*)ka[6])
#define out ((GAS u16*)ka[7])
#define ors __builtin_amdgcn_make_buffer_rsrc((void*)ka[6], 0, O_BYTES, 0x00020000)
  const int bid = BIDX;
  const int xcd = bid & 7, idx = bid >> 3;
  const int tl = idx & 7, gl = idx >> 3;
  const int tile = (xcd & 1) * 8 + tl;
  const int grp = (xcd >> 1) * 4 + gl;
  int rot = ROT == 1 ? tl + 2 * gl : ROT == 2 ? (tl >> 1) * 2 + 2 * gl : ROT == 6 ? (tl % 3) * 3 + (gl % 3) * 3 : ROT == 7 ? (((tl >> 1) + gl) & 3) * 2 + (tl & 1 & (gl >> 1))
          : ROT == 8 ? (((tl + gl) & 3) * 9) >> 2 : ROT == 9 ? ((tl + gl + (xcd >> 1)) % 3) * 3 : ROT == 10 ? (((tl + gl) & 3) * 9 >> 2) + (xcd & 1)
          : ROT == 11 ? ((tl + gl) & 3) * 2 + ((xcd ^ (xcd >> 1)) & 1) : ROT == 12 ? ((tl + gl) % 3) * 3 + ((xcd & 1) + (xcd >> 1)) % 3
          : ROT == 13 ? ((tl + gl) & 1) * 4 + (((xcd & 1) * 2 + (xcd >> 1)) & 3) : ROT == 14 ? (tl + 2 * gl + ((xcd >> 1) & 1)) % 9
          : ROT == 15 ? (((tl + gl) & 3) * 2 + ((xcd ^ (xcd >> 1)) & 1) + (xcd >> 2)) : 0;
  if (rot >= 9) rot -= 9;
  const int tid = TIDX, lane = tid & 63;
  const int wave = __builtin_amdgcn_readfirstlane(tid >> 6);
  const int vl = lane >> 3;
  STAMP_DECL;
  STAMP(0);
  RSTAMP(60);
  if (tid < 7) {
    u64 pc0;
    unsigned len;
    asm volatile("s_getpc_b64 %0\n\ts_mov_b32 %1, .Lfk_tail-fused_kernel+128" : "=s"(pc0), "=s"(len));
    unsigned off = (idx * 7 + tid) * 128;
    off = off < len ? off : 0;
    asm volatile("s_mov_b32 m0, %2\n\ts_nop 0\n\tglobal_load_lds_dword %0, %1" ::"v"(off), "s"(pc0 & ~255ull), "s"(L + SCR_OFF) : "memory");
  }
  u64 tag;
  const int lin0 = xcd * 30 + idx;
  const bool act2 = idx < 30 && lin0 < 234;
  const int lin = act2 ? lin0 : 0;
  const int rt = (lin * 57) >> 9, ct = lin - rt * 9;
  const int rot2 = 3 * (rt - ((rt * 171) >> 9) * 3);
  const float sg = (grp >= 8) ? 1.4426950408889634f : -1.4426950408889634f;
  const unsigned wsch = ((((lane & 7) >> 1) ^ ((vl >> 1) & 3)) << 5) | ((lane & 1) << 4);
  const unsigned offn = vl * XROW + wsch, offl = ((vl >> 2) * 72 + (vl & 3)) * XROW + wsch;
  auto wo_piece = [&](int kq, int i, int j) {
    const GAS char* b = (const GAS char*)(kq >= 2 ? woB : woA) + ct * 128 + (kq & 1) * (288 * XROW);
    if (i == 8) b += ((j & 1) * 144 + ((j >> 1) ? 52 : 16)) * XROW;
    else b += (((i + rot2) & 7) * 36 + (j >> 1) * 20 + 8 * (j & 1)) * XROW;
    glds16(i == 8 ? offl : offn, b, L + (kq * 9 + i) * 4096 + j * 1024);
  };
  const int g = lane >> 4, q = (lane >> 2) & 3, p = lane & 3, c = lane & 15;
  const int cw = wave & 3;

  if (__builtin_expect(wave >= 4, 1)) {
    __builtin_amdgcn_s_setprio(2);
    const int lw = cw, tl256 = tid - 256;
    const unsigned offx = (tile * 128 + 8 * lw + vl) * XROW + ((lane & 7) ^ ((4 * lw + (vl >> 1)) & 7)) * 16;
    KARG_WAIT();
    const GAS char* w = (const GAS char*)((grp >= 8) ? wB : wA) + (grp & 7) * 144;
    unsigned offw[7], offh;
    auto kchunk = [&](int st) { int kc = st + rot; return kc >= 9 ? kc - 9 : kc; };
    auto issueX = [&](int kc, unsigned sb, int jj) { glds16(offx, (const GAS char*)x + kc * 128 + jj * (32 * XROW), sb + STG_W + (2 + lw + 4 * jj) * 1024); };
    auto issueW = [&](int kc, unsigned sb, int j) { glds16(offw[j], w + kc * (64 * WROW), sb + (lw + 4 * j) * 1024); };
    auto issue6 = [&](int kc, unsigned sb) {
      if (lw < 3) issueW(kc, sb, 6);
      else { glds16(offw[6], (const GAS char*)x + kc * 128, sb + STG_W); glds16(offh, (const GAS char*)x + kc * 128, sb + STG_W + 1024); }
    };
    const int kc0 = rot, kc1 = kchunk(1);
    const unsigned sb0 = L, sb1 = L + STG;
    auto calcw = [&](int j) {
      unsigned qq = 256 * j + tl256;
      unsigned u = (qq * 2428u) >> 16, cc = qq - u * 27;
      unsigned row = (u & 0x38) | ((u & 1) << 2) | ((u >> 1) & 3);
      offw[j] = row * WROW + cc * 16 + ((cc * 57u) >> 9) * 1008;
    };
    issueX(kc0, sb0, 0);
    u32x2 gv;
    const unsigned gi = tl256 < 144 ? tl256 : 143;
    asm volatile("global_load_dwordx2 %0, %1, %2" : "=v"(gv) : "v"(gi * 8), "s"(gw) : "memory");
#pragma unroll
    for (int j = 0; j < 6; ++j) {
      SCHED0();
      calcw(j);
      SCHED0();
      issueW(kc0, sb0, j);
      if (j < 3) issueX(kc0, sb0, j + 1);
    }
    calcw(6);
    {
      int t0 = tile * 128 - 16 + vl, t1 = t0 + 8;
      t0 = t0 < 0 ? 0 : t0; t1 = t1 < 0 ? 0 : t1;
      unsigned h0 = t0 * XROW + ((lane & 7) ^ (vl >> 1)) * 16;
      offh = t1 * XROW + ((lane & 7) ^ (4 + (vl >> 1))) * 16;
      offw[6] = lw == 3 ? h0 : offw[6];
    }
    issue6(kc0, sb0);
    STAMP(1);
    issueW(kc1, sb1, 0);
    int slot = 0, kn = kc1;
    const GAS char* xp = (const GAS char*)x + kn * 128;
    unsigned b1 = sb1, xd = sb1 + STG_W + (2 + lw) * 1024;
    PINS(xp); PINS(xd);
    SCHED0();
#pragma unroll
    for (int j = 1; j < PRE0; ++j) issueW(kc1, sb1, j);
    STAMP(2);
    asm volatile("s_waitcnt vmcnt(%1)" : "+v"(gv) : "i"(10 + PRE0) : "memory");
    {
      unsigned a0 = __builtin_amdgcn_perm(gv[0], gv[0], 0x01000100), a1 = __builtin_amdgcn_perm(gv[0], gv[0], 0x03020302);
      unsigned a2 = __builtin_amdgcn_perm(gv[1], gv[1], 0x01000100), a3 = __builtin_amdgcn_perm(gv[1], gv[1], 0x03020302);
      *(LDSP u32x4*)(size_t)(L + G_OFF + gi * 32) = u32x4{a0, a0, a1, a1};
      *(LDSP u32x4*)(size_t)(L + G_OFF + gi * 32 + 16) = u32x4{a2, a2, a3, a3};
    }
    WAIT_LGKM0();
    asm volatile("s_waitcnt vmcnt(%0)" ::"i"(PRE0) : "memory");
#pragma clang loop unroll(disable)
    for (int st = 0; st < 7; ++st) {
      BARRIER();
      glds16(offx, xp, xd);
      SCHED0();
      STAMP(3 + st);
      glds16(offx, xp + 32 * XROW, xd + 4096);
      if (st == 0) {
#pragma unroll
        for (int j = PRE0; j < 6; ++j) issueW(kc1, sb1, j);
        tag = __hip_atomic_load((GAS u64*)(ws + EPOCH_OFF), RLX_AGENT);
      }
      issue6(kn, b1);
      glds16(offx, xp + 64 * XROW, xd + 8192);
      glds16(offx, xp + 96 * XROW, xd + 12288);
      SCHED0();
      kn = kn == 8 ? 0 : kn + 1;
      b1 = L + (slot == 0 ? 2 : slot - 1) * STG;
      issueW(kn, b1, 0); issueW(kn, b1, 1);
#pragma unroll
      for (int j = 2; j < 6; ++j) issueW(kn, b1, j);
      slot = slot == 2 ? 0 : slot + 1;
      xp = (const GAS char*)x + kn * 128;
      xd = b1 + STG_W + (2 + lw) * 1024;
      PINS(xp); PINS(xd);
      asm volatile("s_waitcnt vmcnt(6)" ::: "memory");
    }
    BARRIER();
    glds16(offx, xp, xd);
    SCHED0();
    STAMP(10);
    glds16(offx, xp + 32 * XROW, xd + 4096);
    issue6(kn, b1);
    glds16(offx, xp + 64 * XROW, xd + 8192);
    glds16(offx, xp + 96 * XROW, xd + 12288);
#pragma unroll
    for (int I = 0; I < 6; ++I) wo_piece(0, I, lw);
    asm volatile("s_waitcnt vmcnt(6)" ::: "memory");
    BARRIER();
    STAMP(11);
    wo_piece(0, 6, lw); wo_piece(0, 7, lw);
    __builtin_amdgcn_s_setprio(0);
#pragma unroll
    for (int I = 8; I < NBX; ++I) wo_piece(I / 9, I % 9, lw);
    WAIT_LGKM0();
    STAMP(12);
    BARRIER();
    STAMP(13);
#pragma unroll
    for (int I = NBX; I < 34; ++I) wo_piece(I / 9, I % 9, lw);
    WAIT_VM0();
    TAG_SCALAR();
    STAMP(14);
  } else {
    const unsigned wrow = L + (8 * g + 2 * q) * PITCH;
    f32x4 maskP, maskC, maskP0;
    {
      const float ninf = -__builtin_inff();
#pragma unroll
      for (int r = 0; r < 4; ++r) {
        bool pm = (r & 1) == (c & 1);
        maskC[r] = (pm && 4 * g + r <= c) ? 0.f : ninf;
        maskP[r] = (pm && 4 * g + r > c) ? 0.f : ninf;
      }
      maskP0 = maskP + ((tile == 0 && wave == 0) ? -998244352.f * 4.242640687119285f : 0.f);
      asm volatile("" : "+v"(maskP), "+v"(maskC), "+v"(maskP0));
    }
    u32x4 sel;
    {
      const int par = c & 1;
      const bool cur1 = 4 * g + par <= c, cur2 = 4 * g + 2 + par <= c;
      const unsigned s1 = par ? 0x01000c0cu : 0x0c0c0100u;
      const unsigned s2 = par ? 0x03020c0cu : 0x0c0c0302u;
      sel[0] = cur1 ? 0x0c0c0c0cu : s1;
      sel[1] = cur2 ? 0x0c0c0c0cu : s2;
      sel[2] = cur1 ? s1 : 0x0c0c0c0cu;
      sel[3] = cur2 ? s2 : 0x0c0c0c0cu;
    }
    unsigned vmain = lane * 16, vleft = lane < 32 ? c * 16 + g * 8 : 0x40000000u;
    unsigned osoff = (tile * 8 + 2 * wave) * 36864 + grp * 2048, oloff = (tile * 8 + 2 * wave) * 36864 + 32768 + grp * 256;
    unsigned osoff1 = osoff + 36864, oloff1 = oloff + 36864;
    PINS(osoff); PINS(oloff); PINS(osoff1); PINS(oloff1);
    asm volatile("" : "+v"(sel), "+v"(vmain), "+v"(vleft));
    f32x4 aK[2][4], aQ[2][4], aKQ[2], aV[2][5], hk, hv, he;
    frag cv[5], xb[3];
    unsigned exr = L + EX_OFF + wave * 5120 + lane * 8, exh = L + EX_OFF + wave * 512 + lane * 8;
    unsigned exgw = L + SCR_OFF + wave * 128 + g * 16, exgr = L + SCR_OFF + wave * 128 + c * 4;
    PIN(exr); PIN(exh); PIN(exgw); PIN(exgr);
    auto mainloop = [&](auto WC) {
      constexpr int W = decltype(WC)::value;
      constexpr int XO = (1 + 2 * W) * 2048;
    hk = f32x4{0, 0, 0, 0}; hv = f32x4{0, 0, 0, 0}; he = f32x4{0, 0, 0, 0};
    PIN(he);
#pragma unroll
    for (int t = 0; t < 2; ++t) {
#pragma unroll
      for (int f = 0; f < 4; ++f) { aK[t][f] = f32x4{0, 0, 0, 0}; aQ[t][f] = f32x4{0, 0, 0, 0}; }
      aKQ[t] = f32x4{0, 0, 0, 0};
#pragma unroll
      for (int f = 0; f < 5; ++f) aV[t][f] = f32x4{0, 0, 0, 0};
    }
    unsigned cW = wrow + 8 * p;
    unsigned cKQ = wrow + (p < 2 ? 144 : 0) + ((p & 1) ? 104 : 32);
    unsigned cX0 = L + STG_W + c * 128 + ((g ^ (c >> 1)) * 16), cX1 = cX0 ^ 64;
    unsigned cV[4];
#pragma unroll
    for (int i = 0; i < 4; ++i)
      cV[i] = p < 2 ? wrow + 288 + (p ? 104 : 32) + (i >> 1) * KO + (i & 1) * PITCH : L + G_OFF + rot * 512 + (8 * g + q) * 8 + (i >> 1) * 256 + (i & 1) * 32;
#pragma unroll
    for (int f = 0; f < 5; ++f) cv[f] = frag{0, 0, 0, 0};
    xb[0] = frag{0, 0, 0, 0}; xb[1] = frag{0, 0, 0, 0}; xb[2] = frag{0, 0, 0, 0};
    asm volatile("" : "+v"(aK[0][0]), "+v"(aK[0][1]), "+v"(aK[0][2]), "+v"(aK[0][3]), "+v"(aK[1][0]), "+v"(aK[1][1]), "+v"(aK[1][2]), "+v"(aK[1][3]),
                      "+v"(aQ[0][0]), "+v"(aQ[0][1]), "+v"(aQ[0][2]), "+v"(aQ[0][3]), "+v"(aQ[1][0]), "+v"(aQ[1][1]), "+v"(aQ[1][2]), "+v"(aQ[1][3]),
                      "+v"(aKQ[0]), "+v"(aKQ[1]), "+v"(hk), "+v"(hv), "+v"(xb[0]), "+v"(xb[1]), "+v"(xb[2]));
    asm volatile("" : "+v"(aV[0][0]), "+v"(aV[0][1]), "+v"(aV[0][2]), "+v"(aV[0][3]), "+v"(aV[0][4]), "+v"(aV[1][0]), "+v"(aV[1][1]), "+v"(aV[1][2]),
                      "+v"(aV[1][3]), "+v"(aV[1][4]), "+v"(cv[0]), "+v"(cv[1]), "+v"(cv[2]), "+v"(cv[3]), "+v"(cv[4]),
                      "+v"(cW), "+v"(cKQ), "+v"(cX0), "+v"(cX1), "+v"(cV[0]), "+v"(cV[1]), "+v"(cV[2]), "+v"(cV[3]));
    SCHED0();
    STAMP(1);
    KARG_WAIT();
    tag = __hip_atomic_load((GAS u64*)(ws + EPOCH_OFF), RLX_AGENT);
    STAMP(2);
    WAIT_LGKM0();
      int slot = 0, kc = rot;
#pragma clang loop unroll(disable)
      for (int st = 0; st < 9; ++st) {
        BARRIER();
        SCHED0();
        frag xa[3], W0[14], W1[9];
        xa[0] = ld128(cX0 + XO); xa[1] = ld128(cX0 + XO + 2048);
#pragma unroll
        for (int f = 0; f < 4; ++f) W0[f ^ 1] = ldW(cW + 144 + HOFF(f ^ 1), cW + 144 + HOFF(f ^ 1) + PITCH);
        xa[2] = ld128(cX0);
#pragma unroll
        for (int f = 0; f < 5; ++f) { aV[0][f] = MFMA(xb[0], cv[f], aV[0][f]); aV[1][f] = MFMA(xb[1], cv[f], aV[1][f]); }
        hv = MFMA(xb[2], cv[W], hv);
        if (W == 1) he = MFMA(xb[2], cv[4], he);
        W0[5] = ldW(cW + HOFF(0), cW + HOFF(0) + PITCH);
        W0[4] = ldW(cKQ, cKQ + PITCH);
        W0[7] = ldW(cW + HOFF(2), cW + HOFF(2) + PITCH);
        W0[6] = ldW(cW + HOFF(1), cW + HOFF(1) + PITCH);
        W0[9] = ldW(cW + 288 + HOFF(0), cW + 288 + HOFF(0) + PITCH);
        W0[8] = ldW(cW + HOFF(3), cW + HOFF(3) + PITCH);
        W0[11] = ldW(cW + 288 + HOFF(2), cW + 288 + HOFF(2) + PITCH);
        W0[10] = ldW(cW + 288 + HOFF(1), cW + 288 + HOFF(1) + PITCH);
        W0[13] = ldW(cV[0], cV[1]);
        W0[12] = ldW(cW + 288 + HOFF(3), cW + 288 + HOFF(3) + PITCH);
        xb[0] = ld128(cX1 + XO); xb[1] = ld128(cX1 + XO + 2048); xb[2] = ld128(cX1);
#pragma unroll
        for (int f = 0; f < 4; ++f) W1[f ^ 1] = ldW(cW + KO + 144 + HOFF(f ^ 1), cW + KO + 144 + HOFF(f ^ 1) + PITCH);
        W1[5] = ldW(cW + KO + HOFF(0), cW + KO + HOFF(0) + PITCH);
        W1[4] = ldW(cKQ + KO, cKQ + KO + PITCH);
        W1[8] = ldW(cW + KO + HOFF(3), cW + KO + HOFF(3) + PITCH);
        W1[7] = ldW(cW + KO + HOFF(2), cW + KO + HOFF(2) + PITCH);
        W1[6] = ldW(cW + KO + HOFF(1), cW + KO + HOFF(1) + PITCH);
#pragma unroll
        for (int f = 0; f < 4; ++f) cv[f] = ldW(cW + KO + 288 + HOFF(f), cW + KO + 288 + HOFF(f) + PITCH);
        cv[4] = ldW(cV[2], cV[3]);
#pragma unroll
        for (int f = 0; f < 4; ++f) { aK[0][f] = MFMA(W0[f], xa[0], aK[0][f]); aK[1][f] = MFMA(W0[f], xa[1], aK[1][f]); }
        hk = MFMA(W0[W], xa[2], hk);
        aKQ[0] = MFMA(W0[4], xa[0], aKQ[0]); aKQ[1] = MFMA(W0[4], xa[1], aKQ[1]);
        if (W == 0) he = MFMA(W0[4], xa[2], he);
#pragma unroll
        for (int f = 0; f < 4; ++f) { aQ[0][f] = MFMA(W0[5 + f], xa[0], aQ[0][f]); aQ[1][f] = MFMA(W0[5 + f], xa[1], aQ[1][f]); }
#pragma unroll
        for (int f = 0; f < 5; ++f) { aV[0][f] = MFMA(xa[0], W0[9 + f], aV[0][f]); aV[1][f] = MFMA(xa[1], W0[9 + f], aV[1][f]); }
        hv = MFMA(xa[2], W0[9 + W], hv);
        if (W == 1) he = MFMA(xa[2], W0[13], he);
#pragma unroll
        for (int f = 0; f < 4; ++f) { aK[0][f] = MFMA(W1[f], xb[0], aK[0][f]); aK[1][f] = MFMA(W1[f], xb[1], aK[1][f]); }
        hk = MFMA(W1[W], xb[2], hk);
        aKQ[0] = MFMA(W1[4], xb[0], aKQ[0]); aKQ[1] = MFMA(W1[4], xb[1], aKQ[1]);
        if (W == 0) he = MFMA(W1[4], xb[2], he);
#pragma unroll
        for (int f = 0; f < 4; ++f) { aQ[0][f] = MFMA(W1[5 + f], xb[0], aQ[0][f]); aQ[1][f] = MFMA(W1[5 + f], xb[1], aQ[1][f]); }
        const int ds = slot == 2 ? -2 * STG : STG, dg = kc == 8 ? -8 * 512 : 512;
        slot = slot == 2 ? 0 : slot + 1;
        kc = kc == 8 ? 0 : kc + 1;
        const int dv = p < 2 ? ds : dg;
        cW += ds; cKQ += ds; cX0 += ds; cX1 += ds;
#pragma unroll
        for (int i = 0; i < 4; ++i) cV[i] += dv;
        SGB(0x100, 2);
#pragma unroll
        for (int i = 0; i < 54; ++i) {
          SGB(0x8, 1);
          if (i < 3 || i == 16 || i == 28 || i == 40) SGB(0x100, 2); else SGB(0x100, 1);
          if (i >= 3 && i < 11) SGB(0x4, 1);
          if (i >= 51) SGB(0x2, 1);
        }
#pragma unroll
        for (int i = 0; i < 4; ++i) { SGB(0x8, 1); SGB(0x2, 2); }
        SGB(0x8, W < 2 ? 4 : 2);
        SCHED0();
        WAIT_LGKM0();
      }
      hv = MFMA(xb[2], cv[W], hv);
      if (W == 1) he = MFMA(xb[2], cv[4], he);
      if (W < 2) {
        u32x2 v = pack4(he);
        if (W == 0 && tile == 0) v = u32x2{0u, 0u};
        st64(L + EX_OFF + lane * 8 + (W == 0 ? 4 : 9) * 512, v);
      }
    };
    if (wave == 0) mainloop(std::integral_constant<int, 0>{});
    else if (wave == 1) mainloop(std::integral_constant<int, 1>{});
    else if (wave == 2) mainloop(std::integral_constant<int, 2>{});
    else mainloop(std::integral_constant<int, 3>{});
    WAIT_VM0();
    STAMP(12);
    const bool g0 = g == 0;
    frag K[3][4], Q[2][4], Vb[5], Va[5];
    u32x2 ks[2], qs[2];
    aV[1][4] = MFMA(xb[1], cv[4], aV[1][4]); PIN(aV[1][4]);
    aV[0][4] = MFMA(xb[0], cv[4], aV[0][4]); PIN(aV[0][4]);
#pragma unroll
    for (int t = 1; t >= 0; --t) {
      u32x2 kq = pack4(aKQ[t]);
      auto r0 = __builtin_amdgcn_permlane32_swap(kq[0], 0u, false, false);
      auto r1 = __builtin_amdgcn_permlane32_swap(kq[1], 0u, false, false);
      ks[t] = u32x2{r0[0], r1[0]}; qs[t] = u32x2{r0[1], r1[1]};
      PIN(ks[t]); PIN(qs[t]);
    }
    SCHED0();
#pragma unroll
    for (int f = 0; f < 4; ++f) { aV[1][f] = MFMA(xb[1], cv[f], aV[1][f]); PIN(aV[1][f]); K[2][f] = cat(pack4(aK[1][f]), ks[1]); PIN(K[2][f]); SCHED0(); }
    {
      u32x2 h = pack4(hk), hh = pack4(hv);
      if (tile == 0) h = u32x2{0u, 0u};
      WR2(exh, h, hh, 0, 5);
    }
    if (c >= 8) { *(LDSP f32x4*)(size_t)(exgw) = aV[0][4]; *(LDSP f32x4*)(size_t)(exgw + 64) = aV[1][4]; }
    SCHED0();
#pragma unroll
    for (int f = 0; f < 4; ++f) {
      aV[0][f] = MFMA(xb[0], cv[f], aV[0][f]); PIN(aV[0][f]);
      u32x2 v = pack4(aV[1][f]); Vb[f][2] = v[0]; Vb[f][3] = v[1]; PIN(Vb[f]); SCHED0();
    }
    { u32x2 v = pack4(aV[1][4]); Vb[4][2] = v[0]; Vb[4][3] = v[1]; PIN(Vb[4]); }
    if (wave < 3) {
#pragma unroll
      for (int f = 0; f < 4; ++f) WR2(exr, (u32x2{K[2][f][0], K[2][f][1]}), (u32x2{Vb[f][2], Vb[f][3]}), 10 + f, 15 + f);
      WR2(exr, ks[1], (u32x2{Vb[4][2], Vb[4][3]}), 14, 19);
    }
    SCHED0();
#pragma unroll
    for (int h = 0; h < 4; ++h) {
      K[1][h] = cat(pack4(aK[0][h]), ks[0]); PIN(K[1][h]);
      u32x2 qt = pack4(aQ[1][h]);
      unsigned e = ((h < 2) == g0) ? qs[1][h & 1] : 0u;
      Q[1][h] = (h & 1) ? frag{qt[0], qt[1], 0u, e} : frag{qt[0], qt[1], e, 0u}; PIN(Q[1][h]);
    }
    SCHED0();
    GAS u64* myflag = (GAS u64*)(ws + FLAG_OFF + ((grp >> 2) * 64 + tile * 4 + cw) * FS1 + (grp & 3) * FS2);
    PINS(myflag);
    TAG_SCALAR();
    WAIT_LGKM0();
    STAMP(23);
    BARRIER();
    STAMP(24);
    SCHED0();
    u32x2 gz;
    asm volatile("ds_read2_b32 %0, %1 offset0:0 offset1:16" : "=v"(gz) : "v"(exgr) : "memory");
#pragma unroll
    for (int h = 0; h < 4; ++h) asm volatile("ds_read2st64_b64 %0, %1 offset0:%2 offset1:4" : "=v"(K[0][h]) : "v"(exr), "i"(h) : "memory");
    f32x4 sc[2][4], sp[2][4];
#pragma unroll
    for (int h = 0; h < 4; ++h) {
      sc[1][h] = MFMA(K[2][h], Q[1][h], maskC); PIN(sc[1][h]);
      sp[1][h] = MFMA(K[1][h], Q[1][h], maskP); PIN(sp[1][h]);
      u32x2 qt = pack4(aQ[0][h]);
      unsigned e = ((h < 2) == g0) ? qs[0][h & 1] : 0u;
      Q[0][h] = (h & 1) ? frag{qt[0], qt[1], 0u, e} : frag{qt[0], qt[1], e, 0u}; PIN(Q[0][h]);
      SCHED0();
    }
    asm volatile("s_waitcnt lgkmcnt(0)" : "+v"(gz), "+v"(K[0][0]), "+v"(K[0][1]), "+v"(K[0][2]), "+v"(K[0][3])::"memory");
    float v[2][4][2];
#pragma unroll
    for (int h = 0; h < 4; ++h) {
      sc[0][h] = MFMA(K[1][h], Q[0][h], maskC); PIN(sc[0][h]);
      sp[0][h] = MFMA(K[0][h], Q[0][h], maskP0); PIN(sp[0][h]);
      v[1][h][0] = fmx(fmx3(sp[1][h][0], sp[1][h][1], sc[1][h][0]), sc[1][h][1]); PIN(v[1][h][0]);
      v[1][h][1] = fmx(fmx3(sp[1][h][2], sp[1][h][3], sc[1][h][2]), sc[1][h][3]); PIN(v[1][h][1]);
      SCHED0();
    }
#pragma unroll
    for (int f = 0; f < 5; ++f) {
      u32x2 a = pack4(aV[0][f]); Vb[f][0] = a[0]; Vb[f][1] = a[1]; PIN(Vb[f]);
      Va[f] = __builtin_shufflevector(Vb[f], Vb[f], -1, -1, 0, 1); PIN(Va[f]);
    }
#pragma unroll
    for (int f = 0; f < 5; ++f) { u32x2 n = *(volatile LDSP u32x2*)(size_t)(exr + (5 + f) * 512); Va[f][0] = n[0]; Va[f][1] = n[1]; }
    u32x4 pk[2];
    auto softmax = [&](float (&w)[4][2], float gate_logit) {
      swap32(w[0][0], w[2][0]); swap32(w[1][0], w[3][0]); swap32(w[0][1], w[2][1]); swap32(w[1][1], w[3][1]);
      swap16(w[0][0], w[1][0]); swap16(w[2][0], w[3][0]); swap16(w[0][1], w[1][1]); swap16(w[2][1], w[3][1]);
      const float cs = 0.23570226039551584f * 1.4426950408889634f;
      float m = fmx(fmx3(fmx3(w[0][0], w[1][0], w[2][0]), w[3][0], w[0][1]), fmx3(w[1][1], w[2][1], w[3][1]));
      f32x2 e[4];
#pragma unroll
      for (int i = 0; i < 4; ++i) {
        f32x2 a = f32x2{w[i][0], w[i][1]} - m;
        PIN(a);
        a *= cs;
        e[i] = f32x2{__builtin_amdgcn_exp2f(a[0]), __builtin_amdgcn_exp2f(a[1])};
      }
      f32x2 s2 = (e[0] + e[1]) + (e[2] + e[3]);
      float s = s2[0] + s2[1];
      float r = __builtin_amdgcn_rcpf(__builtin_fmaf(s, __builtin_amdgcn_exp2f(gate_logit * sg), s));
      float pf[4];
#pragma unroll
      for (int i = 0; i < 4; ++i) { f32x2 pp = e[i] * r; pf[i] = __uint_as_float(cvtpk(pp[0], pp[1])); }
      swap32(pf[0], pf[2]); swap32(pf[1], pf[3]);
      swap16(pf[0], pf[1]); swap16(pf[2], pf[3]);
      return u32x4{__float_as_uint(pf[0]), __float_as_uint(pf[1]), __float_as_uint(pf[2]), __float_as_uint(pf[3])};
    };
    SCHED0();
#pragma unroll
    for (int h = 0; h < 4; ++h) {
      v[0][h][0] = fmx(fmx3(sp[0][h][0], sp[0][h][1], sc[0][h][0]), sc[0][h][1]);
      v[0][h][1] = fmx(fmx3(sp[0][h][2], sp[0][h][3], sc[0][h][2]), sc[0][h][3]);
    }
    pk[1] = softmax(v[1], __uint_as_float(gz[1]));
#pragma unroll
    for (int h = 0; h < 4; ++h) { PIN(v[0][h][0]); PIN(v[0][h][1]); }
    SCHED0();
    pv_store(pk[1], Vb, sel, vmain, vleft, ors, osoff1, oloff1, g0);
    pk[0] = softmax(v[0], __uint_as_float(gz[0]));
    SCHED0();
    pv_store(pk[0], Va, sel, vmain, vleft, ors, osoff, oloff, g0);
    STAMP(25);
    WAIT_VM0();
    if (lane == 0) __hip_atomic_store(myflag, tag, RLX_AGENT);
    STAMP(26);
    RSTAMP(61);
    asm volatile(".Lfk_tail:");
  }
  BARRIER();
  if (!act2) {
    if (bid == 255) {
      if (wave < 4) {
        const int b = wave * 64 + lane;
        GAS u64* fp = (GAS u64*)(ws + FLAG_OFF + (((b & 15) >> 2) * 64 + (b >> 4) * 4) * FS1 + (b & 3) * FS2);
#pragma clang loop unroll(disable)
        for (unsigned spins = 0; spins < (1u << 17); ++spins) {
          u64 v = __hip_atomic_load(fp, RLX_AGENT);
          if (__all(v == tag)) break;
          __builtin_amdgcn_s_sleep(8);
        }
      }
      BARRIER();
      if (tid == 0) __hip_atomic_store((GAS u64*)(ws + EPOCH_OFF), tag, RLX_AGENT);
    }
    return;
  }
  if (cw == 3) {
#pragma unroll
    for (int j = 0; j < 4; ++j) { wo_piece(3, 7, j); wo_piece(3, 8, j); }
  }
  unsigned ra[2][2][2];
#pragma unroll
  for (int it = 0; it < 2; ++it)
#pragma unroll
    for (int hf = 0; hf < 2; ++hf)
#pragma unroll
      for (int wp = 0; wp < 2; ++wp) {
        ra[it][hf][wp] = L + wp * 73728 + (wave * 16 + it * 8 + vl) * 256 + ((((lane & 7) * 2 + hf) ^ (it * 8 + vl)) * 16);
        PIN(ra[it][hf][wp]);
      }
  GAS char* op = (GAS char*)out + (size_t)(rt * 80 + wave * 16 + vl) * XROW + ct * 128 + (lane & 7) * 16;
  GAS char* op1 = op + 8 * XROW;
  PIN(op); PIN(op1);
  if (wave < 4) phase2<2>(L, ws, ors, tag, rt * 5 + 3, 48, cw, rot2, lane STAMP_PASS);
  else phase2<3>(L, ws, ors, tag, rt * 5, 0, cw, rot2, lane STAMP_PASS);
  WAIT_LGKM0();
  BARRIER();
  STAMP(30);
  if (wave < 5 && rt * 5 + wave < 128) {
    f32x4 u[2][2][4];
#pragma unroll
    for (int it = 0; it < 2; ++it)
#pragma unroll
      for (int hf = 0; hf < 2; ++hf)
#pragma unroll
        for (int w = 0; w < 4; ++w) u[it][hf][w] = *(LDSP f32x4*)(size_t)(ra[it][hf][w >> 1] + (w & 1) * 36864);
    SCHED0();
#pragma unroll
    for (int it = 0; it < 2; ++it) {
      f32x4 s0 = (u[it][0][0] + u[it][0][1]) + (u[it][0][2] + u[it][0][3]), s1 = (u[it][1][0] + u[it][1][1]) + (u[it][1][2] + u[it][1][3]);
      u32x4 o = {cvtpk(s0[0], s0[1]), cvtpk(s0[2], s0[3]), cvtpk(s1[0], s1[1]), cvtpk(s1[2], s1[3])};
      *(GAS u32x4*)(it ? op1 : op) = o;
      SCHED0();
    }
  }
  STAMP(31);
  RSTAMP(63);
  STAMP_OUT();
}

#undef x
#undef wA
#undef wB
#undef woA
#undef woB
#undef gw
#undef ws
#undef out
#undef ors

extern "C" void kernel_launch(void* const* d_in, const int* in_sizes, int n_in, void* d_out, int out_size, void* d_ws, size_t ws_size,
                              hipStream_t stream) {
  fused_kernel<<<256, 512, 0, stream>>>((const u16*)d_in[0], (const u16*)d_in[1], (const u16*)d_in[4], (const u16*)d_in[2], (const u16*)d_in[5],
                                        (const u16*)d_in[6], (char*)d_ws, (u16*)d_out);
}
```

```cpp
#define WIN3 3
#define WIN2 3
#define KSH 0
#include <hip/hip_runtime.h>
#include <hip/hip_bf16.h>
#include <stdint.h>
#include <type_traits>

typedef unsigned short u16;
typedef unsigned long long u64;
typedef short s16x4 __attribute__((ext_vector_type(4)));
typedef float f32x2 __attribute__((ext_vector_type(2)));
typedef float f32x4 __attribute__((ext_vector_type(4)));
typedef unsigned u32x2 __attribute__((ext_vector_type(2)));
typedef unsigned u32x4 __attribute__((ext_vector_type(4)));
typedef u32x4 frag;
typedef __bf16 bf16x2 __attribute__((ext_vector_type(2)));
typedef __bf16 bfx8 __attribute__((ext_vector_type(8)));
#define LDSP __attribute__((address_space(3)))
#define GAS __attribute__((address_space(1)))
#define DI __device__ __forceinline__

constexpr int WROW = 3456;
constexpr int XROW = 1152;
constexpr int PITCH = 432;
constexpr int KO = 32 * PITCH;
constexpr int STG_W = 64 * PITCH;
constexpr int STG = STG_W + 144 * 128;
constexpr int G_OFF = 3 * STG;
constexpr int SCR_OFF = G_OFF + 4608;
constexpr int EX_OFF = 143360;
constexpr int LDS_BYTES = 163840;
constexpr int O_BYTES = 128 * 36 * 1024;
constexpr int FLAG_OFF = 8 << 20;
constexpr int EPOCH_OFF = FLAG_OFF + (1 << 20);
#define ROT 11
#define PRE0 5
#define NBX 22
#define FS1 128
#define FS2 8
#define PSLEEP 1
#define CODEPF 22656
#define NB1 5

#define MFMA(a, b, c) __builtin_amdgcn_mfma_f32_16x16x32_bf16(__builtin_bit_cast(bfx8, (a)), __builtin_bit_cast(bfx8, (b)), (c), 0, 0, 0)
#define RLX_AGENT __ATOMIC_RELAXED, __HIP_MEMORY_SCOPE_AGENT
#define HOFF(h) (36 * (h) + ((h) & 1) * 4)

DI void glds16(unsigned voff, const GAS void* sbase, unsigned lds_addr) {
  asm volatile("s_mov_b32 m0, %2\n\ts_nop 0\n\tglobal_load_lds_dwordx4 %0, %1" ::"v"(voff), "s"(sbase), "s"(lds_addr) : "memory");
}
DI u32x2 trd(unsigned a) { return __builtin_bit_cast(u32x2, __builtin_amdgcn_ds_read_tr16_b64_v4i16((LDSP s16x4*)(size_t)a)); }
DI frag ldW(unsigned a0, unsigned a1) {
  u32x2 lo = trd(a0), hi = trd(a1);
  return __builtin_shufflevector(lo, hi, 0, 1, 2, 3);
}
DI frag ld128(unsigned a) { return *(LDSP frag*)(size_t)a; }
DI u32x2 ld64(unsigned a) { return *(LDSP u32x2*)(size_t)a; }
DI void st64(unsigned a, u32x2 v) { *(LDSP u32x2*)(size_t)a = v; }
DI unsigned cvtpk(float a, float b) {
  f32x2 f = {a, b};
  return __builtin_bit_cast(unsigned, __builtin_convertvector(f, bf16x2));
}
DI u32x2 pack4(f32x4 a) { return u32x2{cvtpk(a[0], a[1]), cvtpk(a[2], a[3])}; }
DI frag cat(u32x2 a, u32x2 b) { return __builtin_shufflevector(a, b, 0, 1, 2, 3); }
DI void swap32(float& a, float& b) {
  auto r = __builtin_amdgcn_permlane32_swap(__float_as_uint(a), __float_as_uint(b), false, false);
  a = __uint_as_float(r[0]); b = __uint_as_float(r[1]);
}
DI void swap16(float& a, float& b) {
  auto r = __builtin_amdgcn_permlane16_swap(__float_as_uint(a), __float_as_uint(b), false, false);
  a = __uint_as_float(r[0]); b = __uint_as_float(r[1]);
}
DI float fmx(float a, float b) { return __builtin_elementwise_maximum(a, b); }
DI float fmx3(float a, float b, float c) { return fmx(fmx(a, b), c); }
#define STAMP(i)
#define RSTAMP(i)
#define STAMP_OUT()
#define STAMP_ARG
#define STAMP_PASS
#define STAMP_DECL
#define SGB(m, n) __builtin_amdgcn_sched_group_barrier(m, n, 0)
#define SCHED0() __builtin_amdgcn_sched_barrier(0)
#define WAIT_LGKM0() do { __builtin_amdgcn_s_waitcnt(0xC07F); asm volatile("" ::: "memory"); } while (0)
#define WAIT_VM0() asm volatile("s_waitcnt vmcnt(0)" ::: "memory")
#define BARRIER() __builtin_amdgcn_s_barrier()
#define PIN(x) asm volatile("" : "+v"(x))
#define TAG_SCALAR() do { unsigned lo_ = __builtin_amdgcn_readfirstlane((unsigned)tag), hi_ = __builtin_amdgcn_readfirstlane((unsigned)(tag >> 32)); \
    tag = (((u64)hi_ << 32) | lo_) + 1; asm volatile("" : "+s"(tag)); } while (0)
#define PINS(x) asm volatile("" : "+s"(x))
#define WR2(addr, a, b, o0, o1) asm volatile("ds_write2st64_b64 %0, %1, %2 offset0:%3 offset1:%4" ::"v"(addr), "v"(a), "v"(b), "i"(o0), "i"(o1) : "memory")

DI void pv_store(u32x4 pk, const frag (&V)[5], u32x4 sel, unsigned vmain, unsigned vleft, __amdgpu_buffer_rsrc_t ors, unsigned so, unsigned sl, bool g0) {
  const f32x4 z = {0.f, 0.f, 0.f, 0.f};
  frag P[4];
  f32x4 o[4], os[4];
  auto mkP = [&](int h) {
    unsigned u = pk[h];
    P[h] = frag{__builtin_amdgcn_perm(u, u, sel[0]), __builtin_amdgcn_perm(u, u, sel[1]), __builtin_amdgcn_perm(u, u, sel[2]), __builtin_amdgcn_perm(u, u, sel[3])};
    PIN(P[h]);
  };
  mkP(0); mkP(1);
  SCHED0();
  u32x4 f0, f1;
  os[0] = MFMA(V[4], P[0], z); PIN(os[0]); o[0] = MFMA(V[0], P[0], z); PIN(o[0]); mkP(2); SCHED0();
  os[1] = MFMA(V[4], P[1], z); PIN(os[1]); o[1] = MFMA(V[1], P[1], z); PIN(o[1]); mkP(3); SCHED0();
  os[2] = MFMA(V[4], P[2], z); PIN(os[2]); os[3] = MFMA(V[4], P[3], z); PIN(os[3]);
  o[2] = MFMA(V[2], P[2], z); PIN(o[2]); o[3] = MFMA(V[3], P[3], z); PIN(o[3]); SCHED0();
  f0 = u32x4{cvtpk(o[0][0], o[0][1]), cvtpk(o[0][2], o[0][3]), cvtpk(o[1][0], o[1][1]), cvtpk(o[1][2], o[1][3])};
  PIN(f0); SCHED0();
  __builtin_amdgcn_raw_buffer_store_b128(f0, ors, vmain, so, 16);
  SCHED0();
  u32x2 f2 = {cvtpk(g0 ? os[0][0] : os[2][0], g0 ? os[0][1] : os[2][1]), cvtpk(g0 ? os[1][2] : os[3][2], g0 ? os[1][3] : os[3][3])};
  PIN(f2); SCHED0();
  f1 = u32x4{cvtpk(o[2][0], o[2][1]), cvtpk(o[2][2], o[2][3]), cvtpk(o[3][0], o[3][1]), cvtpk(o[3][2], o[3][3])};
  PIN(f1); PIN(f0); SCHED0();
  __builtin_amdgcn_raw_buffer_store_b128(f1, ors, vmain + 1024, so, 16);
  __builtin_amdgcn_raw_buffer_store_b64(f2, ors, vleft, sl, 16);
  SCHED0();
}

template <int MT>
DI void phase2(unsigned L, GAS char* ws, __amdgpu_buffer_rsrc_t ors, u64 tag, int tt0, int mrow, int kq, int rot2, int lane STAMP_ARG) {
  const int g = lane >> 4, q = (lane >> 2) & 3, p = lane & 3, c = lane & 15;
  const unsigned sw = (2 * g + (q >> 1)) & 3;
  unsigned bw[4];
#pragma unroll
  for (int n = 0; n < 4; ++n) bw[n] = L + kq * 36864 + (4 * g + q) * 128 + 8 * p + ((n ^ sw) * 32);
  frag wf[9][4];
  auto rdw = [&](int i) {
#pragma unroll
    for (int n = 0; n < 4; ++n) wf[i][n] = ldW(bw[n] + i * 4096, bw[n] + i * 4096 + 2048);
  };
  rdw(0); rdw(1); rdw(2); rdw(3); rdw(4);
  SCHED0();
  unsigned so[9], vo[MT];
#pragma unroll
  for (int i = 0; i < 9; ++i) so[i] = (i < 8 ? 8 * kq + ((i + rot2) & 7) : 32 + kq) * 1024;
#pragma unroll
  for (int m = 0; m < MT; ++m) { int tt = tt0 + m; tt = tt > 127 ? 127 : tt; vo[m] = lane * 16 + tt * 36864; }
  int tf = (lane & 4) ? tt0 + MT - 1 : tt0; tf = tf > 127 ? 127 : tf;
  GAS u64* fp = (GAS u64*)(ws + FLAG_OFF + (kq * 64 + (tf >> 1)) * FS1 + (lane & 3) * FS2);
  unsigned rb[4];
#pragma unroll
  for (int n = 0; n < 4; ++n) rb[n] = L + kq * 36864 + (mrow + c) * 256 + (((4 * n + g) ^ c) * 16);
#pragma unroll
  for (int i = 0; i < 9; ++i) PINS(so[i]);
#pragma unroll
  for (int m = 0; m < MT; ++m) PIN(vo[m]);
#pragma unroll
  for (int n = 0; n < 4; ++n) PIN(rb[n]);
  PIN(fp);
  WAIT_LGKM0();
  BARRIER();
  STAMP(27);
#pragma clang loop unroll(disable)
  for (unsigned spins = 0; spins < (1u << 17); ++spins) {
    u64 v = __hip_atomic_load(fp, RLX_AGENT);
    if (__all(v == tag)) break;
    __builtin_amdgcn_s_sleep(PSLEEP);
  }
  __builtin_amdgcn_fence(__ATOMIC_ACQUIRE, "wavefront");
  asm volatile("" ::: "memory");
  STAMP(28);
  RSTAMP(62);
  frag of[9][MT];
  constexpr int WIN = MT == 3 ? WIN3 : WIN2;
#pragma unroll
  for (int i = 0; i < WIN; ++i)
#pragma unroll
    for (int m = 0; m < MT; ++m) { of[i][m] = __builtin_amdgcn_raw_buffer_load_b128(ors, vo[m], so[i], 16); SCHED0(); }
  f32x4 acc[MT][4];
#pragma unroll
  for (int i = 0; i < 8; ++i) {
    const bool rd = i >= 2 && i + 3 < 9;
    if (rd) rdw(i + 3);
#pragma unroll
    for (int m = 0; m < MT; ++m) {
#pragma unroll
      for (int n = 0; n < 4; ++n) acc[m][n] = MFMA(wf[i][n], of[i][m], i == 0 ? (f32x4{0, 0, 0, 0}) : acc[m][n]);
      if (i + WIN < 9) of[i + WIN][m] = __builtin_amdgcn_raw_buffer_load_b128(ors, vo[m], so[i + WIN], 16);
    }
#pragma unroll
    for (int m = 0; m < MT; ++m) {
#pragma unroll
      for (int n = 0; n < 4; ++n) { SGB(0x8, 1); if (m < 2 && rd) SGB(0x100, 1); }
      if (i + WIN < 9) SGB(0x20, 1);
    }
    SCHED0();
  }
#pragma unroll
  for (int m = 0; m <= MT; ++m) {
    if (m < MT) {
#pragma unroll
      for (int n = 0; n < 4; ++n) { acc[m][n] = MFMA(wf[8][n], of[8][m], acc[m][n]); PIN(acc[m][n]); }
    }
    SCHED0();
    if (m > 0) {
#pragma unroll
      for (int n = 0; n < 4; ++n) *(LDSP f32x4*)(size_t)(rb[n] + (m - 1) * 4096) = acc[m - 1][n];
    }
    SCHED0();
  }
  STAMP(29);
}

typedef u64 u64x8 __attribute__((ext_vector_type(8)));
extern "C" __global__ __launch_bounds__(512, 2) void fused_kernel(const u16* x_, const u16* wA_, const u16* wB_, const u16* woA_, const u16* woB_,
                                                                   const u16* gw_, char* ws_, u16* out_) {
  __shared__ __attribute__((aligned(1024))) char lds[LDS_BYTES];
  const unsigned L = (unsigned)(size_t)(LDSP char*)lds;
  const int BIDX = blockIdx.x, TIDX = threadIdx.x;
  u64x8 ka;
  asm volatile("s_load_dwordx16 %0, %1, 0x0" : "=s"(ka) : "s"(__builtin_amdgcn_kernarg_segment_ptr()));
#define KARG_WAIT() do { SCHED0(); asm volatile("s_waitcnt lgkmcnt(0)" ::: "memory"); SCHED0(); asm volatile("" : "+s"(ka)); } while (0)
#define x ((const GAS u16*)ka[0])
#define wA ((const GAS u16*)ka[1])
#define wB ((const GAS u16*)ka[2])
#define woA ((const GAS u16*)ka[3])
#define woB ((const GAS u16*)ka[4])
#define gw ((const GAS u16*)ka[5])
#define ws ((GAS char*)ka[6])
#define out ((GAS u16*)ka[7])
#define ors __builtin_amdgcn_make_buffer_rsrc((void*)ka[6], 0, O_BYTES, 0x00020000)
  const int bid = BIDX;
  const int xcd = bid & 7, idx = bid >> 3;
  const int tl = idx & 7, gl = idx >> 3;
  const int tile = (xcd & 1) * 8 + tl;
  const int grp = (xcd >> 1) * 4 + gl;
  int rot = ROT == 1 ? tl + 2 * gl : ROT == 2 ? (tl >> 1) * 2 + 2 * gl : ROT == 6 ? (tl % 3) * 3 + (gl % 3) * 3 : ROT == 7 ? (((tl >> 1) + gl) & 3) * 2 + (tl & 1 & (gl >> 1))
          : ROT == 8 ? (((tl + gl) & 3) * 9) >> 2 : ROT == 9 ? ((tl + gl + (xcd >> 1)) % 3) * 3 : ROT == 10 ? (((tl + gl) & 3) * 9 >> 2) + (xcd & 1)
          : ROT == 11 ? ((tl + gl) & 3) * 2 + ((xcd ^ (xcd >> 1)) & 1) : ROT == 12 ? ((tl + gl) % 3) * 3 + ((xcd & 1) + (xcd >> 1)) % 3
          : ROT == 13 ? ((tl + gl) & 1) * 4 + (((xcd & 1) * 2 + (xcd >> 1)) & 3) : ROT == 14 ? (tl + 2 * gl + ((xcd >> 1) & 1)) % 9
          : ROT == 15 ? (((tl + gl) & 3) * 2 + ((xcd ^ (xcd >> 1)) & 1) + (xcd >> 2)) : 0;
  if (rot >= 9) rot -= 9;
  const int tid = TIDX, lane = tid & 63;
  const int wave = __builtin_amdgcn_readfirstlane(tid >> 6);
  const int vl = lane >> 3;
  STAMP_DECL;
  STAMP(0);
  RSTAMP(60);
  if (tid < 7) {
    u64 pc0;
    unsigned len;
    asm volatile("s_getpc_b64 %0\n\ts_mov_b32 %1, .Lfk_tail-fused_kernel+128" : "=s"(pc0), "=s"(len));
    unsigned off = (idx * 7 + tid) * 128;
    off = off < len ? off : 0;
    asm volatile("s_mov_b32 m0, %2\n\ts_nop 0\n\tglobal_load_lds_dword %0, %1" ::"v"(off), "s"(pc0 & ~255ull), "s"(L + SCR_OFF) : "memory");
  }
  u64 tag;
  const int lin0 = xcd * 30 + idx;
  const bool act2 = idx < 30 && lin0 < 234;
  const int lin = act2 ? lin0 : 0;
  const int rt = (lin * 57) >> 9, ct = lin - rt * 9;
  const int rot2 = 3 * (rt - ((rt * 171) >> 9) * 3);
  const float sg = (grp >= 8) ? 1.4426950408889634f : -1.4426950408889634f;
  const unsigned wsch = ((((lane & 7) >> 1) ^ ((vl >> 1) & 3)) << 5) | ((lane & 1) << 4);
  const unsigned offn = vl * XROW + wsch, offl = ((vl >> 2) * 72 + (vl & 3)) * XROW + wsch;
  auto wo_piece = [&](int kq, int i, int j) {
    const GAS char* b = (const GAS char*)(kq >= 2 ? woB : woA) + ct * 128 + (kq & 1) * (288 * XROW);
    if (i == 8) b += ((j & 1) * 144 + ((j >> 1) ? 52 : 16)) * XROW;
    else b += (((i + rot2) & 7) * 36 + (j >> 1) * 20 + 8 * (j & 1)) * XROW;
    glds16(i == 8 ? offl : offn, b, L + (kq * 9 + i) * 4096 + j * 1024);
  };
  const int g = lane >> 4, q = (lane >> 2) & 3, p = lane & 3, c = lane & 15;
  const int cw = wave & 3;

  if (__builtin_expect(wave >= 4, 1)) {
    __builtin_amdgcn_s_setprio(2);
    const int lw = cw, tl256 = tid - 256;
    const unsigned offx = (tile * 128 + 8 * lw + vl) * XROW + ((lane & 7) ^ ((4 * lw + (vl >> 1)) & 7)) * 16;
    KARG_WAIT();
    const GAS char* w = (const GAS char*)((grp >= 8) ? wB : wA) + (grp & 7) * 144;
    unsigned offw[7], offh;
    auto kchunk = [&](int st) { int kc = st + rot; return kc >= 9 ? kc - 9 : kc; };
    auto issueX = [&](int kc, unsigned sb, int jj) { glds16(offx, (const GAS char*)x + kc * 128 + jj * (32 * XROW), sb + STG_W + (2 + lw + 4 * jj) * 1024); };
    auto issueW = [&](int kc, unsigned sb, int j) { glds16(offw[j], w + kc * (64 * WROW), sb + (lw + 4 * j) * 1024); };
    auto issue6 = [&](int kc, unsigned sb) {
      if (lw < 3) issueW(kc, sb, 6);
      else { glds16(offw[6], (const GAS char*)x + kc * 128, sb + STG_W); glds16(offh, (const GAS char*)x + kc * 128, sb + STG_W + 1024); }
    };
    const int kc0 = rot, kc1 = kchunk(1);
    const unsigned sb0 = L, sb1 = L + STG;
    auto calcw = [&](int j) {
      unsigned qq = 256 * j + tl256;
      unsigned u = (qq * 2428u) >> 16, cc = qq - u * 27;
      unsigned row = (u & 0x38) | ((u & 1) << 2) | ((u >> 1) & 3);
      offw[j] = row * WROW + cc * 16 + ((cc * 57u) >> 9) * 1008;
    };
    issueX(kc0, sb0, 0);
    u32x2 gv;
    const unsigned gi = tl256 < 144 ? tl256 : 143;
    asm volatile("global_load_dwordx2 %0, %1, %2" : "=v"(gv) : "v"(gi * 8), "s"(gw) : "memory");
#pragma unroll
    for (int j = 0; j < 6; ++j) {
      SCHED0();
      calcw(j);
      SCHED0();
      issueW(kc0, sb0, j);
      if (j < 3) issueX(kc0, sb0, j + 1);
    }
    calcw(6);
    {
      int t0 = tile * 128 - 16 + vl, t1 = t0 + 8;
      t0 = t0 < 0 ? 0 : t0; t1 = t1 < 0 ? 0 : t1;
      unsigned h0 = t0 * XROW + ((lane & 7) ^ (vl >> 1)) * 16;
      offh = t1 * XROW + ((lane & 7) ^ (4 + (vl >> 1))) * 16;
      offw[6] = lw == 3 ? h0 : offw[6];
    }
    issue6(kc0, sb0);
    STAMP(1);
    issueW(kc1, sb1, 0);
    int slot = 0, kn = kc1;
    const GAS char* xp = (const GAS char*)x + kn * 128;
    unsigned b1 = sb1, xd = sb1 + STG_W + (2 + lw) * 1024;
    PINS(xp); PINS(xd);
    SCHED0();
#pragma unroll
    for (int j = 1; j < PRE0; ++j) issueW(kc1, sb1, j);
    STAMP(2);
    asm volatile("s_waitcnt vmcnt(%1)" : "+v"(gv) : "i"(10 + PRE0) : "memory");
    {
      unsigned a0 = __builtin_amdgcn_perm(gv[0], gv[0], 0x01000100), a1 = __builtin_amdgcn_perm(gv[0], gv[0], 0x03020302);
      unsigned a2 = __builtin_amdgcn_perm(gv[1], gv[1], 0x01000100), a3 = __builtin_amdgcn_perm(gv[1], gv[1], 0x03020302);
      *(LDSP u32x4*)(size_t)(L + G_OFF + gi * 32) = u32x4{a0, a0, a1, a1};
      *(LDSP u32x4*)(size_t)(L + G_OFF + gi * 32 + 16) = u32x4{a2, a2, a3, a3};
    }
    WAIT_LGKM0();
    asm volatile("s_waitcnt vmcnt(%0)" ::"i"(PRE0) : "memory");
#pragma clang loop unroll(disable)
    for (int st = 0; st < 7; ++st) {
      BARRIER();
      glds16(offx, xp, xd);
      SCHED0();
      STAMP(3 + st);
      glds16(offx, xp + 32 * XROW, xd + 4096);
      if (st == 0) {
#pragma unroll
        for (int j = PRE0; j < 6; ++j) issueW(kc1, sb1, j);
        tag = __hip_atomic_load((GAS u64*)(ws + EPOCH_OFF), RLX_AGENT);
      }
      issue6(kn, b1);
      glds16(offx, xp + 64 * XROW, xd + 8192);
      glds16(offx, xp + 96 * XROW, xd + 12288);
      SCHED0();
      kn = kn == 8 ? 0 : kn + 1;
      b1 = L + (slot == 0 ? 2 : slot - 1) * STG;
      issueW(kn, b1, 0); issueW(kn, b1, 1);
#pragma unroll
      for (int j = 2; j < 6; ++j) issueW(kn, b1, j);
      slot = slot == 2 ? 0 : slot + 1;
      xp = (const GAS char*)x + kn * 128;
      xd = b1 + STG_W + (2 + lw) * 1024;
      PINS(xp); PINS(xd);
      asm volatile("s_waitcnt vmcnt(6)" ::: "memory");
    }
    BARRIER();
    glds16(offx, xp, xd);
    SCHED0();
    STAMP(10);
    glds16(offx, xp + 32 * XROW, xd + 4096);
    issue6(kn, b1);
    glds16(offx, xp + 64 * XROW, xd + 8192);
    glds16(offx, xp + 96 * XROW, xd + 12288);
#pragma unroll
    for (int I = 0; I < 6; ++I) wo_piece(0, I, lw);
    asm volatile("s_waitcnt vmcnt(6)" ::: "memory");
    BARRIER();
    STAMP(11);
    wo_piece(0, 6, lw); wo_piece(0, 7, lw);
    __builtin_amdgcn_s_setprio(0);
#pragma unroll
    for (int I = 8; I < NBX; ++I) wo_piece(I / 9, I % 9, lw);
    WAIT_LGKM0();
    STAMP(12);
    BARRIER();
    STAMP(13);
#pragma unroll
    for (int I = NBX; I < 34; ++I) wo_piece(I / 9, I % 9, lw);
    WAIT_VM0();
    TAG_SCALAR();
    STAMP(14);
  } else {
    const unsigned wrow = L + (8 * g + 2 * q) * PITCH;
    f32x4 maskP, maskC, maskP0;
    {
      const float ninf = -__builtin_inff();
#pragma unroll
      for (int r = 0; r < 4; ++r) {
        bool pm = (r & 1) == (c & 1);
        maskC[r] = (pm && 4 * g + r <= c) ? 0.f : ninf;
        maskP[r] = (pm && 4 * g + r > c) ? 0.f : ninf;
      }
      maskP0 = maskP + ((tile == 0 && wave == 0) ? -998244352.f * 4.242640687119285f : 0.f);
      asm volatile("" : "+v"(maskP), "+v"(maskC), "+v"(maskP0));
    }
    u32x4 sel;
    {
      const int par = c & 1;
      const bool cur1 = 4 * g + par <= c, cur2 = 4 * g + 2 + par <= c;
      const unsigned s1 = par ? 0x01000c0cu : 0x0c0c0100u;
      const unsigned s2 = par ? 0x03020c0cu : 0x0c0c0302u;
      sel[0] = cur1 ? 0x0c0c0c0cu : s1;
      sel[1] = cur2 ? 0x0c0c0c0cu : s2;
      sel[2] = cur1 ? s1 : 0x0c0c0c0cu;
      sel[3] = cur2 ? s2 : 0x0c0c0c0cu;
    }
    unsigned vmain = lane * 16, vleft = lane < 32 ? c * 16 + g * 8 : 0x40000000u;
    unsigned osoff = (tile * 8 + 2 * wave) * 36864 + grp * 2048, oloff = (tile * 8 + 2 * wave) * 36864 + 32768 + grp * 256;
    unsigned osoff1 = osoff + 36864, oloff1 = oloff + 36864;
    PINS(osoff); PINS(oloff); PINS(osoff1); PINS(oloff1);
    asm volatile("" : "+v"(sel), "+v"(vmain), "+v"(vleft));
    f32x4 aK[2][4], aQ[2][4], aKQ[2], aV[2][5], hk, hv, he;
    frag cv[5], xb[3];
    unsigned exr = L + EX_OFF + wave * 5120 + lane * 8, exh = L + EX_OFF + wave * 512 + lane * 8;
    unsigned exgw = L + SCR_OFF + wave * 128 + g * 16, exgr = L + SCR_OFF + wave * 128 + c * 4;
    PIN(exr); PIN(exh); PIN(exgw); PIN(exgr);
    auto mainloop = [&](auto WC) {
      constexpr int W = decltype(WC)::value;
      constexpr int XO = (1 + 2 * W) * 2048;
    hk = f32x4{0, 0, 0, 0}; hv = f32x4{0, 0, 0, 0}; he = f32x4{0, 0, 0, 0};
    PIN(he);
#pragma unroll
    for (int t = 0; t < 2; ++t) {
#pragma unroll
      for (int f = 0; f < 4; ++f) { aK[t][f] = f32x4{0, 0, 0, 0}; aQ[t][f] = f32x4{0, 0, 0, 0}; }
      aKQ[t] = f32x4{0, 0, 0, 0};
#pragma unroll
      for (int f = 0; f < 5; ++f) aV[t][f] = f32x4{0, 0, 0, 0};
    }
    unsigned cW = wrow + 8 * p;
    unsigned cKQ = wrow + (p < 2 ? 144 : 0) + ((p & 1) ? 104 : 32);
    unsigned cX0 = L + STG_W + c * 128 + ((g ^ (c >> 1)) * 16), cX1 = cX0 ^ 64;
    unsigned cV[4];
#pragma unroll
    for (int i = 0; i < 4; ++i)
      cV[i] = p < 2 ? wrow + 288 + (p ? 104 : 32) + (i >> 1) * KO + (i & 1) * PITCH : L + G_OFF + rot * 512 + (8 * g + q) * 8 + (i >> 1) * 256 + (i & 1) * 32;
#pragma unroll
    for (int f = 0; f < 5; ++f) cv[f] = frag{0, 0, 0, 0};
    xb[0] = frag{0, 0, 0, 0}; xb[1] = frag{0, 0, 0, 0}; xb[2] = frag{0, 0, 0, 0};
    asm volatile("" : "+v"(aK[0][0]), "+v"(aK[0][1]), "+v"(aK[0][2]), "+v"(aK[0][3]), "+v"(aK[1][0]), "+v"(aK[1][1]), "+v"(aK[1][2]), "+v"(aK[1][3]),
                      "+v"(aQ[0][0]), "+v"(aQ[0][1]), "+v"(aQ[0][2]), "+v"(aQ[0][3]), "+v"(aQ[1][0]), "+v"(aQ[1][1]), "+v"(aQ[1][2]), "+v"(aQ[1][3]),
                      "+v"(aKQ[0]), "+v"(aKQ[1]), "+v"(hk), "+v"(hv), "+v"(xb[0]), "+v"(xb[1]), "+v"(xb[2]));
    asm volatile("" : "+v"(aV[0][0]), "+v"(aV[0][1]), "+v"(aV[0][2]), "+v"(aV[0][3]), "+v"(aV[0][4]), "+v"(aV[1][0]), "+v"(aV[1][1]), "+v"(aV[1][2]),
                      "+v"(aV[1][3]), "+v"(aV[1][4]), "+v"(cv[0]), "+v"(cv[1]), "+v"(cv[2]), "+v"(cv[3]), "+v"(cv[4]),
                      "+v"(cW), "+v"(cKQ), "+v"(cX0), "+v"(cX1), "+v"(cV[0]), "+v"(cV[1]), "+v"(cV[2]), "+v"(cV[3]));
    SCHED0();
    STAMP(1);
    KARG_WAIT();
    tag = __hip_atomic_load((GAS u64*)(ws + EPOCH_OFF), RLX_AGENT);
    STAMP(2);
    WAIT_LGKM0();
      int slot = 0, kc = rot;
#pragma clang loop unroll(disable)
      for (int st = 0; st < 9; ++st) {
        BARRIER();
        SCHED0();
        frag xa[3], W0[14], W1[9];
        xa[0] = ld128(cX0 + XO); xa[1] = ld128(cX0 + XO + 2048);
#pragma unroll
        for (int f = 0; f < 4; ++f) W0[f ^ 1] = ldW(cW + 144 + HOFF(f ^ 1), cW + 144 + HOFF(f ^ 1) + PITCH);
        xa[2] = ld128(cX0);
#pragma unroll
        for (int f = 0; f < 5; ++f) { aV[0][f] = MFMA(xb[0], cv[f], aV[0][f]); aV[1][f] = MFMA(xb[1], cv[f], aV[1][f]); }
        hv = MFMA(xb[2], cv[W], hv);
        if (W == 1) he = MFMA(xb[2], cv[4], he);
        W0[5] = ldW(cW + HOFF(0), cW + HOFF(0) + PITCH);
        W0[4] = ldW(cKQ, cKQ + PITCH);
        W0[7] = ldW(cW + HOFF(2), cW + HOFF(2) + PITCH);
        W0[6] = ldW(cW + HOFF(1), cW + HOFF(1) + PITCH);
        W0[9] = ldW(cW + 288 + HOFF(0), cW + 288 + HOFF(0) + PITCH);
        W0[8] = ldW(cW + HOFF(3), cW + HOFF(3) + PITCH);
        W0[11] = ldW(cW + 288 + HOFF(2), cW + 288 + HOFF(2) + PITCH);
        W0[10] = ldW(cW + 288 + HOFF(1), cW + 288 + HOFF(1) + PITCH);
        W0[13] = ldW(cV[0], cV[1]);
        W0[12] = ldW(cW + 288 + HOFF(3), cW + 288 + HOFF(3) + PITCH);
        xb[0] = ld128(cX1 + XO); xb[1] = ld128(cX1 + XO + 2048); xb[2] = ld128(cX1);
#pragma unroll
        for (int f = 0; f < 4; ++f) W1[f ^ 1] = ldW(cW + KO + 144 + HOFF(f ^ 1), cW + KO + 144 + HOFF(f ^ 1) + PITCH);
        W1[5] = ldW(cW + KO + HOFF(0), cW + KO + HOFF(0) + PITCH);
        W1[4] = ldW(cKQ + KO, cKQ + KO + PITCH);
        W1[8] = ldW(cW + KO + HOFF(3), cW + KO + HOFF(3) + PITCH);
        W1[7] = ldW(cW + KO + HOFF(2), cW + KO + HOFF(2) + PITCH);
        W1[6] = ldW(cW + KO + HOFF(1), cW + KO + HOFF(1) + PITCH);
#pragma unroll
        for (int f = 0; f < 4; ++f) cv[f] = ldW(cW + KO + 288 + HOFF(f), cW + KO + 288 + HOFF(f) + PITCH);
        cv[4] = ldW(cV[2], cV[3]);
#pragma unroll
        for (int f = 0; f < 4; ++f) { aK[0][f] = MFMA(W0[f], xa[0], aK[0][f]); aK[1][f] = MFMA(W0[f], xa[1], aK[1][f]); }
        hk = MFMA(W0[W], xa[2], hk);
        aKQ[0] = MFMA(W0[4], xa[0], aKQ[0]); aKQ[1] = MFMA(W0[4], xa[1], aKQ[1]);
        if (W == 0) he = MFMA(W0[4], xa[2], he);
#pragma unroll
        for (int f = 0; f < 4; ++f) { aQ[0][f] = MFMA(W0[5 + f], xa[0], aQ[0][f]); aQ[1][f] = MFMA(W0[5 + f], xa[1], aQ[1][f]); }
#pragma unroll
        for (int f = 0; f < 5; ++f) { aV[0][f] = MFMA(xa[0], W0[9 + f], aV[0][f]); aV[1][f] = MFMA(xa[1], W0[9 + f], aV[1][f]); }
        hv = MFMA(xa[2], W0[9 + W], hv);
        if (W == 1) he = MFMA(xa[2], W0[13], he);
#pragma unroll
        for (int f = 0; f < 4; ++f) { aK[0][f] = MFMA(W1[f], xb[0], aK[0][f]); aK[1][f] = MFMA(W1[f], xb[1], aK[1][f]); }
        hk = MFMA(W1[W], xb[2], hk);
        aKQ[0] = MFMA(W1[4], xb[0], aKQ[0]); aKQ[1] = MFMA(W1[4], xb[1], aKQ[1]);
        if (W == 0) he = MFMA(W1[4], xb[2], he);
#pragma unroll
        for (int f = 0; f < 4; ++f) { aQ[0][f] = MFMA(W1[5 + f], xb[0], aQ[0][f]); aQ[1][f] = MFMA(W1[5 + f], xb[1], aQ[1][f]); }
        const int ds = slot == 2 ? -2 * STG : STG, dg = kc == 8 ? -8 * 512 : 512;
        slot = slot == 2 ? 0 : slot + 1;
        kc = kc == 8 ? 0 : kc + 1;
        const int dv = p < 2 ? ds : dg;
        cW += ds; cKQ += ds; cX0 += ds; cX1 += ds;
#pragma unroll
        for (int i = 0; i < 4; ++i) cV[i] += dv;
        SGB(0x100, 2);
#pragma unroll
        for (int i = 0; i < 54; ++i) {
          SGB(0x8, 1);
          if (i < 3 || i == 16 || i == 28 || i == 40) SGB(0x100, 2); else SGB(0x100, 1);
          if (i >= 3 && i < 11) SGB(0x4, 1);
          if (i >= 51) SGB(0x2, 1);
        }
#pragma unroll
        for (int i = 0; i < 4; ++i) { SGB(0x8, 1); SGB(0x2, 2); }
        SGB(0x8, W < 2 ? 4 : 2);
        SCHED0();
        WAIT_LGKM0();
      }
      hv = MFMA(xb[2], cv[W], hv);
      if (W == 1) he = MFMA(xb[2], cv[4], he);
      if (W < 2) {
        u32x2 v = pack4(he);
        if (W == 0 && tile == 0) v = u32x2{0u, 0u};
        st64(L + EX_OFF + lane * 8 + (W == 0 ? 4 : 9) * 512, v);
      }
    };
    if (wave == 0) mainloop(std::integral_constant<int, 0>{});
    else if (wave == 1) mainloop(std::integral_constant<int, 1>{});
    else if (wave == 2) mainloop(std::integral_constant<int, 2>{});
    else mainloop(std::integral_constant<int, 3>{});
    WAIT_VM0();
    STAMP(12);
    const bool g0 = g == 0;
    frag K[3][4], Q[2][4], Vb[5], Va[5];
    u32x2 ks[2], qs[2];
    aV[1][4] = MFMA(xb[1], cv[4], aV[1][4]); PIN(aV[1][4]);
    aV[0][4] = MFMA(xb[0], cv[4], aV[0][4]); PIN(aV[0][4]);
#pragma unroll
    for (int t = 1; t >= 0; --t) {
      u32x2 kq = pack4(aKQ[t]);
      auto r0 = __builtin_amdgcn_permlane32_swap(kq[0], 0u, false, false);
      auto r1 = __builtin_amdgcn_permlane32_swap(kq[1], 0u, false, false);
      ks[t] = u32x2{r0[0], r1[0]}; qs[t] = u32x2{r0[1], r1[1]};
      PIN(ks[t]); PIN(qs[t]);
    }
    SCHED0();
#pragma unroll
    for (int f = 0; f < 4; ++f) { aV[1][f] = MFMA(xb[1], cv[f], aV[1][f]); PIN(aV[1][f]); K[2][f] = cat(pack4(aK[1][f]), ks[1]); PIN(K[2][f]); SCHED0(); }
    {
      u32x2 h = pack4(hk), hh = pack4(hv);
      if (tile == 0) h = u32x2{0u, 0u};
      WR2(exh, h, hh, 0, 5);
    }
    if (c >= 8) { *(LDSP f32x4*)(size_t)(exgw) = aV[0][4]; *(LDSP f32x4*)(size_t)(exgw + 64) = aV[1][4]; }
    SCHED0();
#pragma unroll
    for (int f = 0; f < 4; ++f) {
      aV[0][f] = MFMA(xb[0], cv[f], aV[0][f]); PIN(aV[0][f]);
      u32x2 v = pack4(aV[1][f]); Vb[f][2] = v[0]; Vb[f][3] = v[1]; PIN(Vb[f]); SCHED0();
    }
    { u32x2 v = pack4(aV[1][4]); Vb[4][2] = v[0]; Vb[4][3] = v[1]; PIN(Vb[4]); }
    if (wave < 3) {
#pragma unroll
      for (int f = 0; f < 4; ++f) WR2(exr, (u32x2{K[2][f][0], K[2][f][1]}), (u32x2{Vb[f][2], Vb[f][3]}), 10 + f, 15 + f);
      WR2(exr, ks[1], (u32x2{Vb[4][2], Vb[4][3]}), 14, 19);
    }
    SCHED0();
#pragma unroll
    for (int h = 0; h < 4; ++h) {
      K[1][h] = cat(pack4(aK[0][h]), ks[0]); PIN(K[1][h]);
      u32x2 qt = pack4(aQ[1][h]);
      unsigned e = ((h < 2) == g0) ? qs[1][h & 1] : 0u;
      Q[1][h] = (h & 1) ? frag{qt[0], qt[1], 0u, e} : frag{qt[0], qt[1], e, 0u}; PIN(Q[1][h]);
    }
    SCHED0();
    GAS u64* myflag = (GAS u64*)(ws + FLAG_OFF + ((grp >> 2) * 64 + tile * 4 + cw) * FS1 + (grp & 3) * FS2);
    PINS(myflag);
    TAG_SCALAR();
    WAIT_LGKM0();
    STAMP(23);
    BARRIER();
    STAMP(24);
    SCHED0();
    u32x2 gz;
    asm volatile("ds_read2_b32 %0, %1 offset0:0 offset1:16" : "=v"(gz) : "v"(exgr) : "memory");
#pragma unroll
    for (int h = 0; h < 4; ++h) asm volatile("ds_read2st64_b64 %0, %1 offset0:%2 offset1:4" : "=v"(K[0][h]) : "v"(exr), "i"(h) : "memory");
    f32x4 sc[2][4], sp[2][4];
#pragma unroll
    for (int h = 0; h < 4; ++h) {
      sc[1][h] = MFMA(K[2][h], Q[1][h], maskC); PIN(sc[1][h]);
      sp[1][h] = MFMA(K[1][h], Q[1][h], maskP); PIN(sp[1][h]);
      u32x2 qt = pack4(aQ[0][h]);
      unsigned e = ((h < 2) == g0) ? qs[0][h & 1] : 0u;
      Q[0][h] = (h & 1) ? frag{qt[0], qt[1], 0u, e} : frag{qt[0], qt[1], e, 0u}; PIN(Q[0][h]);
      SCHED0();
    }
    asm volatile("s_waitcnt lgkmcnt(0)" : "+v"(gz), "+v"(K[0][0]), "+v"(K[0][1]), "+v"(K[0][2]), "+v"(K[0][3])::"memory");
    float v[2][4][2];
#pragma unroll
    for (int h = 0; h < 4; ++h) {
      sc[0][h] = MFMA(K[1][h], Q[0][h], maskC); PIN(sc[0][h]);
      sp[0][h] = MFMA(K[0][h], Q[0][h], maskP0); PIN(sp[0][h]);
      v[1][h][0] = fmx(fmx3(sp[1][h][0], sp[1][h][1], sc[1][h][0]), sc[1][h][1]); PIN(v[1][h][0]);
      v[1][h][1] = fmx(fmx3(sp[1][h][2], sp[1][h][3], sc[1][h][2]), sc[1][h][3]); PIN(v[1][h][1]);
      SCHED0();
    }
#pragma unroll
    for (int f = 0; f < 5; ++f) {
      u32x2 a = pack4(aV[0][f]); Vb[f][0] = a[0]; Vb[f][1] = a[1]; PIN(Vb[f]);
      Va[f] = __builtin_shufflevector(Vb[f], Vb[f], -1, -1, 0, 1); PIN(Va[f]);
    }
#pragma unroll
    for (int f = 0; f < 5; ++f) { u32x2 n = *(volatile LDSP u32x2*)(size_t)(exr + (5 + f) * 512); Va[f][0] = n[0]; Va[f][1] = n[1]; }
    u32x4 pk[2];
    auto softmax = [&](float (&w)[4][2], float gate_logit) {
      swap32(w[0][0], w[2][0]); swap32(w[1][0], w[3][0]); swap32(w[0][1], w[2][1]); swap32(w[1][1], w[3][1]);
      swap16(w[0][0], w[1][0]); swap16(w[2][0], w[3][0]); swap16(w[0][1], w[1][1]); swap16(w[2][1], w[3][1]);
      const float cs = 0.23570226039551584f * 1.4426950408889634f;
      float m = fmx(fmx3(fmx3(w[0][0], w[1][0], w[2][0]), w[3][0], w[0][1]), fmx3(w[1][1], w[2][1], w[3][1]));
      f32x2 e[4];
#pragma unroll
      for (int i = 0; i < 4; ++i) {
        f32x2 a = f32x2{w[i][0], w[i][1]} - m;
        PIN(a);
        a *= cs;
        e[i] = f32x2{__builtin_amdgcn_exp2f(a[0]), __builtin_amdgcn_exp2f(a[1])};
      }
      f32x2 s2 = (e[0] + e[1]) + (e[2] + e[3]);
      float s = s2[0] + s2[1];
      float r = __builtin_amdgcn_rcpf(__builtin_fmaf(s, __builtin_amdgcn_exp2f(gate_logit * sg), s));
      float pf[4];
#pragma unroll
      for (int i = 0; i < 4; ++i) { f32x2 pp = e[i] * r; pf[i] = __uint_as_float(cvtpk(pp[0], pp[1])); }
      swap32(pf[0], pf[2]); swap32(pf[1], pf[3]);
      swap16(pf[0], pf[1]); swap16(pf[2], pf[3]);
      return u32x4{__float_as_uint(pf[0]), __float_as_uint(pf[1]), __float_as_uint(pf[2]), __float_as_uint(pf[3])};
    };
    SCHED0();
#pragma unroll
    for (int h = 0; h < 4; ++h) {
      v[0][h][0] = fmx(fmx3(sp[0][h][0], sp[0][h][1], sc[0][h][0]), sc[0][h][1]);
      v[0][h][1] = fmx(fmx3(sp[0][h][2], sp[0][h][3], sc[0][h][2]), sc[0][h][3]);
    }
    pk[1] = softmax(v[1], __uint_as_float(gz[1]));
#pragma unroll
    for (int h = 0; h < 4; ++h) { PIN(v[0][h][0]); PIN(v[0][h][1]); }
    SCHED0();
    pv_store(pk[1], Vb, sel, vmain, vleft, ors, osoff1, oloff1, g0);
    pk[0] = softmax(v[0], __uint_as_float(gz[0]));
    SCHED0();
    pv_store(pk[0], Va, sel, vmain, vleft, ors, osoff, oloff, g0);
    STAMP(25);
    WAIT_VM0();
    if (lane == 0) __hip_atomic_store(myflag, tag, RLX_AGENT);
    STAMP(26);
    RSTAMP(61);
    asm volatile(".Lfk_tail:");
  }
  BARRIER();
  if (!act2) {
    if (bid == 255) {
      if (wave < 4) {
        const int b = wave * 64 + lane;
        GAS u64* fp = (GAS u64*)(ws + FLAG_OFF + (((b & 15) >> 2) * 64 + (b >> 4) * 4) * FS1 + (b & 3) * FS2);
#pragma clang loop unroll(disable)
        for (unsigned spins = 0; spins < (1u << 17); ++spins) {
          u64 v = __hip_atomic_load(fp, RLX_AGENT);
          if (__all(v == tag)) break;
          __builtin_amdgcn_s_sleep(8);
        }
      }
      BARRIER();
      if (tid == 0) __hip_atomic_store((GAS u64*)(ws + EPOCH_OFF), tag, RLX_AGENT);
    }
    return;
  }
  const int kq = (wave + (wave < 4 ? KSH : 0)) & 3;
  if (kq == 3) {
#pragma unroll
    for (int j = 0; j < 4; ++j) { wo_piece(3, 7, j); wo_piece(3, 8, j); }
  }
  unsigned ra[2][2][2];
#pragma unroll
  for (int it = 0; it < 2; ++it)
#pragma unroll
    for (int hf = 0; hf < 2; ++hf)
#pragma unroll
      for (int wp = 0; wp < 2; ++wp) {
        ra[it][hf][wp] = L + wp * 73728 + (wave * 16 + it * 8 + vl) * 256 + ((((lane & 7) * 2 + hf) ^ (it * 8 + vl)) * 16);
        PIN(ra[it][hf][wp]);
      }
  GAS char* op = (GAS char*)out + (size_t)(rt * 80 + wave * 16 + vl) * XROW + ct * 128 + (lane & 7) * 16;
  GAS char* op1 = op + 8 * XROW;
  PIN(op); PIN(op1);
  if (wave < 4) phase2<2>(L, ws, ors, tag, rt * 5 + 3, 48, kq, rot2, lane STAMP_PASS);
  else phase2<3>(L, ws, ors, tag, rt * 5, 0, kq, rot2, lane STAMP_PASS);
  WAIT_LGKM0();
  BARRIER();
  STAMP(30);
  if (wave < 5 && rt * 5 + wave < 128) {
    f32x4 u[2][2][4];
#pragma unroll
    for (int it = 0; it < 2; ++it)
#pragma unroll
      for (int hf = 0; hf < 2; ++hf)
#pragma unroll
        for (int w = 0; w < 4; ++w) u[it][hf][w] = *(LDSP f32x4*)(size_t)(ra[it][hf][w >> 1] + (w & 1) * 36864);
    SCHED0();
#pragma unroll
    for (int it = 0; it < 2; ++it) {
      f32x4 s0 = (u[it][0][0] + u[it][0][1]) + (u[it][0][2] + u[it][0][3]), s1 = (u[it][1][0] + u[it][1][1]) + (u[it][1][2] + u[it][1][3]);
      u32x4 o = {cvtpk(s0[0], s0[1]), cvtpk(s0[2], s0[3]), cvtpk(s1[0], s1[1]), cvtpk(s1[2], s1[3])};
      *(GAS u32x4*)(it ? op1 : op) = o;
      SCHED0();
    }
  }
  STAMP(31);
  RSTAMP(63);
  STAMP_OUT();
}

#undef x
#undef wA
#undef wB
#undef woA
#undef woB
#undef gw
#undef ws
#undef out
#undef ors

extern "C" void kernel_launch(void* const* d_in, const int* in_sizes, int n_in, void* d_out, int out_size, void* d_ws, size_t ws_size,
                              hipStream_t stream) {
  fused_kernel<<<256, 512, 0, stream>>>((const u16*)d_in[0], (const u16*)d_in[1], (const u16*)d_in[4], (const u16*)d_in[2], (const u16*)d_in[5],
                                        (const u16*)d_in[6], (char*)d_ws, (u16*)d_out);
}
```

```cpp
#define BXH 1
#include <hip/hip_runtime.h>
#include <hip/hip_bf16.h>
#include <stdint.h>
#include <type_traits>

typedef unsigned short u16;
typedef unsigned long long u64;
typedef short s16x4 __attribute__((ext_vector_type(4)));
typedef float f32x2 __attribute__((ext_vector_type(2)));
typedef float f32x4 __attribute__((ext_vector_type(4)));
typedef unsigned u32x2 __attribute__((ext_vector_type(2)));
typedef unsigned u32x4 __attribute__((ext_vector_type(4)));
typedef u32x4 frag;
typedef __bf16 bf16x2 __attribute__((ext_vector_type(2)));
typedef __bf16 bfx8 __attribute__((ext_vector_type(8)));
#define LDSP __attribute__((address_space(3)))
#define GAS __attribute__((address_space(1)))
#define DI __device__ __forceinline__

constexpr int WROW = 3456;
constexpr int XROW = 1152;
constexpr int PITCH = 432;
constexpr int KO = 32 * PITCH;
constexpr int STG_W = 64 * PITCH;
constexpr int STG = STG_W + 144 * 128;
constexpr int G_OFF = 3 * STG;
constexpr int SCR_OFF = G_OFF + 4608;
constexpr int EX_OFF = 143360;
constexpr int LDS_BYTES = 163840;
constexpr int O_BYTES = 128 * 36 * 1024;
constexpr int FLAG_OFF = 8 << 20;
constexpr int EPOCH_OFF = FLAG_OFF + (1 << 20);
#define ROT 11
#define PRE0 5
#define NBX 22
#define FS1 128
#define FS2 8
#define PSLEEP 1
#define CODEPF 22656
#define NB1 5

#define MFMA(a, b, c) __builtin_amdgcn_mfma_f32_16x16x32_bf16(__builtin_bit_cast(bfx8, (a)), __builtin_bit_cast(bfx8, (b)), (c), 0, 0, 0)
#define RLX_AGENT __ATOMIC_RELAXED, __HIP_MEMORY_SCOPE_AGENT
#define HOFF(h) (36 * (h) + ((h) & 1) * 4)

DI void glds16(unsigned voff, const GAS void* sbase, unsigned lds_addr) {
  asm volatile("s_mov_b32 m0, %2\n\ts_nop 0\n\tglobal_load_lds_dwordx4 %0, %1" ::"v"(voff), "s"(sbase), "s"(lds_addr) : "memory");
}
DI u32x2 trd(unsigned a) { return __builtin_bit_cast(u32x2, __builtin_amdgcn_ds_read_tr16_b64_v4i16((LDSP s16x4*)(size_t)a)); }
DI frag ldW(unsigned a0, unsigned a1) {
  u32x2 lo = trd(a0), hi = trd(a1);
  return __builtin_shufflevector(lo, hi, 0, 1, 2, 3);
}
DI frag ld128(unsigned a) { return *(LDSP frag*)(size_t)a; }
DI u32x2 ld64(unsigned a) { return *(LDSP u32x2*)(size_t)a; }
DI void st64(unsigned a, u32x2 v) { *(LDSP u32x2*)(size_t)a = v; }
DI unsigned cvtpk(float a, float b) {
  f32x2 f = {a, b};
  return __builtin_bit_cast(unsigned, __builtin_convertvector(f, bf16x2));
}
DI u32x2 pack4(f32x4 a) { return u32x2{cvtpk(a[0], a[1]), cvtpk(a[2], a[3])}; }
DI frag cat(u32x2 a, u32x2 b) { return __builtin_shufflevector(a, b, 0, 1, 2, 3); }
DI void swap32(float& a, float& b) {
  auto r = __builtin_amdgcn_permlane32_swap(__float_as_uint(a), __float_as_uint(b), false, false);
  a = __uint_as_float(r[0]); b = __uint_as_float(r[1]);
}
DI void swap16(float& a, float& b) {
  auto r = __builtin_amdgcn_permlane16_swap(__float_as_uint(a), __float_as_uint(b), false, false);
  a = __uint_as_float(r[0]); b = __uint_as_float(r[1]);
}
DI float fmx(float a, float b) { return __builtin_elementwise_maximum(a, b); }
DI float fmx3(float a, float b, float c) { return fmx(fmx(a, b), c); }
#define STAMP(i)
#define RSTAMP(i)
#define STAMP_OUT()
#define STAMP_ARG
#define STAMP_PASS
#define STAMP_DECL
#define SGB(m, n) __builtin_amdgcn_sched_group_barrier(m, n, 0)
#define SCHED0() __builtin_amdgcn_sched_barrier(0)
#define WAIT_LGKM0() do { __builtin_amdgcn_s_waitcnt(0xC07F); asm volatile("" ::: "memory"); } while (0)
#define WAIT_VM0() asm volatile("s_waitcnt vmcnt(0)" ::: "memory")
#define BARRIER() __builtin_amdgcn_s_barrier()
#define PIN(x) asm volatile("" : "+v"(x))
#define TAG_SCALAR() do { unsigned lo_ = __builtin_amdgcn_readfirstlane((unsigned)tag), hi_ = __builtin_amdgcn_readfirstlane((unsigned)(tag >> 32)); \
    tag = (((u64)hi_ << 32) | lo_) + 1; asm volatile("" : "+s"(tag)); } while (0)
#define PINS(x) asm volatile("" : "+s"(x))
#define WR2(addr, a, b, o0, o1) asm volatile("ds_write2st64_b64 %0, %1, %2 offset0:%3 offset1:%4" ::"v"(addr), "v"(a), "v"(b), "i"(o0), "i"(o1) : "memory")

DI void pv_store(u32x4 pk, const frag (&V)[5], u32x4 sel, unsigned vmain, unsigned vleft, __amdgpu_buffer_rsrc_t ors, unsigned so, unsigned sl, bool g0) {
  const f32x4 z = {0.f, 0.f, 0.f, 0.f};
  frag P[4];
  f32x4 o[4], os[4];
  auto mkP = [&](int h) {
    unsigned u = pk[h];
    P[h] = frag{__builtin_amdgcn_perm(u, u, sel[0]), __builtin_amdgcn_perm(u, u, sel[1]), __builtin_amdgcn_perm(u, u, sel[2]), __builtin_amdgcn_perm(u, u, sel[3])};
    PIN(P[h]);
  };
  mkP(0); mkP(1);
  SCHED0();
  u32x4 f0, f1;
  os[0] = MFMA(V[4], P[0], z); PIN(os[0]); o[0] = MFMA(V[0], P[0], z); PIN(o[0]); mkP(2); SCHED0();
  os[1] = MFMA(V[4], P[1], z); PIN(os[1]); o[1] = MFMA(V[1], P[1], z); PIN(o[1]); mkP(3); SCHED0();
  os[2] = MFMA(V[4], P[2], z); PIN(os[2]); os[3] = MFMA(V[4], P[3], z); PIN(os[3]);
  o[2] = MFMA(V[2], P[2], z); PIN(o[2]); o[3] = MFMA(V[3], P[3], z); PIN(o[3]); SCHED0();
  f0 = u32x4{cvtpk(o[0][0], o[0][1]), cvtpk(o[0][2], o[0][3]), cvtpk(o[1][0], o[1][1]), cvtpk(o[1][2], o[1][3])};
  PIN(f0); SCHED0();
  __builtin_amdgcn_raw_buffer_store_b128(f0, ors, vmain, so, 16);
  SCHED0();
  u32x2 f2 = {cvtpk(g0 ? os[0][0] : os[2][0], g0 ? os[0][1] : os[2][1]), cvtpk(g0 ? os[1][2] : os[3][2], g0 ? os[1][3] : os[3][3])};
  PIN(f2); SCHED0();
  f1 = u32x4{cvtpk(o[2][0], o[2][1]), cvtpk(o[2][2], o[2][3]), cvtpk(o[3][0], o[3][1]), cvtpk(o[3][2], o[3][3])};
  PIN(f1); PIN(f0); SCHED0();
  __builtin_amdgcn_raw_buffer_store_b128(f1, ors, vmain + 1024, so, 16);
  __builtin_amdgcn_raw_buffer_store_b64(f2, ors, vleft, sl, 16);
  SCHED0();
}

template <int MT>
DI void phase2(unsigned L, GAS char* ws, __amdgpu_buffer_rsrc_t ors, u64 tag, int tt0, int mrow, int kq, int rot2, int lane STAMP_ARG) {
  const int g = lane >> 4, q = (lane >> 2) & 3, p = lane & 3, c = lane & 15;
  const unsigned sw = (2 * g + (q >> 1)) & 3;
  unsigned bw[4];
#pragma unroll
  for (int n = 0; n < 4; ++n) bw[n] = L + kq * 36864 + (4 * g + q) * 128 + 8 * p + ((n ^ sw) * 32);
  frag wf[9][4];
  auto rdw = [&](int i) {
#pragma unroll
    for (int n = 0; n < 4; ++n) wf[i][n] = ldW(bw[n] + i * 4096, bw[n] + i * 4096 + 2048);
  };
  rdw(0); rdw(1); rdw(2); rdw(3); rdw(4);
  SCHED0();
  unsigned so[9], vo[MT];
#pragma unroll
  for (int i = 0; i < 9; ++i) so[i] = (i < 8 ? 8 * kq + ((i + rot2) & 7) : 32 + kq) * 1024;
#pragma unroll
  for (int m = 0; m < MT; ++m) { int tt = tt0 + m; tt = tt > 127 ? 127 : tt; vo[m] = lane * 16 + tt * 36864; }
  int tf = (lane & 4) ? tt0 + MT - 1 : tt0; tf = tf > 127 ? 127 : tf;
  GAS u64* fp = (GAS u64*)(ws + FLAG_OFF + (kq * 64 + (tf >> 1)) * FS1 + (lane & 3) * FS2);
  unsigned rb[4];
#pragma unroll
  for (int n = 0; n < 4; ++n) rb[n] = L + kq * 36864 + (mrow + c) * 256 + (((4 * n + g) ^ c) * 16);
#pragma unroll
  for (int i = 0; i < 9; ++i) PINS(so[i]);
#pragma unroll
  for (int m = 0; m < MT; ++m) PIN(vo[m]);
#pragma unroll
  for (int n = 0; n < 4; ++n) PIN(rb[n]);
  PIN(fp);
  WAIT_LGKM0();
  BARRIER();
  STAMP(27);
#pragma clang loop unroll(disable)
  for (unsigned spins = 0; spins < (1u << 17); ++spins) {
    u64 v = __hip_atomic_load(fp, RLX_AGENT);
    if (__all(v == tag)) break;
    __builtin_amdgcn_s_sleep(PSLEEP);
  }
  __builtin_amdgcn_fence(__ATOMIC_ACQUIRE, "wavefront");
  asm volatile("" ::: "memory");
  STAMP(28);
  RSTAMP(62);
  frag of[9][MT];
  constexpr int WIN = 2;
#pragma unroll
  for (int i = 0; i < WIN; ++i)
#pragma unroll
    for (int m = 0; m < MT; ++m) { of[i][m] = __builtin_amdgcn_raw_buffer_load_b128(ors, vo[m], so[i], 16); SCHED0(); }
  f32x4 acc[MT][4];
#pragma unroll
  for (int i = 0; i < 8; ++i) {
    const bool rd = i >= 2 && i + 3 < 9;
    if (rd) rdw(i + 3);
#pragma unroll
    for (int m = 0; m < MT; ++m) {
#pragma unroll
      for (int n = 0; n < 4; ++n) acc[m][n] = MFMA(wf[i][n], of[i][m], i == 0 ? (f32x4{0, 0, 0, 0}) : acc[m][n]);
      if (i + WIN < 9) of[i + WIN][m] = __builtin_amdgcn_raw_buffer_load_b128(ors, vo[m], so[i + WIN], 16);
    }
#pragma unroll
    for (int m = 0; m < MT; ++m) {
#pragma unroll
      for (int n = 0; n < 4; ++n) { SGB(0x8, 1); if (m < 2 && rd) SGB(0x100, 1); }
      if (i + WIN < 9) SGB(0x20, 1);
    }
    SCHED0();
  }
#pragma unroll
  for (int m = 0; m <= MT; ++m) {
    if (m < MT) {
#pragma unroll
      for (int n = 0; n < 4; ++n) { acc[m][n] = MFMA(wf[8][n], of[8][m], acc[m][n]); PIN(acc[m][n]); }
    }
    SCHED0();
    if (m > 0) {
#pragma unroll
      for (int n = 0; n < 4; ++n) *(LDSP f32x4*)(size_t)(rb[n] + (m - 1) * 4096) = acc[m - 1][n];
    }
    SCHED0();
  }
  STAMP(29);
}

typedef u64 u64x8 __attribute__((ext_vector_type(8)));
extern "C" __global__ __launch_bounds__(512, 2) void fused_kernel(const u16* x_, const u16* wA_, const u16* wB_, const u16* woA_, const u16* woB_,
                                                                   const u16* gw_, char* ws_, u16* out_) {
  __shared__ __attribute__((aligned(1024))) char lds[LDS_BYTES];
  const unsigned L = (unsigned)(size_t)(LDSP char*)lds;
  const int BIDX = blockIdx.x, TIDX = threadIdx.x;
  u64x8 ka;
  asm volatile("s_load_dwordx16 %0, %1, 0x0" : "=s"(ka) : "s"(__builtin_amdgcn_kernarg_segment_ptr()));
#define KARG_WAIT() do { SCHED0(); asm volatile("s_waitcnt lgkmcnt(0)" ::: "memory"); SCHED0(); asm volatile("" : "+s"(ka)); } while (0)
#define x ((const GAS u16*)ka[0])
#define wA ((const GAS u16*)ka[1])
#define wB ((const GAS u16*)ka[2])
#define woA ((const GAS u16*)ka[3])
#define woB ((const GAS u16*)ka[4])
#define gw ((const GAS u16*)ka[5])
#define ws ((GAS char*)ka[6])
#define out ((GAS u16*)ka[7])
#define ors __builtin_amdgcn_make_buffer_rsrc((void*)ka[6], 0, O_BYTES, 0x00020000)
  const int bid = BIDX;
  const int xcd = bid & 7, idx = bid >> 3;
  const int tl = idx & 7, gl = idx >> 3;
  const int tile = (xcd & 1) * 8 + tl;
  const int grp = (xcd >> 1) * 4 + gl;
  int rot = ROT == 1 ? tl + 2 * gl : ROT == 2 ? (tl >> 1) * 2 + 2 * gl : ROT == 6 ? (tl % 3) * 3 + (gl % 3) * 3 : ROT == 7 ? (((tl >> 1) + gl) & 3) * 2 + (tl & 1 & (gl >> 1))
          : ROT == 8 ? (((tl + gl) & 3) * 9) >> 2 : ROT == 9 ? ((tl + gl + (xcd >> 1)) % 3) * 3 : ROT == 10 ? (((tl + gl) & 3) * 9 >> 2) + (xcd & 1)
          : ROT == 11 ? ((tl + gl) & 3) * 2 + ((xcd ^ (xcd >> 1)) & 1) : ROT == 12 ? ((tl + gl) % 3) * 3 + ((xcd & 1) + (xcd >> 1)) % 3
          : ROT == 13 ? ((tl + gl) & 1) * 4 + (((xcd & 1) * 2 + (xcd >> 1)) & 3) : ROT == 14 ? (tl + 2 * gl + ((xcd >> 1) & 1)) % 9
          : ROT == 15 ? (((tl + gl) & 3) * 2 + ((xcd ^ (xcd >> 1)) & 1) + (xcd >> 2)) : 0;
  if (rot >= 9) rot -= 9;
  const int tid = TIDX, lane = tid & 63;
  const int wave = __builtin_amdgcn_readfirstlane(tid >> 6);
  const int vl = lane >> 3;
  STAMP_DECL;
  STAMP(0);
  RSTAMP(60);
  if (tid < 7) {
    u64 pc0;
    unsigned len;
    asm volatile("s_getpc_b64 %0\n\ts_mov_b32 %1, .Lfk_tail-fused_kernel+128" : "=s"(pc0), "=s"(len));
    unsigned off = (idx * 7 + tid) * 128;
    off = off < len ? off : 0;
    asm volatile("s_mov_b32 m0, %2\n\ts_nop 0\n\tglobal_load_lds_dword %0, %1" ::"v"(off), "s"(pc0 & ~255ull), "s"(L + SCR_OFF) : "memory");
  }
  u64 tag;
  const int lin0 = xcd * 30 + idx;
  const bool act2 = idx < 30 && lin0 < 234;
  const int lin = act2 ? lin0 : 0;
  const int rt = (lin * 57) >> 9, ct = lin - rt * 9;
  const int rot2 = 3 * (rt - ((rt * 171) >> 9) * 3);
  const unsigned wsch = ((((lane & 7) >> 1) ^ ((vl >> 1) & 3)) << 5) | ((lane & 1) << 4);
  const unsigned offn = vl * XROW + wsch, offl = ((vl >> 2) * 72 + (vl & 3)) * XROW + wsch;
  auto wo_piece = [&](int kq, int i, int j) {
    const GAS char* b = (const GAS char*)(kq >= 2 ? woB : woA) + ct * 128 + (kq & 1) * (288 * XROW);
    if (i == 8) b += ((j & 1) * 144 + ((j >> 1) ? 52 : 16)) * XROW;
    else b += (((i + rot2) & 7) * 36 + (j >> 1) * 20 + 8 * (j & 1)) * XROW;
    glds16(i == 8 ? offl : offn, b, L + (kq * 9 + i) * 4096 + j * 1024);
  };
  const int g = lane >> 4, q = (lane >> 2) & 3, p = lane & 3, c = lane & 15;
  const int cw = wave & 3;

  if (__builtin_expect(wave >= 4, 1)) {
    __builtin_amdgcn_s_setprio(2);
    const int lw = cw, tl256 = tid - 256;
    const unsigned offx = (tile * 128 + 8 * lw + vl) * XROW + ((lane & 7) ^ ((4 * lw + (vl >> 1)) & 7)) * 16;
    KARG_WAIT();
    const GAS char* w = (const GAS char*)((grp >= 8) ? wB : wA) + (grp & 7) * 144;
    unsigned offw[7], offh;
    auto kchunk = [&](int st) { int kc = st + rot; return kc >= 9 ? kc - 9 : kc; };
    auto issueX = [&](int kc, unsigned sb, int jj) { glds16(offx, (const GAS char*)x + kc * 128 + jj * (32 * XROW), sb + STG_W + (2 + lw + 4 * jj) * 1024); };
    auto issueW = [&](int kc, unsigned sb, int j) { glds16(offw[j], w + kc * (64 * WROW), sb + (lw + 4 * j) * 1024); };
    auto issue6 = [&](int kc, unsigned sb) {
      if (lw < 3) issueW(kc, sb, 6);
      else { glds16(offw[6], (const GAS char*)x + kc * 128, sb + STG_W); glds16(offh, (const GAS char*)x + kc * 128, sb + STG_W + 1024); }
    };
    const int kc0 = rot, kc1 = kchunk(1);
    const unsigned sb0 = L, sb1 = L + STG;
    auto calcw = [&](int j) {
      unsigned qq = 256 * j + tl256;
      unsigned u = (qq * 2428u) >> 16, cc = qq - u * 27;
      unsigned row = (u & 0x38) | ((u & 1) << 2) | ((u >> 1) & 3);
      offw[j] = row * WROW + cc * 16 + ((cc * 57u) >> 9) * 1008;
    };
    issueX(kc0, sb0, 0);
    u32x2 gv;
    const unsigned gi = tl256 < 144 ? tl256 : 143;
    asm volatile("global_load_dwordx2 %0, %1, %2" : "=v"(gv) : "v"(gi * 8), "s"(gw) : "memory");
#pragma unroll
    for (int j = 0; j < 6; ++j) {
      SCHED0();
      calcw(j);
      SCHED0();
      issueW(kc0, sb0, j);
      if (j < 3) issueX(kc0, sb0, j + 1);
    }
    calcw(6);
    {
      int t0 = tile * 128 - 16 + vl, t1 = t0 + 8;
      t0 = t0 < 0 ? 0 : t0; t1 = t1 < 0 ? 0 : t1;
      unsigned h0 = t0 * XROW + ((lane & 7) ^ (vl >> 1)) * 16;
      offh = t1 * XROW + ((lane & 7) ^ (4 + (vl >> 1))) * 16;
      offw[6] = lw == 3 ? h0 : offw[6];
    }
    issue6(kc0, sb0);
    STAMP(1);
    issueW(kc1, sb1, 0);
    int slot = 0, kn = kc1;
    const GAS char* xp = (const GAS char*)x + kn * 128;
    unsigned b1 = sb1, xd = sb1 + STG_W + (2 + lw) * 1024;
    PINS(xp); PINS(xd);
    SCHED0();
#pragma unroll
    for (int j = 1; j < PRE0; ++j) issueW(kc1, sb1, j);
    STAMP(2);
    asm volatile("s_waitcnt vmcnt(%1)" : "+v"(gv) : "i"(10 + PRE0) : "memory");
    {
      unsigned a0 = __builtin_amdgcn_perm(gv[0], gv[0], 0x01000100), a1 = __builtin_amdgcn_perm(gv[0], gv[0], 0x03020302);
      unsigned a2 = __builtin_amdgcn_perm(gv[1], gv[1], 0x01000100), a3 = __builtin_amdgcn_perm(gv[1], gv[1], 0x03020302);
      *(LDSP u32x4*)(size_t)(L + G_OFF + gi * 32) = u32x4{a0, a0, a1, a1};
      *(LDSP u32x4*)(size_t)(L + G_OFF + gi * 32 + 16) = u32x4{a2, a2, a3, a3};
    }
    WAIT_LGKM0();
    asm volatile("s_waitcnt vmcnt(%0)" ::"i"(PRE0) : "memory");
#pragma clang loop unroll(disable)
    for (int st = 0; st < 7; ++st) {
      BARRIER();
      glds16(offx, xp, xd);
      SCHED0();
      STAMP(3 + st);
      glds16(offx, xp + 32 * XROW, xd + 4096);
      if (st == 0) {
#pragma unroll
        for (int j = PRE0; j < 6; ++j) issueW(kc1, sb1, j);
        tag = __hip_atomic_load((GAS u64*)(ws + EPOCH_OFF), RLX_AGENT);
      }
      issue6(kn, b1);
      glds16(offx, xp + 64 * XROW, xd + 8192);
      glds16(offx, xp + 96 * XROW, xd + 12288);
      SCHED0();
      kn = kn == 8 ? 0 : kn + 1;
      b1 = L + (slot == 0 ? 2 : slot - 1) * STG;
      issueW(kn, b1, 0); issueW(kn, b1, 1);
#pragma unroll
      for (int j = 2; j < 6; ++j) issueW(kn, b1, j);
      slot = slot == 2 ? 0 : slot + 1;
      xp = (const GAS char*)x + kn * 128;
      xd = b1 + STG_W + (2 + lw) * 1024;
      PINS(xp); PINS(xd);
      asm volatile("s_waitcnt vmcnt(6)" ::: "memory");
    }
    BARRIER();
    glds16(offx, xp, xd);
    SCHED0();
    STAMP(10);
    glds16(offx, xp + 32 * XROW, xd + 4096);
    issue6(kn, b1);
    glds16(offx, xp + 64 * XROW, xd + 8192);
    glds16(offx, xp + 96 * XROW, xd + 12288);
#pragma unroll
    for (int I = 0; I < 6; ++I) wo_piece(0, I, lw);
    asm volatile("s_waitcnt vmcnt(6)" ::: "memory");
    BARRIER();
    STAMP(11);
    wo_piece(0, 6, lw); wo_piece(0, 7, lw);
    __builtin_amdgcn_s_setprio(0);
#pragma unroll
    for (int I = 8; I < NBX; ++I) wo_piece(I / 9, I % 9, lw);
    WAIT_LGKM0();
    STAMP(12);
    BARRIER();
    STAMP(13);
#pragma unroll
    for (int I = NBX; I < 35; ++I) wo_piece(I / 9, I % 9, lw);
    WAIT_VM0();
    TAG_SCALAR();
    STAMP(14);
  } else {
    const unsigned wrow = L + (8 * g + 2 * q) * PITCH;
    f32x4 maskP, maskC, maskP0;
    {
      const float ninf = -__builtin_inff();
#pragma unroll
      for (int r = 0; r < 4; ++r) {
        bool pm = (r & 1) == (c & 1);
        maskC[r] = (pm && 4 * g + r <= c) ? 0.f : ninf;
        maskP[r] = (pm && 4 * g + r > c) ? 0.f : ninf;
      }
      maskP0 = maskP + ((tile == 0 && wave == 0) ? -998244352.f * 4.242640687119285f : 0.f);
      asm volatile("" : "+v"(maskP), "+v"(maskC), "+v"(maskP0));
    }
    u32x4 sel;
    {
      const int par = c & 1;
      const bool cur1 = 4 * g + par <= c, cur2 = 4 * g + 2 + par <= c;
      const unsigned s1 = par ? 0x01000c0cu : 0x0c0c0100u;
      const unsigned s2 = par ? 0x03020c0cu : 0x0c0c0302u;
      sel[0] = cur1 ? 0x0c0c0c0cu : s1;
      sel[1] = cur2 ? 0x0c0c0c0cu : s2;
      sel[2] = cur1 ? s1 : 0x0c0c0c0cu;
      sel[3] = cur2 ? s2 : 0x0c0c0c0cu;
    }
    unsigned vmain = lane * 16, vleft = lane < 32 ? c * 16 + g * 8 : 0x40000000u;
    unsigned osoff = (tile * 8 + 2 * wave) * 36864 + grp * 2048, oloff = (tile * 8 + 2 * wave) * 36864 + 32768 + grp * 256;
    unsigned osoff1 = osoff + 36864, oloff1 = oloff + 36864;
    PINS(osoff); PINS(oloff); PINS(osoff1); PINS(oloff1);
    asm volatile("" : "+v"(sel), "+v"(vmain), "+v"(vleft));
    GAS u64* myflag;
    unsigned sgb = (grp >= 8) ? 0x3fb8aa3bu : 0xbfb8aa3bu;
    PINS(sgb);
    const float sg = __uint_as_float(sgb);
    f32x4 aK[2][4], aQ[2][4], aKQ[2], aV[2][5], hk, hv, he;
    frag cv[5], xb[3];
    unsigned exr = L + EX_OFF + wave * 5120 + lane * 8, exh = L + EX_OFF + wave * 512 + lane * 8;
    unsigned exgw = L + SCR_OFF + wave * 128 + g * 16, exgr = L + SCR_OFF + wave * 128 + c * 4;
    PIN(exr); PIN(exh); PIN(exgw); PIN(exgr);
    auto mainloop = [&](auto WC) {
      constexpr int W = decltype(WC)::value;
      constexpr int XO = (1 + 2 * W) * 2048;
    hk = f32x4{0, 0, 0, 0}; hv = f32x4{0, 0, 0, 0}; he = f32x4{0, 0, 0, 0};
    PIN(he);
#pragma unroll
    for (int t = 0; t < 2; ++t) {
#pragma unroll
      for (int f = 0; f < 4; ++f) { aK[t][f] = f32x4{0, 0, 0, 0}; aQ[t][f] = f32x4{0, 0, 0, 0}; }
      aKQ[t] = f32x4{0, 0, 0, 0};
#pragma unroll
      for (int f = 0; f < 5; ++f) aV[t][f] = f32x4{0, 0, 0, 0};
    }
    unsigned cW = wrow + 8 * p;
    unsigned cKQ = wrow + (p < 2 ? 144 : 0) + ((p & 1) ? 104 : 32);
    unsigned cX0 = L + STG_W + c * 128 + ((g ^ (c >> 1)) * 16), cX1 = cX0 ^ 64;
    unsigned cV[4];
#pragma unroll
    for (int i = 0; i < 4; ++i)
      cV[i] = p < 2 ? wrow + 288 + (p ? 104 : 32) + (i >> 1) * KO + (i & 1) * PITCH : L + G_OFF + rot * 512 + (8 * g + q) * 8 + (i >> 1) * 256 + (i & 1) * 32;
#pragma unroll
    for (int f = 0; f < 5; ++f) cv[f] = frag{0, 0, 0, 0};
    xb[0] = frag{0, 0, 0, 0}; xb[1] = frag{0, 0, 0, 0}; xb[2] = frag{0, 0, 0, 0};
    asm volatile("" : "+v"(aK[0][0]), "+v"(aK[0][1]), "+v"(aK[0][2]), "+v"(aK[0][3]), "+v"(aK[1][0]), "+v"(aK[1][1]), "+v"(aK[1][2]), "+v"(aK[1][3]),
                      "+v"(aQ[0][0]), "+v"(aQ[0][1]), "+v"(aQ[0][2]), "+v"(aQ[0][3]), "+v"(aQ[1][0]), "+v"(aQ[1][1]), "+v"(aQ[1][2]), "+v"(aQ[1][3]),
                      "+v"(aKQ[0]), "+v"(aKQ[1]), "+v"(hk), "+v"(hv), "+v"(xb[0]), "+v"(xb[1]), "+v"(xb[2]));
    asm volatile("" : "+v"(aV[0][0]), "+v"(aV[0][1]), "+v"(aV[0][2]), "+v"(aV[0][3]), "+v"(aV[0][4]), "+v"(aV[1][0]), "+v"(aV[1][1]), "+v"(aV[1][2]),
                      "+v"(aV[1][3]), "+v"(aV[1][4]), "+v"(cv[0]), "+v"(cv[1]), "+v"(cv[2]), "+v"(cv[3]), "+v"(cv[4]),
                      "+v"(cW), "+v"(cKQ), "+v"(cX0), "+v"(cX1), "+v"(cV[0]), "+v"(cV[1]), "+v"(cV[2]), "+v"(cV[3]));
    SCHED0();
    STAMP(1);
    KARG_WAIT();
    tag = __hip_atomic_load((GAS u64*)(ws + EPOCH_OFF), RLX_AGENT);
    myflag = (GAS u64*)(ws + FLAG_OFF + ((grp >> 2) * 64 + tile * 4 + cw) * FS1 + (grp & 3) * FS2);
    PINS(myflag);
    STAMP(2);
    WAIT_LGKM0();
      int slot = 0, kc = rot;
#pragma clang loop unroll(disable)
      for (int st = 0; st < 9; ++st) {
        BARRIER();
        SCHED0();
        frag xa[3], W0[14], W1[9];
        xa[0] = ld128(cX0 + XO); xa[1] = ld128(cX0 + XO + 2048);
#pragma unroll
        for (int f = 0; f < 4; ++f) W0[f ^ 1] = ldW(cW + 144 + HOFF(f ^ 1), cW + 144 + HOFF(f ^ 1) + PITCH);
        xa[2] = ld128(cX0);
#pragma unroll
        for (int f = 0; f < 5; ++f) { aV[0][f] = MFMA(xb[0], cv[f], aV[0][f]); aV[1][f] = MFMA(xb[1], cv[f], aV[1][f]); }
        hv = MFMA(xb[2], cv[W], hv);
        if (W == 1) he = MFMA(xb[2], cv[4], he);
        W0[5] = ldW(cW + HOFF(0), cW + HOFF(0) + PITCH);
        W0[4] = ldW(cKQ, cKQ + PITCH);
        W0[7] = ldW(cW + HOFF(2), cW + HOFF(2) + PITCH);
        W0[6] = ldW(cW + HOFF(1), cW + HOFF(1) + PITCH);
        W0[9] = ldW(cW + 288 + HOFF(0), cW + 288 + HOFF(0) + PITCH);
        W0[8] = ldW(cW + HOFF(3), cW + HOFF(3) + PITCH);
        W0[11] = ldW(cW + 288 + HOFF(2), cW + 288 + HOFF(2) + PITCH);
        W0[10] = ldW(cW + 288 + HOFF(1), cW + 288 + HOFF(1) + PITCH);
        W0[13] = ldW(cV[0], cV[1]);
        W0[12] = ldW(cW + 288 + HOFF(3), cW + 288 + HOFF(3) + PITCH);
        xb[0] = ld128(cX1 + XO); xb[1] = ld128(cX1 + XO + 2048); xb[2] = ld128(cX1);
#pragma unroll
        for (int f = 0; f < 4; ++f) W1[f ^ 1] = ldW(cW + KO + 144 + HOFF(f ^ 1), cW + KO + 144 + HOFF(f ^ 1) + PITCH);
        W1[5] = ldW(cW + KO + HOFF(0), cW + KO + HOFF(0) + PITCH);
        W1[4] = ldW(cKQ + KO, cKQ + KO + PITCH);
        W1[8] = ldW(cW + KO + HOFF(3), cW + KO + HOFF(3) + PITCH);
        W1[7] = ldW(cW + KO + HOFF(2), cW + KO + HOFF(2) + PITCH);
        W1[6] = ldW(cW + KO + HOFF(1), cW + KO + HOFF(1) + PITCH);
#pragma unroll
        for (int f = 0; f < 4; ++f) cv[f] = ldW(cW + KO + 288 + HOFF(f), cW + KO + 288 + HOFF(f) + PITCH);
        cv[4] = ldW(cV[2], cV[3]);
#pragma unroll
        for (int f = 0; f < 4; ++f) { aK[0][f] = MFMA(W0[f], xa[0], aK[0][f]); aK[1][f] = MFMA(W0[f], xa[1], aK[1][f]); }
        hk = MFMA(W0[W], xa[2], hk);
        aKQ[0] = MFMA(W0[4], xa[0], aKQ[0]); aKQ[1] = MFMA(W0[4], xa[1], aKQ[1]);
        if (W == 0) he = MFMA(W0[4], xa[2], he);
#pragma unroll
        for (int f = 0; f < 4; ++f) { aQ[0][f] = MFMA(W0[5 + f], xa[0], aQ[0][f]); aQ[1][f] = MFMA(W0[5 + f], xa[1], aQ[1][f]); }
#pragma unroll
        for (int f = 0; f < 5; ++f) { aV[0][f] = MFMA(xa[0], W0[9 + f], aV[0][f]); aV[1][f] = MFMA(xa[1], W0[9 + f], aV[1][f]); }
        hv = MFMA(xa[2], W0[9 + W], hv);
        if (W == 1) he = MFMA(xa[2], W0[13], he);
#pragma unroll
        for (int f = 0; f < 4; ++f) { aK[0][f] = MFMA(W1[f], xb[0], aK[0][f]); aK[1][f] = MFMA(W1[f], xb[1], aK[1][f]); }
        hk = MFMA(W1[W], xb[2], hk);
        aKQ[0] = MFMA(W1[4], xb[0], aKQ[0]); aKQ[1] = MFMA(W1[4], xb[1], aKQ[1]);
        if (W == 0) he = MFMA(W1[4], xb[2], he);
#pragma unroll
        for (int f = 0; f < 4; ++f) { aQ[0][f] = MFMA(W1[5 + f], xb[0], aQ[0][f]); aQ[1][f] = MFMA(W1[5 + f], xb[1], aQ[1][f]); }
        const int ds = slot == 2 ? -2 * STG : STG, dg = kc == 8 ? -8 * 512 : 512;
        slot = slot == 2 ? 0 : slot + 1;
        kc = kc == 8 ? 0 : kc + 1;
        const int dv = p < 2 ? ds : dg;
        cW += ds; cKQ += ds; cX0 += ds; cX1 += ds;
#pragma unroll
        for (int i = 0; i < 4; ++i) cV[i] += dv;
        SGB(0x100, 2);
#pragma unroll
        for (int i = 0; i < 54; ++i) {
          SGB(0x8, 1);
          if (i < 3 || i == 16 || i == 28 || i == 40) SGB(0x100, 2); else SGB(0x100, 1);
          if (i >= 3 && i < 11) SGB(0x4, 1);
          if (i >= 51) SGB(0x2, 1);
        }
#pragma unroll
        for (int i = 0; i < 4; ++i) { SGB(0x8, 1); SGB(0x2, 2); }
        SGB(0x8, W < 2 ? 4 : 2);
        SCHED0();
        WAIT_LGKM0();
      }
      hv = MFMA(xb[2], cv[W], hv);
      if (W == 1) he = MFMA(xb[2], cv[4], he);
      if (W < 2) {
        u32x2 v = pack4(he);
        if (W == 0 && tile == 0) v = u32x2{0u, 0u};
        st64(L + EX_OFF + lane * 8 + (W == 0 ? 4 : 9) * 512, v);
      }
    };
    if (wave == 0) mainloop(std::integral_constant<int, 0>{});
    else if (wave == 1) mainloop(std::integral_constant<int, 1>{});
    else if (wave == 2) mainloop(std::integral_constant<int, 2>{});
    else mainloop(std::integral_constant<int, 3>{});
    WAIT_VM0();
    STAMP(12);
    TAG_SCALAR();
    const bool g0 = g == 0;
    frag KN[4], Q0[4], V[5];
    u32x2 kb[5], vb[5], ksa, qs[2];
    aV[1][4] = MFMA(xb[1], cv[4], aV[1][4]); PIN(aV[1][4]);
    aV[0][4] = MFMA(xb[0], cv[4], aV[0][4]); PIN(aV[0][4]);
    {
      u32x2 k1 = pack4(aKQ[1]), k0 = pack4(aKQ[0]);
      auto r0 = __builtin_amdgcn_permlane32_swap(k1[0], 0u, false, false);
      auto r1 = __builtin_amdgcn_permlane32_swap(k1[1], 0u, false, false);
      auto r2 = __builtin_amdgcn_permlane32_swap(k0[0], 0u, false, false);
      auto r3 = __builtin_amdgcn_permlane32_swap(k0[1], 0u, false, false);
      qs[1] = u32x2{r0[1], r1[1]}; qs[0] = u32x2{r2[1], r3[1]};
      ksa = u32x2{r2[0], r3[0]}; kb[4] = u32x2{r0[0], r1[0]};
    }
#pragma unroll
    for (int f = 0; f < 4; ++f) { aV[1][f] = MFMA(xb[1], cv[f], aV[1][f]); PIN(aV[1][f]); }
#pragma unroll
    for (int f = 0; f < 4; ++f) kb[f] = pack4(aK[1][f]);
    {
      u32x2 h = pack4(hk), hh = pack4(hv);
      if (tile == 0) h = u32x2{0u, 0u};
      WR2(exh, h, hh, 0, 5);
    }
    if (c >= 8) { *(LDSP f32x4*)(size_t)(exgw) = aV[0][4]; *(LDSP f32x4*)(size_t)(exgw + 64) = aV[1][4]; }
#pragma unroll
    for (int f = 0; f < 4; ++f) { aV[0][f] = MFMA(xb[0], cv[f], aV[0][f]); PIN(aV[0][f]); }
#pragma unroll
    for (int f = 0; f < 5; ++f) vb[f] = pack4(aV[1][f]);
    if (wave < 3) {
#pragma unroll
      for (int f = 0; f < 5; ++f) WR2(exr, kb[f], vb[f], 10 + f, 15 + f);
    }
    u32x2 gz;
    asm volatile("ds_read2_b32 %0, %1 offset0:0 offset1:16" : "=v"(gz) : "v"(exgr) : "memory");
    f32x4 sc[2][4], sp[2][4];
#pragma unroll
    for (int h = 0; h < 4; ++h) {
      u32x2 q1 = pack4(aQ[1][h]), q0 = pack4(aQ[0][h]);
      unsigned e1 = ((h < 2) == g0) ? qs[1][h & 1] : 0u, e0 = ((h < 2) == g0) ? qs[0][h & 1] : 0u;
      frag Q1 = (h & 1) ? frag{q1[0], q1[1], 0u, e1} : frag{q1[0], q1[1], e1, 0u};
      Q0[h] = (h & 1) ? frag{q0[0], q0[1], 0u, e0} : frag{q0[0], q0[1], e0, 0u};
      frag KA = cat(pack4(aK[0][h]), ksa);
      sc[1][h] = MFMA(cat(kb[h], kb[4]), Q1, maskC); PIN(sc[1][h]);
      sp[1][h] = MFMA(KA, Q1, maskP); PIN(sp[1][h]);
      sc[0][h] = MFMA(KA, Q0[h], maskC); PIN(sc[0][h]);
      if (h == BXH) {
        SCHED0();
        asm volatile("s_waitcnt lgkmcnt(0)" : "+v"(gz)::"memory");
        STAMP(23);
        BARRIER();
        STAMP(24);
        SCHED0();
#pragma unroll
        for (int k = 0; k < 4; ++k) asm volatile("ds_read2st64_b64 %0, %1 offset0:%2 offset1:4" : "=v"(KN[k]) : "v"(exr), "i"(k) : "memory");
        SCHED0();
      }
    }
    float v[2][4][2];
#pragma unroll
    for (int h = 0; h < 4; ++h) {
      v[1][h][0] = fmx(fmx3(sp[1][h][0], sp[1][h][1], sc[1][h][0]), sc[1][h][1]);
      v[1][h][1] = fmx(fmx3(sp[1][h][2], sp[1][h][3], sc[1][h][2]), sc[1][h][3]);
    }
    asm volatile("s_waitcnt lgkmcnt(0)" : "+v"(KN[0]), "+v"(KN[1]), "+v"(KN[2]), "+v"(KN[3]), "+v"(v[1][0][0]), "+v"(v[1][0][1]), "+v"(v[1][1][0]), "+v"(v[1][1][1]),
                 "+v"(v[1][2][0]), "+v"(v[1][2][1]), "+v"(v[1][3][0]), "+v"(v[1][3][1])::"memory");
#pragma unroll
    for (int h = 0; h < 4; ++h) { sp[0][h] = MFMA(KN[h], Q0[h], maskP0); PIN(sp[0][h]); }
#pragma unroll
    for (int f = 0; f < 5; ++f) V[f] = cat(vb[f], pack4(aV[0][f]));
    u32x4 pk[2];
    auto softmax = [&](float (&w)[4][2], float gate_logit) {
      swap32(w[0][0], w[2][0]); swap32(w[1][0], w[3][0]); swap32(w[0][1], w[2][1]); swap32(w[1][1], w[3][1]);
      swap16(w[0][0], w[1][0]); swap16(w[2][0], w[3][0]); swap16(w[0][1], w[1][1]); swap16(w[2][1], w[3][1]);
      const float cs = 0.23570226039551584f * 1.4426950408889634f;
      float m = fmx(fmx3(fmx3(w[0][0], w[1][0], w[2][0]), w[3][0], w[0][1]), fmx3(w[1][1], w[2][1], w[3][1]));
      f32x2 e[4];
#pragma unroll
      for (int i = 0; i < 4; ++i) {
        f32x2 a = (f32x2{w[i][0], w[i][1]} - m) * cs;
        e[i] = f32x2{__builtin_amdgcn_exp2f(a[0]), __builtin_amdgcn_exp2f(a[1])};
      }
      f32x2 s2 = (e[0] + e[1]) + (e[2] + e[3]);
      float s = s2[0] + s2[1];
      float r = __builtin_amdgcn_rcpf(__builtin_fmaf(s, __builtin_amdgcn_exp2f(gate_logit * sg), s));
      float pf[4];
#pragma unroll
      for (int i = 0; i < 4; ++i) { f32x2 pp = e[i] * r; pf[i] = __uint_as_float(cvtpk(pp[0], pp[1])); }
      swap32(pf[0], pf[2]); swap32(pf[1], pf[3]);
      swap16(pf[0], pf[1]); swap16(pf[2], pf[3]);
      return u32x4{__float_as_uint(pf[0]), __float_as_uint(pf[1]), __float_as_uint(pf[2]), __float_as_uint(pf[3])};
    };
    SCHED0();
#pragma unroll
    for (int h = 0; h < 4; ++h) {
      v[0][h][0] = fmx(fmx3(sp[0][h][0], sp[0][h][1], sc[0][h][0]), sc[0][h][1]);
      v[0][h][1] = fmx(fmx3(sp[0][h][2], sp[0][h][3], sc[0][h][2]), sc[0][h][3]);
    }
    pk[1] = softmax(v[1], __uint_as_float(gz[1]));
#pragma unroll
    for (int h = 0; h < 4; ++h) { PIN(v[0][h][0]); PIN(v[0][h][1]); }
    SCHED0();
    pv_store(pk[1], V, u32x4{sel[2], sel[3], sel[0], sel[1]}, vmain, vleft, ors, osoff1, oloff1, g0);
#pragma unroll
    for (int f = 0; f < 5; ++f) { u32x2 n = *(volatile LDSP u32x2*)(size_t)(exr + (5 + f) * 512); V[f][0] = n[0]; V[f][1] = n[1]; }
    SCHED0();
    pk[0] = softmax(v[0], __uint_as_float(gz[0]));
    SCHED0();
    pv_store(pk[0], V, sel, vmain, vleft, ors, osoff, oloff, g0);
    STAMP(25);
    WAIT_VM0();
    if (lane == 0) __hip_atomic_store(myflag, tag, RLX_AGENT);
    STAMP(26);
    RSTAMP(61);
    asm volatile(".Lfk_tail:");
  }
  BARRIER();
  if (!act2) {
    if (bid == 255) {
      if (wave < 4) {
        const int b = wave * 64 + lane;
        GAS u64* fp = (GAS u64*)(ws + FLAG_OFF + (((b & 15) >> 2) * 64 + (b >> 4) * 4) * FS1 + (b & 3) * FS2);
#pragma clang loop unroll(disable)
        for (unsigned spins = 0; spins < (1u << 17); ++spins) {
          u64 v = __hip_atomic_load(fp, RLX_AGENT);
          if (__all(v == tag)) break;
          __builtin_amdgcn_s_sleep(8);
        }
      }
      BARRIER();
      if (tid == 0) __hip_atomic_store((GAS u64*)(ws + EPOCH_OFF), tag, RLX_AGENT);
    }
    return;
  }
  if (cw == 3) {
#pragma unroll
    for (int j = 0; j < 4; ++j) wo_piece(3, 8, j);
  }
  unsigned ra[2][2][2];
#pragma unroll
  for (int it = 0; it < 2; ++it)
#pragma unroll
    for (int hf = 0; hf < 2; ++hf)
#pragma unroll
      for (int wp = 0; wp < 2; ++wp) {
        ra[it][hf][wp] = L + wp * 73728 + (wave * 16 + it * 8 + vl) * 256 + ((((lane & 7) * 2 + hf) ^ (it * 8 + vl)) * 16);
        PIN(ra[it][hf][wp]);
      }
  GAS char* op = (GAS char*)out + (size_t)(rt * 80 + wave * 16 + vl) * XROW + ct * 128 + (lane & 7) * 16;
  GAS char* op1 = op + 8 * XROW;
  PIN(op); PIN(op1);
  if (wave < 4) phase2<2>(L, ws, ors, tag, rt * 5 + 3, 48, cw, rot2, lane STAMP_PASS);
  else phase2<3>(L, ws, ors, tag, rt * 5, 0, cw, rot2, lane STAMP_PASS);
  WAIT_LGKM0();
  BARRIER();
  STAMP(30);
  if (wave < 5 && rt * 5 + wave < 128) {
    f32x4 u[2][2][4];
#pragma unroll
    for (int it = 0; it < 2; ++it)
#pragma unroll
      for (int hf = 0; hf < 2; ++hf)
#pragma unroll
        for (int w = 0; w < 4; ++w) u[it][hf][w] = *(LDSP f32x4*)(size_t)(ra[it][hf][w >> 1] + (w & 1) * 36864);
    SCHED0();
#pragma unroll
    for (int it = 0; it < 2; ++it) {
      f32x4 s0 = (u[it][0][0] + u[it][0][1]) + (u[it][0][2] + u[it][0][3]), s1 = (u[it][1][0] + u[it][1][1]) + (u[it][1][2] + u[it][1][3]);
      u32x4 o = {cvtpk(s0[0], s0[1]), cvtpk(s0[2], s0[3]), cvtpk(s1[0], s1[1]), cvtpk(s1[2], s1[3])};
      *(GAS u32x4*)(it ? op1 : op) = o;
      SCHED0();
    }
  }
  STAMP(31);
  RSTAMP(63);
  STAMP_OUT();
}

#undef x
#undef wA
#undef wB
#undef woA
#undef woB
#undef gw
#undef ws
#undef out
#undef ors

extern "C" void kernel_launch(void* const* d_in, const int* in_sizes, int n_in, void* d_out, int out_size, void* d_ws, size_t ws_size,
                              hipStream_t stream) {
  fused_kernel<<<256, 512, 0, stream>>>((const u16*)d_in[0], (const u16*)d_in[1], (const u16*)d_in[4], (const u16*)d_in[2], (const u16*)d_in[5],
                                        (const u16*)d_in[6], (char*)d_ws, (u16*)d_out);
}
```

```cpp
#define BXH 1
#include <hip/hip_runtime.h>
#include <hip/hip_bf16.h>
#include <stdint.h>
#include <type_traits>

typedef unsigned short u16;
typedef unsigned long long u64;
typedef short s16x4 __attribute__((ext_vector_type(4)));
typedef float f32x2 __attribute__((ext_vector_type(2)));
typedef float f32x4 __attribute__((ext_vector_type(4)));
typedef unsigned u32x2 __attribute__((ext_vector_type(2)));
typedef unsigned u32x4 __attribute__((ext_vector_type(4)));
typedef u32x4 frag;
typedef __bf16 bf16x2 __attribute__((ext_vector_type(2)));
typedef __bf16 bfx8 __attribute__((ext_vector_type(8)));
#define LDSP __attribute__((address_space(3)))
#define GAS __attribute__((address_space(1)))
#define DI __device__ __forceinline__

constexpr int WROW = 3456;
constexpr int XROW = 1152;
constexpr int PITCH = 432;
constexpr int KO = 32 * PITCH;
constexpr int STG_W = 64 * PITCH;
constexpr int STG = STG_W + 144 * 128;
constexpr int G_OFF = 3 * STG;
constexpr int SCR_OFF = G_OFF + 4608;
constexpr int EX_OFF = 143360;
constexpr int LDS_BYTES = 163840;
constexpr int O_BYTES = 128 * 36 * 1024;
constexpr int FLAG_OFF = 8 << 20;
constexpr int EPOCH_OFF = FLAG_OFF + (1 << 20);
#define ROT 11
#define PRE0 5
#define NBX 22
#define FS1 128
#define FS2 8
#define PSLEEP 1
#define CODEPF 22656
#define NB1 5

#define MFMA(a, b, c) __builtin_amdgcn_mfma_f32_16x16x32_bf16(__builtin_bit_cast(bfx8, (a)), __builtin_bit_cast(bfx8, (b)), (c), 0, 0, 0)
#define RLX_AGENT __ATOMIC_RELAXED, __HIP_MEMORY_SCOPE_AGENT
#define HOFF(h) (36 * (h) + ((h) & 1) * 4)

DI void glds16(unsigned voff, const GAS void* sbase, unsigned lds_addr) {
  asm volatile("s_mov_b32 m0, %2\n\ts_nop 0\n\tglobal_load_lds_dwordx4 %0, %1" ::"v"(voff), "s"(sbase), "s"(lds_addr) : "memory");
}
DI u32x2 trd(unsigned a) { return __builtin_bit_cast(u32x2, __builtin_amdgcn_ds_read_tr16_b64_v4i16((LDSP s16x4*)(size_t)a)); }
DI frag ldW(unsigned a0, unsigned a1) {
  u32x2 lo = trd(a0), hi = trd(a1);
  return __builtin_shufflevector(lo, hi, 0, 1, 2, 3);
}
DI frag ld128(unsigned a) { return *(LDSP frag*)(size_t)a; }
DI u32x2 ld64(unsigned a) { return *(LDSP u32x2*)(size_t)a; }
DI void st64(unsigned a, u32x2 v) { *(LDSP u32x2*)(size_t)a = v; }
DI unsigned cvtpk(float a, float b) {
  f32x2 f = {a, b};
  return __builtin_bit_cast(unsigned, __builtin_convertvector(f, bf16x2));
}
DI u32x2 pack4(f32x4 a) { return u32x2{cvtpk(a[0], a[1]), cvtpk(a[2], a[3])}; }
DI frag cat(u32x2 a, u32x2 b) { return __builtin_shufflevector(a, b, 0, 1, 2, 3); }
DI void swap32(float& a, float& b) {
  auto r = __builtin_amdgcn_permlane32_swap(__float_as_uint(a), __float_as_uint(b), false, false);
  a = __uint_as_float(r[0]); b = __uint_as_float(r[1]);
}
DI void swap16(float& a, float& b) {
  auto r = __builtin_amdgcn_permlane16_swap(__float_as_uint(a), __float_as_uint(b), false, false);
  a = __uint_as_float(r[0]); b = __uint_as_float(r[1]);
}
DI float fmx(float a, float b) { return __builtin_elementwise_maximum(a, b); }
DI float fmx3(float a, float b, float c) { return fmx(fmx(a, b), c); }
#define STAMP(i)
#define RSTAMP(i)
#define STAMP_OUT()
#define STAMP_ARG
#define STAMP_PASS
#define STAMP_DECL
#define SGB(m, n) __builtin_amdgcn_sched_group_barrier(m, n, 0)
#define SCHED0() __builtin_amdgcn_sched_barrier(0)
#define WAIT_LGKM0() do { __builtin_amdgcn_s_waitcnt(0xC07F); asm volatile("" ::: "memory"); } while (0)
#define WAIT_VM0() asm volatile("s_waitcnt vmcnt(0)" ::: "memory")
#define BARRIER() __builtin_amdgcn_s_barrier()
#define PIN(x) asm volatile("" : "+v"(x))
#define TAG_SCALAR() do { unsigned lo_ = __builtin_amdgcn_readfirstlane((unsigned)tag), hi_ = __builtin_amdgcn_readfirstlane((unsigned)(tag >> 32)); \
    tag = (((u64)hi_ << 32) | lo_) + 1; asm volatile("" : "+s"(tag)); } while (0)
#define PINS(x) asm volatile("" : "+s"(x))
#define WR2(addr, a, b, o0, o1) asm volatile("ds_write2st64_b64 %0, %1, %2 offset0:%3 offset1:%4" ::"v"(addr), "v"(a), "v"(b), "i"(o0), "i"(o1) : "memory")

DI void pv_store(u32x4 pk, const frag (&V)[5], u32x4 sel, unsigned vmain, unsigned vleft, __amdgpu_buffer_rsrc_t ors, unsigned so, unsigned sl, bool g0) {
  const f32x4 z = {0.f, 0.f, 0.f, 0.f};
  frag P[4];
  f32x4 o[4], os[4];
  auto mkP = [&](int h) {
    unsigned u = pk[h];
    P[h] = frag{__builtin_amdgcn_perm(u, u, sel[0]), __builtin_amdgcn_perm(u, u, sel[1]), __builtin_amdgcn_perm(u, u, sel[2]), __builtin_amdgcn_perm(u, u, sel[3])};
    PIN(P[h]);
  };
  mkP(0); mkP(1);
  SCHED0();
  u32x4 f0, f1;
  os[0] = MFMA(V[4], P[0], z); PIN(os[0]); o[0] = MFMA(V[0], P[0], z); PIN(o[0]); mkP(2); SCHED0();
  os[1] = MFMA(V[4], P[1], z); PIN(os[1]); o[1] = MFMA(V[1], P[1], z); PIN(o[1]); mkP(3); SCHED0();
  os[2] = MFMA(V[4], P[2], z); PIN(os[2]); os[3] = MFMA(V[4], P[3], z); PIN(os[3]);
  o[2] = MFMA(V[2], P[2], z); PIN(o[2]); o[3] = MFMA(V[3], P[3], z); PIN(o[3]); SCHED0();
  f0 = u32x4{cvtpk(o[0][0], o[0][1]), cvtpk(o[0][2], o[0][3]), cvtpk(o[1][0], o[1][1]), cvtpk(o[1][2], o[1][3])};
  PIN(f0); SCHED0();
  __builtin_amdgcn_raw_buffer_store_b128(f0, ors, vmain, so, 16);
  SCHED0();
  u32x2 f2 = {cvtpk(g0 ? os[0][0] : os[2][0], g0 ? os[0][1] : os[2][1]), cvtpk(g0 ? os[1][2] : os[3][2], g0 ? os[1][3] : os[3][3])};
  PIN(f2); SCHED0();
  f1 = u32x4{cvtpk(o[2][0], o[2][1]), cvtpk(o[2][2], o[2][3]), cvtpk(o[3][0], o[3][1]), cvtpk(o[3][2], o[3][3])};
  PIN(f1); PIN(f0); SCHED0();
  __builtin_amdgcn_raw_buffer_store_b128(f1, ors, vmain + 1024, so, 16);
  __builtin_amdgcn_raw_buffer_store_b64(f2, ors, vleft, sl, 16);
  SCHED0();
}

struct P2 {
  unsigned so[9], vo[3], bw[4], rb[4], ra[2][2][2];
  GAS u64* fp;
  GAS char *op, *op1;
};
template <int MT>
DI void phase2(const P2& A, __amdgpu_buffer_rsrc_t ors, u64 tag, int lane STAMP_ARG) {
  const unsigned (&so)[9] = A.so; const unsigned (&vo)[3] = A.vo; const unsigned (&bw)[4] = A.bw; const unsigned (&rb)[4] = A.rb;
  GAS u64* fp = A.fp;
  frag wf[9][4];
  auto rdw = [&](int i) {
#pragma unroll
    for (int n = 0; n < 4; ++n) wf[i][n] = ldW(bw[n] + i * 4096, bw[n] + i * 4096 + 2048);
  };
  rdw(0); rdw(1); rdw(2); rdw(3); rdw(4);
  SCHED0();
  WAIT_LGKM0();
  BARRIER();
  STAMP(27);
#pragma clang loop unroll(disable)
  for (unsigned spins = 0; spins < (1u << 17); ++spins) {
    u64 v = __hip_atomic_load(fp, RLX_AGENT);
    if (__all(v == tag)) break;
    __builtin_amdgcn_s_sleep(PSLEEP);
  }
  __builtin_amdgcn_fence(__ATOMIC_ACQUIRE, "wavefront");
  asm volatile("" ::: "memory");
  STAMP(28);
  RSTAMP(62);
  frag of[9][MT];
  constexpr int WIN = 2;
#pragma unroll
  for (int i = 0; i < WIN; ++i)
#pragma unroll
    for (int m = 0; m < MT; ++m) { of[i][m] = __builtin_amdgcn_raw_buffer_load_b128(ors, vo[m], so[i], 16); SCHED0(); }
  f32x4 acc[MT][4];
#pragma unroll
  for (int i = 0; i < 8; ++i) {
    const bool rd = i >= 2 && i + 3 < 9;
    if (rd) rdw(i + 3);
#pragma unroll
    for (int m = 0; m < MT; ++m) {
#pragma unroll
      for (int n = 0; n < 4; ++n) acc[m][n] = MFMA(wf[i][n], of[i][m], i == 0 ? (f32x4{0, 0, 0, 0}) : acc[m][n]);
      if (i + WIN < 9) of[i + WIN][m] = __builtin_amdgcn_raw_buffer_load_b128(ors, vo[m], so[i + WIN], 16);
    }
#pragma unroll
    for (int m = 0; m < MT; ++m) {
#pragma unroll
      for (int n = 0; n < 4; ++n) { SGB(0x8, 1); if (m < 2 && rd) SGB(0x100, 1); }
      if (i + WIN < 9) SGB(0x20, 1);
    }
    SCHED0();
  }
#pragma unroll
  for (int m = 0; m <= MT; ++m) {
    if (m < MT) {
#pragma unroll
      for (int n = 0; n < 4; ++n) { acc[m][n] = MFMA(wf[8][n], of[8][m], acc[m][n]); PIN(acc[m][n]); }
    }
    SCHED0();
    if (m > 0) {
#pragma unroll
      for (int n = 0; n < 4; ++n) *(LDSP f32x4*)(size_t)(rb[n] + (m - 1) * 4096) = acc[m - 1][n];
    }
    SCHED0();
  }
  STAMP(29);
}

typedef u64 u64x8 __attribute__((ext_vector_type(8)));
extern "C" __global__ __launch_bounds__(512, 2) void fused_kernel(const u16* x_, const u16* wA_, const u16* wB_, const u16* woA_, const u16* woB_,
                                                                   const u16* gw_, char* ws_, u16* out_) {
  __shared__ __attribute__((aligned(1024))) char lds[LDS_BYTES];
  const unsigned L = (unsigned)(size_t)(LDSP char*)lds;
  const int BIDX = blockIdx.x, TIDX = threadIdx.x;
  u64x8 ka;
  asm volatile("s_load_dwordx16 %0, %1, 0x0" : "=s"(ka) : "s"(__builtin_amdgcn_kernarg_segment_ptr()));
#define KARG_WAIT() do { SCHED0(); asm volatile("s_waitcnt lgkmcnt(0)" ::: "memory"); SCHED0(); asm volatile("" : "+s"(ka)); } while (0)
#define x ((const GAS u16*)ka[0])
#define wA ((const GAS u16*)ka[1])
#define wB ((const GAS u16*)ka[2])
#define woA ((const GAS u16*)ka[3])
#define woB ((const GAS u16*)ka[4])
#define gw ((const GAS u16*)ka[5])
#define ws ((GAS char*)ka[6])
#define out ((GAS u16*)ka[7])
#define ors __builtin_amdgcn_make_buffer_rsrc((void*)ka[6], 0, O_BYTES, 0x00020000)
  const int bid = BIDX;
  const int xcd = bid & 7, idx = bid >> 3;
  const int tl = idx & 7, gl = idx >> 3;
  const int tile = (xcd & 1) * 8 + tl;
  const int grp = (xcd >> 1) * 4 + gl;
  int rot = ROT == 1 ? tl + 2 * gl : ROT == 2 ? (tl >> 1) * 2 + 2 * gl : ROT == 6 ? (tl % 3) * 3 + (gl % 3) * 3 : ROT == 7 ? (((tl >> 1) + gl) & 3) * 2 + (tl & 1 & (gl >> 1))
          : ROT == 8 ? (((tl + gl) & 3) * 9) >> 2 : ROT == 9 ? ((tl + gl + (xcd >> 1)) % 3) * 3 : ROT == 10 ? (((tl + gl) & 3) * 9 >> 2) + (xcd & 1)
          : ROT == 11 ? ((tl + gl) & 3) * 2 + ((xcd ^ (xcd >> 1)) & 1) : ROT == 12 ? ((tl + gl) % 3) * 3 + ((xcd & 1) + (xcd >> 1)) % 3
          : ROT == 13 ? ((tl + gl) & 1) * 4 + (((xcd & 1) * 2 + (xcd >> 1)) & 3) : ROT == 14 ? (tl + 2 * gl + ((xcd >> 1) & 1)) % 9
          : ROT == 15 ? (((tl + gl) & 3) * 2 + ((xcd ^ (xcd >> 1)) & 1) + (xcd >> 2)) : 0;
  if (rot >= 9) rot -= 9;
  const int tid = TIDX, lane = tid & 63;
  const int wave = __builtin_amdgcn_readfirstlane(tid >> 6);
  const int vl = lane >> 3;
  STAMP_DECL;
  STAMP(0);
  RSTAMP(60);
  if (tid < 7) {
    u64 pc0;
    unsigned len;
    asm volatile("s_getpc_b64 %0\n\ts_mov_b32 %1, .Lfk_tail-fused_kernel+128" : "=s"(pc0), "=s"(len));
    unsigned off = (idx * 7 + tid) * 128;
    off = off < len ? off : 0;
    asm volatile("s_mov_b32 m0, %2\n\ts_nop 0\n\tglobal_load_lds_dword %0, %1" ::"v"(off), "s"(pc0 & ~255ull), "s"(L + SCR_OFF) : "memory");
  }
  u64 tag;
  const int lin0 = xcd * 30 + idx;
  const bool act2 = idx < 30 && lin0 < 234;
  const int lin = act2 ? lin0 : 0;
  const int rt = (lin * 57) >> 9, ct = lin - rt * 9;
  const int rot2 = 3 * (rt - ((rt * 171) >> 9) * 3);
  const unsigned wsch = ((((lane & 7) >> 1) ^ ((vl >> 1) & 3)) << 5) | ((lane & 1) << 4);
  const unsigned offn = vl * XROW + wsch, offl = ((vl >> 2) * 72 + (vl & 3)) * XROW + wsch;
  auto wo_piece = [&](int kq, int i, int j) {
    const GAS char* b = (const GAS char*)(kq >= 2 ? woB : woA) + ct * 128 + (kq & 1) * (288 * XROW);
    if (i == 8) b += ((j & 1) * 144 + ((j >> 1) ? 52 : 16)) * XROW;
    else b += (((i + rot2) & 7) * 36 + (j >> 1) * 20 + 8 * (j & 1)) * XROW;
    glds16(i == 8 ? offl : offn, b, L + (kq * 9 + i) * 4096 + j * 1024);
  };
  const int g = lane >> 4, q = (lane >> 2) & 3, p = lane & 3, c = lane & 15;
  const int cw = wave & 3;

  auto retire = [&]() {
    if (bid == 255) {
      if (wave < 4) {
        const int b = wave * 64 + lane;
        GAS u64* fp = (GAS u64*)(ws + FLAG_OFF + (((b & 15) >> 2) * 64 + (b >> 4) * 4) * FS1 + (b & 3) * FS2);
#pragma clang loop unroll(disable)
        for (unsigned spins = 0; spins < (1u << 17); ++spins) {
          u64 v = __hip_atomic_load(fp, RLX_AGENT);
          if (__all(v == tag)) break;
          __builtin_amdgcn_s_sleep(8);
        }
      }
      BARRIER();
      if (tid == 0) __hip_atomic_store((GAS u64*)(ws + EPOCH_OFF), tag, RLX_AGENT);
    }
  };
  P2 A;
  const GAS char* l8;
  auto p2setup = [&](int MT, int tt0, int mrow) {
    int l2 = lane;
    PIN(l2);
    const int g = l2 >> 4, q = (l2 >> 2) & 3, p = l2 & 3, c = l2 & 15, v2 = l2 >> 3;
    const unsigned sw = (2 * g + (q >> 1)) & 3;
#pragma unroll
    for (int n = 0; n < 4; ++n) { A.bw[n] = L + cw * 36864 + (4 * g + q) * 128 + 8 * p + ((n ^ sw) * 32); PIN(A.bw[n]); }
#pragma unroll
    for (int i = 0; i < 9; ++i) { A.so[i] = (i < 8 ? 8 * cw + ((i + rot2) & 7) : 32 + cw) * 1024; PINS(A.so[i]); }
#pragma unroll
    for (int m = 0; m < 3; ++m) { int tt = tt0 + m; tt = tt > 127 ? 127 : tt; A.vo[m] = l2 * 16 + tt * 36864; PIN(A.vo[m]); }
    int tf = (l2 & 4) ? tt0 + MT - 1 : tt0; tf = tf > 127 ? 127 : tf;
    A.fp = (GAS u64*)(ws + FLAG_OFF + (cw * 64 + (tf >> 1)) * FS1 + (l2 & 3) * FS2);
    PIN(A.fp);
#pragma unroll
    for (int n = 0; n < 4; ++n) { A.rb[n] = L + cw * 36864 + (mrow + c) * 256 + (((4 * n + g) ^ c) * 16); PIN(A.rb[n]); }
#pragma unroll
    for (int it = 0; it < 2; ++it)
#pragma unroll
      for (int hf = 0; hf < 2; ++hf)
#pragma unroll
        for (int wp = 0; wp < 2; ++wp) {
          A.ra[it][hf][wp] = L + wp * 73728 + (wave * 16 + it * 8 + v2) * 256 + ((((l2 & 7) * 2 + hf) ^ (it * 8 + v2)) * 16);
          PIN(A.ra[it][hf][wp]);
        }
    A.op = (GAS char*)out + (size_t)(rt * 80 + wave * 16 + v2) * XROW + ct * 128 + (l2 & 7) * 16;
    A.op1 = A.op + 8 * XROW;
    PIN(A.op); PIN(A.op1);
    l8 = (const GAS char*)woB + ct * 128 + (288 + 16) * XROW;
    PINS(l8);
  };

  if (__builtin_expect(wave >= 4, 1)) {
    __builtin_amdgcn_s_setprio(2);
    const int lw = cw, tl256 = tid - 256;
    const unsigned offx = (tile * 128 + 8 * lw + vl) * XROW + ((lane & 7) ^ ((4 * lw + (vl >> 1)) & 7)) * 16;
    KARG_WAIT();
    const GAS char* w = (const GAS char*)((grp >= 8) ? wB : wA) + (grp & 7) * 144;
    unsigned offw[7], offh;
    auto kchunk = [&](int st) { int kc = st + rot; return kc >= 9 ? kc - 9 : kc; };
    auto issueX = [&](int kc, unsigned sb, int jj) { glds16(offx, (const GAS char*)x + kc * 128 + jj * (32 * XROW), sb + STG_W + (2 + lw + 4 * jj) * 1024); };
    auto issueW = [&](int kc, unsigned sb, int j) { glds16(offw[j], w + kc * (64 * WROW), sb + (lw + 4 * j) * 1024); };
    auto issue6 = [&](int kc, unsigned sb) {
      if (lw < 3) issueW(kc, sb, 6);
      else { glds16(offw[6], (const GAS char*)x + kc * 128, sb + STG_W); glds16(offh, (const GAS char*)x + kc * 128, sb + STG_W + 1024); }
    };
    const int kc0 = rot, kc1 = kchunk(1);
    const unsigned sb0 = L, sb1 = L + STG;
    auto calcw = [&](int j) {
      unsigned qq = 256 * j + tl256;
      unsigned u = (qq * 2428u) >> 16, cc = qq - u * 27;
      unsigned row = (u & 0x38) | ((u & 1) << 2) | ((u >> 1) & 3);
      offw[j] = row * WROW + cc * 16 + ((cc * 57u) >> 9) * 1008;
    };
    issueX(kc0, sb0, 0);
    u32x2 gv;
    const unsigned gi = tl256 < 144 ? tl256 : 143;
    asm volatile("global_load_dwordx2 %0, %1, %2" : "=v"(gv) : "v"(gi * 8), "s"(gw) : "memory");
#pragma unroll
    for (int j = 0; j < 6; ++j) {
      SCHED0();
      calcw(j);
      SCHED0();
      issueW(kc0, sb0, j);
      if (j < 3) issueX(kc0, sb0, j + 1);
    }
    calcw(6);
    {
      int t0 = tile * 128 - 16 + vl, t1 = t0 + 8;
      t0 = t0 < 0 ? 0 : t0; t1 = t1 < 0 ? 0 : t1;
      unsigned h0 = t0 * XROW + ((lane & 7) ^ (vl >> 1)) * 16;
      offh = t1 * XROW + ((lane & 7) ^ (4 + (vl >> 1))) * 16;
      offw[6] = lw == 3 ? h0 : offw[6];
    }
    issue6(kc0, sb0);
    STAMP(1);
    issueW(kc1, sb1, 0);
    int slot = 0, kn = kc1;
    const GAS char* xp = (const GAS char*)x + kn * 128;
    unsigned b1 = sb1, xd = sb1 + STG_W + (2 + lw) * 1024;
    PINS(xp); PINS(xd);
    SCHED0();
#pragma unroll
    for (int j = 1; j < PRE0; ++j) issueW(kc1, sb1, j);
    STAMP(2);
    asm volatile("s_waitcnt vmcnt(%1)" : "+v"(gv) : "i"(10 + PRE0) : "memory");
    {
      unsigned a0 = __builtin_amdgcn_perm(gv[0], gv[0], 0x01000100), a1 = __builtin_amdgcn_perm(gv[0], gv[0], 0x03020302);
      unsigned a2 = __builtin_amdgcn_perm(gv[1], gv[1], 0x01000100), a3 = __builtin_amdgcn_perm(gv[1], gv[1], 0x03020302);
      *(LDSP u32x4*)(size_t)(L + G_OFF + gi * 32) = u32x4{a0, a0, a1, a1};
      *(LDSP u32x4*)(size_t)(L + G_OFF + gi * 32 + 16) = u32x4{a2, a2, a3, a3};
    }
    WAIT_LGKM0();
    asm volatile("s_waitcnt vmcnt(%0)" ::"i"(PRE0) : "memory");
#pragma clang loop unroll(disable)
    for (int st = 0; st < 7; ++st) {
      BARRIER();
      glds16(offx, xp, xd);
      SCHED0();
      STAMP(3 + st);
      glds16(offx, xp + 32 * XROW, xd + 4096);
      if (st == 0) {
#pragma unroll
        for (int j = PRE0; j < 6; ++j) issueW(kc1, sb1, j);
        tag = __hip_atomic_load((GAS u64*)(ws + EPOCH_OFF), RLX_AGENT);
        asm volatile("s_mov_b32 m0, %2\n\ts_nop 0\n\tglobal_load_lds_dword %0, %1 sc1" ::"v"(0u), "s"(ws + FLAG_OFF + ((grp >> 2) * 64 + tile * 4 + lw) * FS1), "s"(L + SCR_OFF + 256) : "memory");
      }
      issue6(kn, b1);
      glds16(offx, xp + 64 * XROW, xd + 8192);
      glds16(offx, xp + 96 * XROW, xd + 12288);
      SCHED0();
      kn = kn == 8 ? 0 : kn + 1;
      b1 = L + (slot == 0 ? 2 : slot - 1) * STG;
      issueW(kn, b1, 0); issueW(kn, b1, 1);
#pragma unroll
      for (int j = 2; j < 6; ++j) issueW(kn, b1, j);
      slot = slot == 2 ? 0 : slot + 1;
      xp = (const GAS char*)x + kn * 128;
      xd = b1 + STG_W + (2 + lw) * 1024;
      PINS(xp); PINS(xd);
      asm volatile("s_waitcnt vmcnt(6)" ::: "memory");
    }
    BARRIER();
    glds16(offx, xp, xd);
    SCHED0();
    STAMP(10);
    glds16(offx, xp + 32 * XROW, xd + 4096);
    issue6(kn, b1);
    glds16(offx, xp + 64 * XROW, xd + 8192);
    glds16(offx, xp + 96 * XROW, xd + 12288);
#pragma unroll
    for (int I = 0; I < 6; ++I) wo_piece(0, I, lw);
    asm volatile("s_waitcnt vmcnt(6)" ::: "memory");
    BARRIER();
    STAMP(11);
    wo_piece(0, 6, lw); wo_piece(0, 7, lw);
    __builtin_amdgcn_s_setprio(0);
#pragma unroll
    for (int I = 8; I < NBX; ++I) wo_piece(I / 9, I % 9, lw);
    WAIT_LGKM0();
    STAMP(12);
    BARRIER();
    STAMP(13);
#pragma unroll
    for (int I = NBX; I < 35; ++I) wo_piece(I / 9, I % 9, lw);
    p2setup(3, rt * 5, 0);
    WAIT_VM0();
    TAG_SCALAR();
    STAMP(14);
    BARRIER();
    if (!act2) { retire(); return; }
    if (cw == 3) {
#pragma unroll
      for (int j = 0; j < 4; ++j) glds16(offl, l8 + ((j & 1) * 144 + (j >> 1) * 36) * XROW, L + 35 * 4096 + j * 1024);
    }
    phase2<3>(A, ors, tag, lane STAMP_PASS);
  } else {
    const unsigned wrow = L + (8 * g + 2 * q) * PITCH;
    f32x4 maskP, maskC, maskP0;
    {
      const float ninf = -__builtin_inff();
#pragma unroll
      for (int r = 0; r < 4; ++r) {
        bool pm = (r & 1) == (c & 1);
        maskC[r] = (pm && 4 * g + r <= c) ? 0.f : ninf;
        maskP[r] = (pm && 4 * g + r > c) ? 0.f : ninf;
      }
      maskP0 = maskP + ((tile == 0 && wave == 0) ? -998244352.f * 4.242640687119285f : 0.f);
      asm volatile("" : "+v"(maskP), "+v"(maskC), "+v"(maskP0));
    }
    u32x4 sel;
    {
      const int par = c & 1;
      const bool cur1 = 4 * g + par <= c, cur2 = 4 * g + 2 + par <= c;
      const unsigned s1 = par ? 0x01000c0cu : 0x0c0c0100u;
      const unsigned s2 = par ? 0x03020c0cu : 0x0c0c0302u;
      sel[0] = cur1 ? 0x0c0c0c0cu : s1;
      sel[1] = cur2 ? 0x0c0c0c0cu : s2;
      sel[2] = cur1 ? s1 : 0x0c0c0c0cu;
      sel[3] = cur2 ? s2 : 0x0c0c0c0cu;
    }
    unsigned vmain = lane * 16, vleft = lane < 32 ? c * 16 + g * 8 : 0x40000000u;
    unsigned osoff = (tile * 8 + 2 * wave) * 36864 + grp * 2048, oloff = (tile * 8 + 2 * wave) * 36864 + 32768 + grp * 256;
    unsigned osoff1 = osoff + 36864, oloff1 = oloff + 36864;
    PINS(osoff); PINS(oloff); PINS(osoff1); PINS(oloff1);
    asm volatile("" : "+v"(sel), "+v"(vmain), "+v"(vleft));
    GAS u64* myflag;
    unsigned sgb = (grp >= 8) ? 0x3fb8aa3bu : 0xbfb8aa3bu;
    PINS(sgb);
    const float sg = __uint_as_float(sgb);
    f32x4 aK[2][4], aQ[2][4], aKQ[2], aV[2][5], hk, hv, he;
    frag cv[5], xb[3];
    unsigned exr = L + EX_OFF + wave * 5120 + lane * 8, exh = L + EX_OFF + wave * 512 + lane * 8;
    unsigned exgw = L + SCR_OFF + wave * 128 + g * 16, exgr = L + SCR_OFF + wave * 128 + c * 4;
    PIN(exr); PIN(exh); PIN(exgw); PIN(exgr);
    auto mainloop = [&](auto WC) {
      constexpr int W = decltype(WC)::value;
      constexpr int XO = (1 + 2 * W) * 2048;
    hk = f32x4{0, 0, 0, 0}; hv = f32x4{0, 0, 0, 0}; he = f32x4{0, 0, 0, 0};
    PIN(he);
#pragma unroll
    for (int t = 0; t < 2; ++t) {
#pragma unroll
      for (int f = 0; f < 4; ++f) { aK[t][f] = f32x4{0, 0, 0, 0}; aQ[t][f] = f32x4{0, 0, 0, 0}; }
      aKQ[t] = f32x4{0, 0, 0, 0};
#pragma unroll
      for (int f = 0; f < 5; ++f) aV[t][f] = f32x4{0, 0, 0, 0};
    }
    unsigned cW = wrow + 8 * p;
    unsigned cKQ = wrow + (p < 2 ? 144 : 0) + ((p & 1) ? 104 : 32);
    unsigned cX0 = L + STG_W + c * 128 + ((g ^ (c >> 1)) * 16), cX1 = cX0 ^ 64;
    unsigned cV[4];
#pragma unroll
    for (int i = 0; i < 4; ++i)
      cV[i] = p < 2 ? wrow + 288 + (p ? 104 : 32) + (i >> 1) * KO + (i & 1) * PITCH : L + G_OFF + rot * 512 + (8 * g + q) * 8 + (i >> 1) * 256 + (i & 1) * 32;
#pragma unroll
    for (int f = 0; f < 5; ++f) cv[f] = frag{0, 0, 0, 0};
    xb[0] = frag{0, 0, 0, 0}; xb[1] = frag{0, 0, 0, 0}; xb[2] = frag{0, 0, 0, 0};
    asm volatile("" : "+v"(aK[0][0]), "+v"(aK[0][1]), "+v"(aK[0][2]), "+v"(aK[0][3]), "+v"(aK[1][0]), "+v"(aK[1][1]), "+v"(aK[1][2]), "+v"(aK[1][3]),
                      "+v"(aQ[0][0]), "+v"(aQ[0][1]), "+v"(aQ[0][2]), "+v"(aQ[0][3]), "+v"(aQ[1][0]), "+v"(aQ[1][1]), "+v"(aQ[1][2]), "+v"(aQ[1][3]),
                      "+v"(aKQ[0]), "+v"(aKQ[1]), "+v"(hk), "+v"(hv), "+v"(xb[0]), "+v"(xb[1]), "+v"(xb[2]));
    asm volatile("" : "+v"(aV[0][0]), "+v"(aV[0][1]), "+v"(aV[0][2]), "+v"(aV[0][3]), "+v"(aV[0][4]), "+v"(aV[1][0]), "+v"(aV[1][1]), "+v"(aV[1][2]),
                      "+v"(aV[1][3]), "+v"(aV[1][4]), "+v"(cv[0]), "+v"(cv[1]), "+v"(cv[2]), "+v"(cv[3]), "+v"(cv[4]),
                      "+v"(cW), "+v"(cKQ), "+v"(cX0), "+v"(cX1), "+v"(cV[0]), "+v"(cV[1]), "+v"(cV[2]), "+v"(cV[3]));
    SCHED0();
    STAMP(1);
    KARG_WAIT();
    tag = __hip_atomic_load((GAS u64*)(ws + EPOCH_OFF), RLX_AGENT);
    myflag = (GAS u64*)(ws + FLAG_OFF + ((grp >> 2) * 64 + tile * 4 + cw) * FS1 + (grp & 3) * FS2);
    PINS(myflag);
    STAMP(2);
    WAIT_LGKM0();
      int slot = 0, kc = rot;
#pragma clang loop unroll(disable)
      for (int st = 0; st < 9; ++st) {
        BARRIER();
        SCHED0();
        frag xa[3], W0[14], W1[9];
        xa[0] = ld128(cX0 + XO); xa[1] = ld128(cX0 + XO + 2048);
#pragma unroll
        for (int f = 0; f < 4; ++f) W0[f ^ 1] = ldW(cW + 144 + HOFF(f ^ 1), cW + 144 + HOFF(f ^ 1) + PITCH);
        xa[2] = ld128(cX0);
#pragma unroll
        for (int f = 0; f < 5; ++f) { aV[0][f] = MFMA(xb[0], cv[f], aV[0][f]); aV[1][f] = MFMA(xb[1], cv[f], aV[1][f]); }
        hv = MFMA(xb[2], cv[W], hv);
        if (W == 1) he = MFMA(xb[2], cv[4], he);
        W0[5] = ldW(cW + HOFF(0), cW + HOFF(0) + PITCH);
        W0[4] = ldW(cKQ, cKQ + PITCH);
        W0[7] = ldW(cW + HOFF(2), cW + HOFF(2) + PITCH);
        W0[6] = ldW(cW + HOFF(1), cW + HOFF(1) + PITCH);
        W0[9] = ldW(cW + 288 + HOFF(0), cW + 288 + HOFF(0) + PITCH);
        W0[8] = ldW(cW + HOFF(3), cW + HOFF(3) + PITCH);
        W0[11] = ldW(cW + 288 + HOFF(2), cW + 288 + HOFF(2) + PITCH);
        W0[10] = ldW(cW + 288 + HOFF(1), cW + 288 + HOFF(1) + PITCH);
        W0[13] = ldW(cV[0], cV[1]);
        W0[12] = ldW(cW + 288 + HOFF(3), cW + 288 + HOFF(3) + PITCH);
        xb[0] = ld128(cX1 + XO); xb[1] = ld128(cX1 + XO + 2048); xb[2] = ld128(cX1);
#pragma unroll
        for (int f = 0; f < 4; ++f) W1[f ^ 1] = ldW(cW + KO + 144 + HOFF(f ^ 1), cW + KO + 144 + HOFF(f ^ 1) + PITCH);
        W1[5] = ldW(cW + KO + HOFF(0), cW + KO + HOFF(0) + PITCH);
        W1[4] = ldW(cKQ + KO, cKQ + KO + PITCH);
        W1[8] = ldW(cW + KO + HOFF(3), cW + KO + HOFF(3) + PITCH);
        W1[7] = ldW(cW + KO + HOFF(2), cW + KO + HOFF(2) + PITCH);
        W1[6] = ldW(cW + KO + HOFF(1), cW + KO + HOFF(1) + PITCH);
#pragma unroll
        for (int f = 0; f < 4; ++f) cv[f] = ldW(cW + KO + 288 + HOFF(f), cW + KO + 288 + HOFF(f) + PITCH);
        cv[4] = ldW(cV[2], cV[3]);
#pragma unroll
        for (int f = 0; f < 4; ++f) { aK[0][f] = MFMA(W0[f], xa[0], aK[0][f]); aK[1][f] = MFMA(W0[f], xa[1], aK[1][f]); }
        hk = MFMA(W0[W], xa[2], hk);
        aKQ[0] = MFMA(W0[4], xa[0], aKQ[0]); aKQ[1] = MFMA(W0[4], xa[1], aKQ[1]);
        if (W == 0) he = MFMA(W0[4], xa[2], he);
#pragma unroll
        for (int f = 0; f < 4; ++f) { aQ[0][f] = MFMA(W0[5 + f], xa[0], aQ[0][f]); aQ[1][f] = MFMA(W0[5 + f], xa[1], aQ[1][f]); }
#pragma unroll
        for (int f = 0; f < 5; ++f) { aV[0][f] = MFMA(xa[0], W0[9 + f], aV[0][f]); aV[1][f] = MFMA(xa[1], W0[9 + f], aV[1][f]); }
        hv = MFMA(xa[2], W0[9 + W], hv);
        if (W == 1) he = MFMA(xa[2], W0[13], he);
#pragma unroll
        for (int f = 0; f < 4; ++f) { aK[0][f] = MFMA(W1[f], xb[0], aK[0][f]); aK[1][f] = MFMA(W1[f], xb[1], aK[1][f]); }
        hk = MFMA(W1[W], xb[2], hk);
        aKQ[0] = MFMA(W1[4], xb[0], aKQ[0]); aKQ[1] = MFMA(W1[4], xb[1], aKQ[1]);
        if (W == 0) he = MFMA(W1[4], xb[2], he);
#pragma unroll
        for (int f = 0; f < 4; ++f) { aQ[0][f] = MFMA(W1[5 + f], xb[0], aQ[0][f]); aQ[1][f] = MFMA(W1[5 + f], xb[1], aQ[1][f]); }
        const int ds = slot == 2 ? -2 * STG : STG, dg = kc == 8 ? -8 * 512 : 512;
        slot = slot == 2 ? 0 : slot + 1;
        kc = kc == 8 ? 0 : kc + 1;
        const int dv = p < 2 ? ds : dg;
        cW += ds; cKQ += ds; cX0 += ds; cX1 += ds;
#pragma unroll
        for (int i = 0; i < 4; ++i) cV[i] += dv;
        SGB(0x100, 2);
#pragma unroll
        for (int i = 0; i < 54; ++i) {
          SGB(0x8, 1);
          if (i < 3 || i == 16 || i == 28 || i == 40) SGB(0x100, 2); else SGB(0x100, 1);
          if (i >= 3 && i < 11) SGB(0x4, 1);
          if (i >= 51) SGB(0x2, 1);
        }
#pragma unroll
        for (int i = 0; i < 4; ++i) { SGB(0x8, 1); SGB(0x2, 2); }
        SGB(0x8, W < 2 ? 4 : 2);
        SCHED0();
        WAIT_LGKM0();
      }
      hv = MFMA(xb[2], cv[W], hv);
      if (W == 1) he = MFMA(xb[2], cv[4], he);
      if (W < 2) {
        u32x2 v = pack4(he);
        if (W == 0 && tile == 0) v = u32x2{0u, 0u};
        st64(L + EX_OFF + lane * 8 + (W == 0 ? 4 : 9) * 512, v);
      }
    };
    if (wave == 0) mainloop(std::integral_constant<int, 0>{});
    else if (wave == 1) mainloop(std::integral_constant<int, 1>{});
    else if (wave == 2) mainloop(std::integral_constant<int, 2>{});
    else mainloop(std::integral_constant<int, 3>{});
    WAIT_VM0();
    STAMP(12);
    TAG_SCALAR();
    const bool g0 = g == 0;
    frag KN[4], Q0[4], V[5];
    u32x2 kb[5], vb[5], ksa, qs[2];
    aV[1][4] = MFMA(xb[1], cv[4], aV[1][4]); PIN(aV[1][4]);
    aV[0][4] = MFMA(xb[0], cv[4], aV[0][4]); PIN(aV[0][4]);
    {
      u32x2 k1 = pack4(aKQ[1]), k0 = pack4(aKQ[0]);
      auto r0 = __builtin_amdgcn_permlane32_swap(k1[0], 0u, false, false);
      auto r1 = __builtin_amdgcn_permlane32_swap(k1[1], 0u, false, false);
      auto r2 = __builtin_amdgcn_permlane32_swap(k0[0], 0u, false, false);
      auto r3 = __builtin_amdgcn_permlane32_swap(k0[1], 0u, false, false);
      qs[1] = u32x2{r0[1], r1[1]}; qs[0] = u32x2{r2[1], r3[1]};
      ksa = u32x2{r2[0], r3[0]}; kb[4] = u32x2{r0[0], r1[0]};
    }
#pragma unroll
    for (int f = 0; f < 4; ++f) { aV[1][f] = MFMA(xb[1], cv[f], aV[1][f]); PIN(aV[1][f]); }
#pragma unroll
    for (int f = 0; f < 4; ++f) kb[f] = pack4(aK[1][f]);
    {
      u32x2 h = pack4(hk), hh = pack4(hv);
      if (tile == 0) h = u32x2{0u, 0u};
      WR2(exh, h, hh, 0, 5);
    }
    if (c >= 8) { *(LDSP f32x4*)(size_t)(exgw) = aV[0][4]; *(LDSP f32x4*)(size_t)(exgw + 64) = aV[1][4]; }
#pragma unroll
    for (int f = 0; f < 4; ++f) { aV[0][f] = MFMA(xb[0], cv[f], aV[0][f]); PIN(aV[0][f]); }
#pragma unroll
    for (int f = 0; f < 5; ++f) vb[f] = pack4(aV[1][f]);
    if (wave < 3) {
#pragma unroll
      for (int f = 0; f < 5; ++f) WR2(exr, kb[f], vb[f], 10 + f, 15 + f);
    }
    u32x2 gz;
    asm volatile("ds_read2_b32 %0, %1 offset0:0 offset1:16" : "=v"(gz) : "v"(exgr) : "memory");
    f32x4 sc[2][4], sp[2][4];
#pragma unroll
    for (int h = 0; h < 4; ++h) {
      u32x2 q1 = pack4(aQ[1][h]), q0 = pack4(aQ[0][h]);
      unsigned e1 = ((h < 2) == g0) ? qs[1][h & 1] : 0u, e0 = ((h < 2) == g0) ? qs[0][h & 1] : 0u;
      frag Q1 = (h & 1) ? frag{q1[0], q1[1], 0u, e1} : frag{q1[0], q1[1], e1, 0u};
      Q0[h] = (h & 1) ? frag{q0[0], q0[1], 0u, e0} : frag{q0[0], q0[1], e0, 0u};
      frag KA = cat(pack4(aK[0][h]), ksa);
      sc[1][h] = MFMA(cat(kb[h], kb[4]), Q1, maskC); PIN(sc[1][h]);
      sp[1][h] = MFMA(KA, Q1, maskP); PIN(sp[1][h]);
      sc[0][h] = MFMA(KA, Q0[h], maskC); PIN(sc[0][h]);
      if (h == BXH) {
        SCHED0();
        asm volatile("s_waitcnt lgkmcnt(0)" : "+v"(gz)::"memory");
        STAMP(23);
        BARRIER();
        STAMP(24);
        SCHED0();
#pragma unroll
        for (int k = 0; k < 4; ++k) asm volatile("ds_read2st64_b64 %0, %1 offset0:%2 offset1:4" : "=v"(KN[k]) : "v"(exr), "i"(k) : "memory");
        SCHED0();
      }
    }
    float v[2][4][2];
#pragma unroll
    for (int h = 0; h < 4; ++h) {
      v[1][h][0] = fmx(fmx3(sp[1][h][0], sp[1][h][1], sc[1][h][0]), sc[1][h][1]);
      v[1][h][1] = fmx(fmx3(sp[1][h][2], sp[1][h][3], sc[1][h][2]), sc[1][h][3]);
    }
    asm volatile("s_waitcnt lgkmcnt(0)" : "+v"(KN[0]), "+v"(KN[1]), "+v"(KN[2]), "+v"(KN[3]), "+v"(v[1][0][0]), "+v"(v[1][0][1]), "+v"(v[1][1][0]), "+v"(v[1][1][1]),
                 "+v"(v[1][2][0]), "+v"(v[1][2][1]), "+v"(v[1][3][0]), "+v"(v[1][3][1])::"memory");
#pragma unroll
    for (int h = 0; h < 4; ++h) { sp[0][h] = MFMA(KN[h], Q0[h], maskP0); PIN(sp[0][h]); }
#pragma unroll
    for (int f = 0; f < 5; ++f) V[f] = cat(vb[f], pack4(aV[0][f]));
    u32x4 pk[2];
    auto softmax = [&](float (&w)[4][2], float gate_logit) {
      swap32(w[0][0], w[2][0]); swap32(w[1][0], w[3][0]); swap32(w[0][1], w[2][1]); swap32(w[1][1], w[3][1]);
      swap16(w[0][0], w[1][0]); swap16(w[2][0], w[3][0]); swap16(w[0][1], w[1][1]); swap16(w[2][1], w[3][1]);
      const float cs = 0.23570226039551584f * 1.4426950408889634f;
      float m = fmx(fmx3(fmx3(w[0][0], w[1][0], w[2][0]), w[3][0], w[0][1]), fmx3(w[1][1], w[2][1], w[3][1]));
      f32x2 e[4];
#pragma unroll
      for (int i = 0; i < 4; ++i) {
        f32x2 a = (f32x2{w[i][0], w[i][1]} - m) * cs;
        e[i] = f32x2{__builtin_amdgcn_exp2f(a[0]), __builtin_amdgcn_exp2f(a[1])};
      }
      f32x2 s2 = (e[0] + e[1]) + (e[2] + e[3]);
      float s = s2[0] + s2[1];
      float r = __builtin_amdgcn_rcpf(__builtin_fmaf(s, __builtin_amdgcn_exp2f(gate_logit * sg), s));
      float pf[4];
#pragma unroll
      for (int i = 0; i < 4; ++i) { f32x2 pp = e[i] * r; pf[i] = __uint_as_float(cvtpk(pp[0], pp[1])); }
      swap32(pf[0], pf[2]); swap32(pf[1], pf[3]);
      swap16(pf[0], pf[1]); swap16(pf[2], pf[3]);
      return u32x4{__float_as_uint(pf[0]), __float_as_uint(pf[1]), __float_as_uint(pf[2]), __float_as_uint(pf[3])};
    };
    SCHED0();
#pragma unroll
    for (int h = 0; h < 4; ++h) {
      v[0][h][0] = fmx(fmx3(sp[0][h][0], sp[0][h][1], sc[0][h][0]), sc[0][h][1]);
      v[0][h][1] = fmx(fmx3(sp[0][h][2], sp[0][h][3], sc[0][h][2]), sc[0][h][3]);
    }
    pk[1] = softmax(v[1], __uint_as_float(gz[1]));
#pragma unroll
    for (int h = 0; h < 4; ++h) { PIN(v[0][h][0]); PIN(v[0][h][1]); }
    SCHED0();
    pv_store(pk[1], V, u32x4{sel[2], sel[3], sel[0], sel[1]}, vmain, vleft, ors, osoff1, oloff1, g0);
#pragma unroll
    for (int f = 0; f < 5; ++f) { u32x2 n = *(volatile LDSP u32x2*)(size_t)(exr + (5 + f) * 512); V[f][0] = n[0]; V[f][1] = n[1]; }
    SCHED0();
    pk[0] = softmax(v[0], __uint_as_float(gz[0]));
    SCHED0();
    pv_store(pk[0], V, sel, vmain, vleft, ors, osoff, oloff, g0);
    STAMP(25);
    p2setup(2, rt * 5 + 3, 48);
    SCHED0();
    WAIT_VM0();
    if (lane == 0) __hip_atomic_store(myflag, tag, RLX_AGENT);
    STAMP(26);
    RSTAMP(61);
    BARRIER();
    if (!act2) { retire(); return; }
    if (cw == 3) {
#pragma unroll
      for (int j = 0; j < 4; ++j) glds16(offl, l8 + ((j & 1) * 144 + (j >> 1) * 36) * XROW, L + 35 * 4096 + j * 1024);
    }
    phase2<2>(A, ors, tag, lane STAMP_PASS);
    asm volatile(".Lfk_tail:");
  }
  WAIT_LGKM0();
  BARRIER();
  STAMP(30);
  if (wave < 5 && rt * 5 + wave < 128) {
    f32x4 u[2][2][4];
#pragma unroll
    for (int it = 0; it < 2; ++it)
#pragma unroll
      for (int hf = 0; hf < 2; ++hf)
#pragma unroll
        for (int w = 0; w < 4; ++w) u[it][hf][w] = *(LDSP f32x4*)(size_t)(A.ra[it][hf][w >> 1] + (w & 1) * 36864);
    SCHED0();
#pragma unroll
    for (int it = 0; it < 2; ++it) {
      f32x4 s0 = (u[it][0][0] + u[it][0][1]) + (u[it][0][2] + u[it][0][3]), s1 = (u[it][1][0] + u[it][1][1]) + (u[it][1][2] + u[it][1][3]);
      u32x4 o = {cvtpk(s0[0], s0[1]), cvtpk(s0[2], s0[3]), cvtpk(s1[0], s1[1]), cvtpk(s1[2], s1[3])};
      *(GAS u32x4*)(it ? A.op1 : A.op) = o;
      SCHED0();
    }
  }
  STAMP(31);
  RSTAMP(63);
  STAMP_OUT();
}

#undef x
#undef wA
#undef wB
#undef woA
#undef woB
#undef gw
#undef ws
#undef out
#undef ors

extern "C" void kernel_launch(void* const* d_in, const int* in_sizes, int n_in, void* d_out, int out_size, void* d_ws, size_t ws_size,
                              hipStream_t stream) {
  fused_kernel<<<256, 512, 0, stream>>>((const u16*)d_in[0], (const u16*)d_in[1], (const u16*)d_in[4], (const u16*)d_in[2], (const u16*)d_in[5],
                                        (const u16*)d_in[6], (char*)d_ws, (u16*)d_out);
}
```

```cpp
#include <hip/hip_runtime.h>
#include <hip/hip_bf16.h>
#include <stdint.h>
#include <type_traits>

typedef unsigned short u16;
typedef unsigned long long u64;
typedef short s16x4 __attribute__((ext_vector_type(4)));
typedef float f32x2 __attribute__((ext_vector_type(2)));
typedef float f32x4 __attribute__((ext_vector_type(4)));
typedef unsigned u32x2 __attribute__((ext_vector_type(2)));
typedef unsigned u32x4 __attribute__((ext_vector_type(4)));
typedef u32x4 frag;
typedef __bf16 bf16x2 __attribute__((ext_vector_type(2)));
typedef __bf16 bfx8 __attribute__((ext_vector_type(8)));
#define LDSP __attribute__((address_space(3)))
#define GAS __attribute__((address_space(1)))
#define DI __device__ __forceinline__

constexpr int WROW = 3456;
constexpr int XROW = 1152;
constexpr int PITCH = 432;
constexpr int KO = 32 * PITCH;
constexpr int STG_W = 64 * PITCH;
constexpr int STG = STG_W + 144 * 128;
constexpr int G_OFF = 3 * STG;
constexpr int SCR_OFF = G_OFF + 4608;
constexpr int EX_OFF = 143360;
constexpr int LDS_BYTES = 163840;
constexpr int O_BYTES = 128 * 36 * 1024;
constexpr int FLAG_OFF = 8 << 20;
constexpr int EPOCH_OFF = FLAG_OFF + (1 << 20);
constexpr int PRE0 = 5;

#define MFMA(a, b, c) __builtin_amdgcn_mfma_f32_16x16x32_bf16(__builtin_bit_cast(bfx8, (a)), __builtin_bit_cast(bfx8, (b)), (c), 0, 0, 0)
#define RLX_AGENT __ATOMIC_RELAXED, __HIP_MEMORY_SCOPE_AGENT
#define HOFF(h) (36 * (h) + ((h) & 1) * 4)

DI void glds16(unsigned voff, const GAS void* sbase, unsigned lds_addr) {
  asm volatile("s_mov_b32 m0, %2\n\ts_nop 0\n\tglobal_load_lds_dwordx4 %0, %1" ::"v"(voff), "s"(sbase), "s"(lds_addr) : "memory");
}
DI u32x2 trd(unsigned a) { return __builtin_bit_cast(u32x2, __builtin_amdgcn_ds_read_tr16_b64_v4i16((LDSP s16x4*)(size_t)a)); }
DI frag ldW(unsigned a0, unsigned a1) {
  u32x2 lo = trd(a0), hi = trd(a1);
  return __builtin_shufflevector(lo, hi, 0, 1, 2, 3);
}
DI frag ld128(unsigned a) { return *(LDSP frag*)(size_t)a; }
DI void st64(unsigned a, u32x2 v) { *(LDSP u32x2*)(size_t)a = v; }
DI unsigned cvtpk(float a, float b) {
  f32x2 f = {a, b};
  return __builtin_bit_cast(unsigned, __builtin_convertvector(f, bf16x2));
}
DI u32x2 pack4(f32x4 a) { return u32x2{cvtpk(a[0], a[1]), cvtpk(a[2], a[3])}; }
DI frag cat(u32x2 a, u32x2 b) { return __builtin_shufflevector(a, b, 0, 1, 2, 3); }
DI void swap32(float& a, float& b) {
  auto r = __builtin_amdgcn_permlane32_swap(__float_as_uint(a), __float_as_uint(b), false, false);
  a = __uint_as_float(r[0]); b = __uint_as_float(r[1]);
}
DI void swap16(float& a, float& b) {
  auto r = __builtin_amdgcn_permlane16_swap(__float_as_uint(a), __float_as_uint(b), false, false);
  a = __uint_as_float(r[0]); b = __uint_as_float(r[1]);
}
DI float fmx(float a, float b) { return __builtin_elementwise_maximum(a, b); }
DI float fmx3(float a, float b, float c) { return fmx(fmx(a, b), c); }
#define SGB(m, n) __builtin_amdgcn_sched_group_barrier(m, n, 0)
#define SCHED0() __builtin_amdgcn_sched_barrier(0)
#define WAIT_LGKM0() do { __builtin_amdgcn_s_waitcnt(0xC07F); asm volatile("" ::: "memory"); } while (0)
#define WAIT_VM0() asm volatile("s_waitcnt vmcnt(0)" ::: "memory")
#define BARRIER() do { asm volatile("" ::: "memory"); __builtin_amdgcn_s_barrier(); asm volatile("" ::: "memory"); } while (0)
#define PIN(x) asm volatile("" : "+v"(x))
#define TAG_SCALAR() do { unsigned lo_ = __builtin_amdgcn_readfirstlane((unsigned)tag), hi_ = __builtin_amdgcn_readfirstlane((unsigned)(tag >> 32)); \
    tag = (((u64)hi_ << 32) | lo_) + 0x9E3779B97F4A7C15ull; asm volatile("" : "+s"(tag)); } while (0)
#define PINS(x) asm volatile("" : "+s"(x))
#define WR2(addr, a, b, o0, o1) asm volatile("ds_write2st64_b64 %0, %1, %2 offset0:%3 offset1:%4" ::"v"(addr), "v"(a), "v"(b), "i"(o0), "i"(o1) : "memory")

DI void pv_store(u32x4 pk, const frag (&V)[5], u32x4 sel, unsigned vmain, unsigned vleft, __amdgpu_buffer_rsrc_t ors, unsigned so, unsigned sl, bool g0) {
  const f32x4 z = {0.f, 0.f, 0.f, 0.f};
  frag P[4];
  f32x4 o[4], os[4];
  auto mkP = [&](int h) {
    unsigned u = pk[h];
    P[h] = frag{__builtin_amdgcn_perm(u, u, sel[0]), __builtin_amdgcn_perm(u, u, sel[1]), __builtin_amdgcn_perm(u, u, sel[2]), __builtin_amdgcn_perm(u, u, sel[3])};
    PIN(P[h]);
  };
  mkP(0); mkP(1);
  SCHED0();
  u32x4 f0, f1;
  os[0] = MFMA(V[4], P[0], z); PIN(os[0]); o[0] = MFMA(V[0], P[0], z); PIN(o[0]); mkP(2); SCHED0();
  os[1] = MFMA(V[4], P[1], z); PIN(os[1]); o[1] = MFMA(V[1], P[1], z); PIN(o[1]); mkP(3); SCHED0();
  os[2] = MFMA(V[4], P[2], z); PIN(os[2]); os[3] = MFMA(V[4], P[3], z); PIN(os[3]);
  o[2] = MFMA(V[2], P[2], z); PIN(o[2]); o[3] = MFMA(V[3], P[3], z); PIN(o[3]); SCHED0();
  f0 = u32x4{cvtpk(o[0][0], o[0][1]), cvtpk(o[0][2], o[0][3]), cvtpk(o[1][0], o[1][1]), cvtpk(o[1][2], o[1][3])};
  PIN(f0); SCHED0();
  __builtin_amdgcn_raw_buffer_store_b128(f0, ors, vmain, so, 16);
  SCHED0();
  u32x2 f2 = {cvtpk(g0 ? os[0][0] : os[2][0], g0 ? os[0][1] : os[2][1]), cvtpk(g0 ? os[1][2] : os[3][2], g0 ? os[1][3] : os[3][3])};
  PIN(f2); SCHED0();
  f1 = u32x4{cvtpk(o[2][0], o[2][1]), cvtpk(o[2][2], o[2][3]), cvtpk(o[3][0], o[3][1]), cvtpk(o[3][2], o[3][3])};
  PIN(f1); PIN(f0); SCHED0();
  __builtin_amdgcn_raw_buffer_store_b128(f1, ors, vmain + 1024, so, 16);
  __builtin_amdgcn_raw_buffer_store_b64(f2, ors, vleft, sl, 16);
  asm volatile("s_nop 0" ::"v"(f1) : "memory");
  SCHED0();
}

struct P2 {
  unsigned so[9], vo[3], bw[4], rb[4], ra[2][2][2];
  GAS u64* fp;
  GAS char *op, *op1;
};
template <int MT>
DI void phase2(const P2& A, __amdgpu_buffer_rsrc_t ors, u64 tag, int lane) {
  const unsigned (&so)[9] = A.so; const unsigned (&vo)[3] = A.vo; const unsigned (&bw)[4] = A.bw; const unsigned (&rb)[4] = A.rb;
  GAS u64* fp = A.fp;
  frag wf[9][4];
  auto rdw = [&](int i) {
#pragma unroll
    for (int n = 0; n < 4; ++n) wf[i][n] = ldW(bw[n] + i * 4096, bw[n] + i * 4096 + 2048);
  };
  rdw(0); rdw(1); rdw(2); rdw(3); rdw(4);
  SCHED0();
  WAIT_LGKM0();
  BARRIER();
#pragma clang loop unroll(disable)
  for (unsigned spins = 0; spins < (1u << 17); ++spins) {
    u64 v = __hip_atomic_load(fp, RLX_AGENT);
    if (__all(v == tag)) break;
    __builtin_amdgcn_s_sleep(1);
  }
  __builtin_amdgcn_fence(__ATOMIC_ACQUIRE, "wavefront");
  asm volatile("" ::: "memory");
  frag of[9][MT];
  constexpr int WIN = 2;
#pragma unroll
  for (int i = 0; i < WIN; ++i)
#pragma unroll
    for (int m = 0; m < MT; ++m) { of[i][m] = __builtin_amdgcn_raw_buffer_load_b128(ors, vo[m], so[i], 16); SCHED0(); }
  f32x4 acc[MT][4];
#pragma unroll
  for (int i = 0; i < 8; ++i) {
    const bool rd = i >= 2 && i + 3 < 9;
    if (rd) rdw(i + 3);
#pragma unroll
    for (int m = 0; m < MT; ++m) {
#pragma unroll
      for (int n = 0; n < 4; ++n) acc[m][n] = MFMA(wf[i][n], of[i][m], i == 0 ? (f32x4{0, 0, 0, 0}) : acc[m][n]);
      if (i + WIN < 9) of[i + WIN][m] = __builtin_amdgcn_raw_buffer_load_b128(ors, vo[m], so[i + WIN], 16);
    }
#pragma unroll
    for (int m = 0; m < MT; ++m) {
#pragma unroll
      for (int n = 0; n < 4; ++n) { SGB(0x8, 1); if (m < 2 && rd) SGB(0x100, 1); }
      if (i + WIN < 9) SGB(0x20, 1);
    }
    SCHED0();
  }
#pragma unroll
  for (int m = 0; m <= MT; ++m) {
    if (m < MT) {
#pragma unroll
      for (int n = 0; n < 4; ++n) { acc[m][n] = MFMA(wf[8][n], of[8][m], acc[m][n]); PIN(acc[m][n]); }
    }
    SCHED0();
    if (m > 0) {
#pragma unroll
      for (int n = 0; n < 4; ++n) *(LDSP f32x4*)(size_t)(rb[n] + (m - 1) * 4096) = acc[m - 1][n];
    }
    SCHED0();
  }
}

typedef u64 u64x8 __attribute__((ext_vector_type(8)));
extern "C" __global__ __launch_bounds__(512, 2) void fused_kernel(const u16* x_, const u16* wA_, const u16* wB_, const u16* woA_, const u16* woB_,
                                                                   const u16* gw_, char* ws_, u16* out_) {
  __shared__ __attribute__((aligned(1024))) char lds[LDS_BYTES];
  const unsigned L = (unsigned)(size_t)(LDSP char*)lds;
  const int BIDX = blockIdx.x, TIDX = threadIdx.x;
  u64x8 ka;
  asm volatile("s_load_dwordx16 %0, %1, 0x0" : "=s"(ka) : "s"(__builtin_amdgcn_kernarg_segment_ptr()));
#define KARG_WAIT() do { SCHED0(); asm volatile("s_waitcnt lgkmcnt(0)" ::: "memory"); SCHED0(); asm volatile("" : "+s"(ka)); } while (0)
#define x ((const GAS u16*)ka[0])
#define wA ((const GAS u16*)ka[1])
#define wB ((const GAS u16*)ka[2])
#define woA ((const GAS u16*)ka[3])
#define woB ((const GAS u16*)ka[4])
#define gw ((const GAS u16*)ka[5])
#define ws ((GAS char*)ka[6])
#define out ((GAS u16*)ka[7])
#define ors __builtin_amdgcn_make_buffer_rsrc((void*)ka[6], 0, O_BYTES, 0x00020000)
  const int bid = BIDX;
  const int xcd = bid & 7, idx = bid >> 3;
  const int tl = idx & 7, gl = idx >> 3;
  const int tile = (xcd & 1) * 8 + tl;
  const int grp = (xcd >> 1) * 4 + gl;
  const int rot = ((tl + gl) & 3) * 2 + ((xcd ^ (xcd >> 1)) & 1);
  const int tid = TIDX, lane = tid & 63;
  const int wave = __builtin_amdgcn_readfirstlane(tid >> 6);
  const int vl = lane >> 3;
  if (tid < 7) {
    u64 pc0;
    unsigned len;
    asm volatile("s_getpc_b64 %0\n\ts_mov_b32 %1, .Lfk_tail-fused_kernel+128" : "=s"(pc0), "=s"(len));
    unsigned off = (idx * 7 + tid) * 128;
    off = off < len ? off : 0;
    asm volatile("s_mov_b32 m0, %2\n\ts_nop 0\n\tglobal_load_lds_dword %0, %1" ::"v"(off), "s"(pc0 & ~255ull), "s"(L + SCR_OFF) : "memory");
  }
  u64 tag;
  const int lin0 = xcd * 30 + idx;
  const bool act2 = idx < 30 && lin0 < 234;
  const int lin = act2 ? lin0 : 0;
  const int rt = (lin * 57) >> 9, ct = lin - rt * 9;
  const int rot2 = 3 * (rt - ((rt * 171) >> 9) * 3);
  const unsigned wsch = ((((lane & 7) >> 1) ^ ((vl >> 1) & 3)) << 5) | ((lane & 1) << 4);
  const unsigned offn = vl * XROW + wsch, offl = ((vl >> 2) * 72 + (vl & 3)) * XROW + wsch;
  auto wo_piece = [&](int kq, int i, int j) {
    const GAS char* b = (const GAS char*)(kq >= 2 ? woB : woA) + ct * 128 + (kq & 1) * (288 * XROW);
    if (i == 8) b += ((j & 1) * 144 + ((j >> 1) ? 52 : 16)) * XROW;
    else b += (((i + rot2) & 7) * 36 + (j >> 1) * 20 + 8 * (j & 1)) * XROW;
    glds16(i == 8 ? offl : offn, b, L + (kq * 9 + i) * 4096 + j * 1024);
  };
  const int g = lane >> 4, q = (lane >> 2) & 3, p = lane & 3, c = lane & 15;
  const int cw = wave & 3;

  auto retire = [&]() {
    if (bid == 255) {
      if (wave < 4) {
        const int b = wave * 64 + lane;
        GAS u64* fp = (GAS u64*)(ws + FLAG_OFF + (((b & 15) >> 2) * 64 + (b >> 4) * 4) * 128 + (b & 3) * 8);
#pragma clang loop unroll(disable)
        for (unsigned spins = 0; spins < (1u << 17); ++spins) {
          u64 v = __hip_atomic_load(fp, RLX_AGENT);
          if (__all(v == tag)) break;
          __builtin_amdgcn_s_sleep(8);
        }
      }
      BARRIER();
      if (tid == 0) __hip_atomic_store((GAS u64*)(ws + EPOCH_OFF), tag, RLX_AGENT);
    }
  };
  P2 A;
  const GAS char* l8;
  auto p2setup = [&](int MT, int tt0, int mrow) {
    int l2 = lane;
    PIN(l2);
    const int g = l2 >> 4, q = (l2 >> 2) & 3, p = l2 & 3, c = l2 & 15, v2 = l2 >> 3;
    const unsigned sw = (2 * g + (q >> 1)) & 3;
#pragma unroll
    for (int n = 0; n < 4; ++n) { A.bw[n] = L + cw * 36864 + (4 * g + q) * 128 + 8 * p + ((n ^ sw) * 32); PIN(A.bw[n]); }
#pragma unroll
    for (int i = 0; i < 9; ++i) { A.so[i] = (i < 8 ? 8 * cw + ((i + rot2) & 7) : 32 + cw) * 1024; PINS(A.so[i]); }
#pragma unroll
    for (int m = 0; m < 3; ++m) { int tt = tt0 + m; tt = tt > 127 ? 127 : tt; A.vo[m] = l2 * 16 + tt * 36864; PIN(A.vo[m]); }
    int tf = (l2 & 4) ? tt0 + MT - 1 : tt0; tf = tf > 127 ? 127 : tf;
    A.fp = (GAS u64*)(ws + FLAG_OFF + (cw * 64 + (tf >> 1)) * 128 + (l2 & 3) * 8);
    PIN(A.fp);
#pragma unroll
    for (int n = 0; n < 4; ++n) { A.rb[n] = L + cw * 36864 + (mrow + c) * 256 + (((4 * n + g) ^ c) * 16); PIN(A.rb[n]); }
#pragma unroll
    for (int it = 0; it < 2; ++it)
#pragma unroll
      for (int hf = 0; hf < 2; ++hf)
#pragma unroll
        for (int wp = 0; wp < 2; ++wp) {
          A.ra[it][hf][wp] = L + wp * 73728 + (wave * 16 + it * 8 + v2) * 256 + ((((l2 & 7) * 2 + hf) ^ (it * 8 + v2)) * 16);
          PIN(A.ra[it][hf][wp]);
        }
    A.op = (GAS char*)out + (size_t)(rt * 80 + wave * 16 + v2) * XROW + ct * 128 + (l2 & 7) * 16;
    A.op1 = A.op + 8 * XROW;
    PIN(A.op); PIN(A.op1);
    l8 = (const GAS char*)woB + ct * 128 + (288 + 16) * XROW;
    PINS(l8);
  };

  if (__builtin_expect(wave >= 4, 1)) {
    __builtin_amdgcn_s_setprio(2);
    const int lw = cw, tl256 = tid - 256;
    const unsigned offx = (tile * 128 + 8 * lw + vl) * XROW + ((lane & 7) ^ ((4 * lw + (vl >> 1)) & 7)) * 16;
    KARG_WAIT();
    const GAS char* w = (const GAS char*)((grp >= 8) ? wB : wA) + (grp & 7) * 144;
    unsigned offw[7], offh;
    auto kchunk = [&](int st) { int kc = st + rot; return kc >= 9 ? kc - 9 : kc; };
    auto issueX = [&](int kc, unsigned sb, int jj) { glds16(offx, (const GAS char*)x + kc * 128 + jj * (32 * XROW), sb + STG_W + (2 + lw + 4 * jj) * 1024); };
    auto issueW = [&](int kc, unsigned sb, int j) { glds16(offw[j], w + kc * (64 * WROW), sb + (lw + 4 * j) * 1024); };
    auto issue6 = [&](int kc, unsigned sb) {
      if (lw < 3) issueW(kc, sb, 6);
      else { glds16(offw[6], (const GAS char*)x + kc * 128, sb + STG_W); glds16(offh, (const GAS char*)x + kc * 128, sb + STG_W + 1024); }
    };
    const int kc0 = rot, kc1 = kchunk(1);
    const unsigned sb0 = L, sb1 = L + STG;
    auto calcw = [&](int j) {
      unsigned qq = 256 * j + tl256;
      unsigned u = (qq * 2428u) >> 16, cc = qq - u * 27;
      unsigned row = (u & 0x38) | ((u & 1) << 2) | ((u >> 1) & 3);
      offw[j] = row * WROW + cc * 16 + ((cc * 57u) >> 9) * 1008;
    };
    issueX(kc0, sb0, 0);
    u32x2 gv;
    const unsigned gi = tl256 < 144 ? tl256 : 143;
    asm volatile("global_load_dwordx2 %0, %1, %2" : "=v"(gv) : "v"(gi * 8), "s"(gw) : "memory");
#pragma unroll
    for (int j = 0; j < 6; ++j) {
      SCHED0();
      calcw(j);
      SCHED0();
      issueW(kc0, sb0, j);
      if (j < 3) issueX(kc0, sb0, j + 1);
    }
    calcw(6);
    {
      int t0 = tile * 128 - 16 + vl, t1 = t0 + 8;
      t0 = t0 < 0 ? 0 : t0; t1 = t1 < 0 ? 0 : t1;
      unsigned h0 = t0 * XROW + ((lane & 7) ^ (vl >> 1)) * 16;
      offh = t1 * XROW + ((lane & 7) ^ (4 + (vl >> 1))) * 16;
      offw[6] = lw == 3 ? h0 : offw[6];
    }
    issue6(kc0, sb0);
    issueW(kc1, sb1, 0);
    int slot = 0, kn = kc1;
    const GAS char* xp = (const GAS char*)x + kn * 128;
    unsigned b1 = sb1, xd = sb1 + STG_W + (2 + lw) * 1024;
    PINS(xp); PINS(xd);
    SCHED0();
#pragma unroll
    for (int j = 1; j < PRE0; ++j) issueW(kc1, sb1, j);
    asm volatile("s_waitcnt vmcnt(%1)" : "+v"(gv) : "i"(10 + PRE0) : "memory");
    {
      unsigned a0 = __builtin_amdgcn_perm(gv[0], gv[0], 0x01000100), a1 = __builtin_amdgcn_perm(gv[0], gv[0], 0x03020302);
      unsigned a2 = __builtin_amdgcn_perm(gv[1], gv[1], 0x01000100), a3 = __builtin_amdgcn_perm(gv[1], gv[1], 0x03020302);
      *(LDSP u32x4*)(size_t)(L + G_OFF + gi * 32) = u32x4{a0, a0, a1, a1};
      *(LDSP u32x4*)(size_t)(L + G_OFF + gi * 32 + 16) = u32x4{a2, a2, a3, a3};
    }
    WAIT_LGKM0();
    asm volatile("s_waitcnt vmcnt(%0)" ::"i"(PRE0) : "memory");
#pragma clang loop unroll(disable)
    for (int st = 0; st < 7; ++st) {
      BARRIER();
      glds16(offx, xp, xd);
      SCHED0();
      glds16(offx, xp + 32 * XROW, xd + 4096);
      if (st == 0) {
#pragma unroll
        for (int j = PRE0; j < 6; ++j) issueW(kc1, sb1, j);
        tag = __hip_atomic_load((GAS u64*)(ws + EPOCH_OFF), RLX_AGENT);
      }
      issue6(kn, b1);
      glds16(offx, xp + 64 * XROW, xd + 8192);
      glds16(offx, xp + 96 * XROW, xd + 12288);
      SCHED0();
      kn = kn == 8 ? 0 : kn + 1;
      b1 = L + (slot == 0 ? 2 : slot - 1) * STG;
#pragma unroll
      for (int j = 0; j < 6; ++j) issueW(kn, b1, j);
      slot = slot == 2 ? 0 : slot + 1;
      xp = (const GAS char*)x + kn * 128;
      xd = b1 + STG_W + (2 + lw) * 1024;
      PINS(xp); PINS(xd);
      asm volatile("s_waitcnt vmcnt(6)" ::: "memory");
    }
    BARRIER();
    glds16(offx, xp, xd);
    SCHED0();
    glds16(offx, xp + 32 * XROW, xd + 4096);
    issue6(kn, b1);
    glds16(offx, xp + 64 * XROW, xd + 8192);
    glds16(offx, xp + 96 * XROW, xd + 12288);
#pragma unroll
    for (int I = 0; I < 6; ++I) wo_piece(0, I, lw);
    asm volatile("s_waitcnt vmcnt(6)" ::: "memory");
    BARRIER();
    wo_piece(0, 6, lw); wo_piece(0, 7, lw);
    __builtin_amdgcn_s_setprio(0);
#pragma unroll
    for (int I = 8; I < 22; ++I) wo_piece(I / 9, I % 9, lw);
    WAIT_LGKM0();
    BARRIER();
#pragma unroll
    for (int I = 22; I < 35; ++I) wo_piece(I / 9, I % 9, lw);
    p2setup(3, rt * 5, 0);
    WAIT_VM0();
    TAG_SCALAR();
    BARRIER();
    if (!act2) { retire(); return; }
    if (cw == 3) {
#pragma unroll
      for (int j = 0; j < 4; ++j) glds16(offl, l8 + ((j & 1) * 144 + (j >> 1) * 36) * XROW, L + 35 * 4096 + j * 1024);
    }
    phase2<3>(A, ors, tag, lane);
  } else {
    const unsigned wrow = L + (8 * g + 2 * q) * PITCH;
    f32x4 maskP, maskC, maskP0;
    {
      const float ninf = -__builtin_inff();
#pragma unroll
      for (int r = 0; r < 4; ++r) {
        bool pm = (r & 1) == (c & 1);
        maskC[r] = (pm && 4 * g + r <= c) ? 0.f : ninf;
        maskP[r] = (pm && 4 * g + r > c) ? 0.f : ninf;
      }
      maskP0 = maskP + ((tile == 0 && wave == 0) ? -998244352.f * 4.242640687119285f : 0.f);
      asm volatile("" : "+v"(maskP), "+v"(maskC), "+v"(maskP0));
    }
    u32x4 sel;
    {
      const int par = c & 1;
      const bool cur1 = 4 * g + par <= c, cur2 = 4 * g + 2 + par <= c;
      const unsigned s1 = par ? 0x01000c0cu : 0x0c0c0100u;
      const unsigned s2 = par ? 0x03020c0cu : 0x0c0c0302u;
      sel[0] = cur1 ? 0x0c0c0c0cu : s1;
      sel[1] = cur2 ? 0x0c0c0c0cu : s2;
      sel[2] = cur1 ? s1 : 0x0c0c0c0cu;
      sel[3] = cur2 ? s2 : 0x0c0c0c0cu;
    }
    unsigned vmain = lane * 16, vleft = lane < 32 ? c * 16 + g * 8 : 0x40000000u;
    unsigned osoff = (tile * 8 + 2 * wave) * 36864 + grp * 2048, oloff = (tile * 8 + 2 * wave) * 36864 + 32768 + grp * 256;
    unsigned osoff1 = osoff + 36864, oloff1 = oloff + 36864;
    PINS(osoff); PINS(oloff); PINS(osoff1); PINS(oloff1);
    asm volatile("" : "+v"(sel), "+v"(vmain), "+v"(vleft));
    GAS u64* myflag;
    unsigned sgb = (grp >= 8) ? 0x3fb8aa3bu : 0xbfb8aa3bu;
    PINS(sgb);
    const float sg = __uint_as_float(sgb);
    f32x4 aK[2][4], aQ[2][4], aKQ[2], aV[2][5], hk, hv, he;
    frag cv[5], xb[3];
    unsigned exr = L + EX_OFF + wave * 5120 + lane * 8, exh = L + EX_OFF + wave * 512 + lane * 8;
    unsigned exgw = L + SCR_OFF + wave * 128 + g * 16, exgr = L + SCR_OFF + wave * 128 + c * 4;
    PIN(exr); PIN(exh); PIN(exgw); PIN(exgr);
    auto mainloop = [&](auto WC) {
      constexpr int W = decltype(WC)::value;
      constexpr int XO = (1 + 2 * W) * 2048;
      hk = f32x4{0, 0, 0, 0}; hv = f32x4{0, 0, 0, 0}; he = f32x4{0, 0, 0, 0};
      PIN(he);
#pragma unroll
      for (int t = 0; t < 2; ++t) {
#pragma unroll
        for (int f = 0; f < 4; ++f) { aK[t][f] = f32x4{0, 0, 0, 0}; aQ[t][f] = f32x4{0, 0, 0, 0}; }
        aKQ[t] = f32x4{0, 0, 0, 0};
#pragma unroll
        for (int f = 0; f < 5; ++f) aV[t][f] = f32x4{0, 0, 0, 0};
      }
      unsigned cW = wrow + 8 * p;
      unsigned cKQ = wrow + (p < 2 ? 144 : 0) + ((p & 1) ? 104 : 32);
      unsigned cX0 = L + STG_W + c * 128 + ((g ^ (c >> 1)) * 16), cX1 = cX0 ^ 64;
      unsigned cV[4];
#pragma unroll
      for (int i = 0; i < 4; ++i)
        cV[i] = p < 2 ? wrow + 288 + (p ? 104 : 32) + (i >> 1) * KO + (i & 1) * PITCH : L + G_OFF + rot * 512 + (8 * g + q) * 8 + (i >> 1) * 256 + (i & 1) * 32;
#pragma unroll
      for (int f = 0; f < 5; ++f) cv[f] = frag{0, 0, 0, 0};
      xb[0] = frag{0, 0, 0, 0}; xb[1] = frag{0, 0, 0, 0}; xb[2] = frag{0, 0, 0, 0};
      asm volatile("" : "+v"(aK[0][0]), "+v"(aK[0][1]), "+v"(aK[0][2]), "+v"(aK[0][3]), "+v"(aK[1][0]), "+v"(aK[1][1]), "+v"(aK[1][2]), "+v"(aK[1][3]),
                        "+v"(aQ[0][0]), "+v"(aQ[0][1]), "+v"(aQ[0][2]), "+v"(aQ[0][3]), "+v"(aQ[1][0]), "+v"(aQ[1][1]), "+v"(aQ[1][2]), "+v"(aQ[1][3]),
                        "+v"(aKQ[0]), "+v"(aKQ[1]), "+v"(hk), "+v"(hv), "+v"(xb[0]), "+v"(xb[1]), "+v"(xb[2]));
      asm volatile("" : "+v"(aV[0][0]), "+v"(aV[0][1]), "+v"(aV[0][2]), "+v"(aV[0][3]), "+v"(aV[0][4]), "+v"(aV[1][0]), "+v"(aV[1][1]), "+v"(aV[1][2]),
                        "+v"(aV[1][3]), "+v"(aV[1][4]), "+v"(cv[0]), "+v"(cv[1]), "+v"(cv[2]), "+v"(cv[3]), "+v"(cv[4]),
                        "+v"(cW), "+v"(cKQ), "+v"(cX0), "+v"(cX1), "+v"(cV[0]), "+v"(cV[1]), "+v"(cV[2]), "+v"(cV[3]));
      SCHED0();
      KARG_WAIT();
      tag = __hip_atomic_load((GAS u64*)(ws + EPOCH_OFF), RLX_AGENT);
      myflag = (GAS u64*)(ws + FLAG_OFF + ((grp >> 2) * 64 + tile * 4 + cw) * 128 + (grp & 3) * 8);
      PINS(myflag);
      WAIT_LGKM0();
      int slot = 0, kc = rot;
#pragma clang loop unroll(disable)
      for (int st = 0; st < 9; ++st) {
        BARRIER();
        SCHED0();
        frag xa[3], W0[14], W1[9];
        xa[0] = ld128(cX0 + XO); xa[1] = ld128(cX0 + XO + 2048);
#pragma unroll
        for (int f = 0; f < 4; ++f) W0[f ^ 1] = ldW(cW + 144 + HOFF(f ^ 1), cW + 144 + HOFF(f ^ 1) + PITCH);
        xa[2] = ld128(cX0);
#pragma unroll
        for (int f = 0; f < 5; ++f) { aV[0][f] = MFMA(xb[0], cv[f], aV[0][f]); aV[1][f] = MFMA(xb[1], cv[f], aV[1][f]); }
        hv = MFMA(xb[2], cv[W], hv);
        if (W == 1) he = MFMA(xb[2], cv[4], he);
        W0[5] = ldW(cW + HOFF(0), cW + HOFF(0) + PITCH);
        W0[4] = ldW(cKQ, cKQ + PITCH);
        W0[7] = ldW(cW + HOFF(2), cW + HOFF(2) + PITCH);
        W0[6] = ldW(cW + HOFF(1), cW + HOFF(1) + PITCH);
        W0[9] = ldW(cW + 288 + HOFF(0), cW + 288 + HOFF(0) + PITCH);
        W0[8] = ldW(cW + HOFF(3), cW + HOFF(3) + PITCH);
        W0[11] = ldW(cW + 288 + HOFF(2), cW + 288 + HOFF(2) + PITCH);
        W0[10] = ldW(cW + 288 + HOFF(1), cW + 288 + HOFF(1) + PITCH);
        W0[13] = ldW(cV[0], cV[1]);
        W0[12] = ldW(cW + 288 + HOFF(3), cW + 288 + HOFF(3) + PITCH);
        xb[0] = ld128(cX1 + XO); xb[1] = ld128(cX1 + XO + 2048); xb[2] = ld128(cX1);
#pragma unroll
        for (int f = 0; f < 4; ++f) W1[f ^ 1] = ldW(cW + KO + 144 + HOFF(f ^ 1), cW + KO + 144 + HOFF(f ^ 1) + PITCH);
        W1[5] = ldW(cW + KO + HOFF(0), cW + KO + HOFF(0) + PITCH);
        W1[4] = ldW(cKQ + KO, cKQ + KO + PITCH);
        W1[8] = ldW(cW + KO + HOFF(3), cW + KO + HOFF(3) + PITCH);
        W1[7] = ldW(cW + KO + HOFF(2), cW + KO + HOFF(2) + PITCH);
        W1[6] = ldW(cW + KO + HOFF(1), cW + KO + HOFF(1) + PITCH);
#pragma unroll
        for (int f = 0; f < 4; ++f) cv[f] = ldW(cW + KO + 288 + HOFF(f), cW + KO + 288 + HOFF(f) + PITCH);
        cv[4] = ldW(cV[2], cV[3]);
#pragma unroll
        for (int f = 0; f < 4; ++f) { aK[0][f] = MFMA(W0[f], xa[0], aK[0][f]); aK[1][f] = MFMA(W0[f], xa[1], aK[1][f]); }
        hk = MFMA(W0[W], xa[2], hk);
        aKQ[0] = MFMA(W0[4], xa[0], aKQ[0]); aKQ[1] = MFMA(W0[4], xa[1], aKQ[1]);
        if (W == 0) he = MFMA(W0[4], xa[2], he);
#pragma unroll
        for (int f = 0; f < 4; ++f) { aQ[0][f] = MFMA(W0[5 + f], xa[0], aQ[0][f]); aQ[1][f] = MFMA(W0[5 + f], xa[1], aQ[1][f]); }
#pragma unroll
        for (int f = 0; f < 5; ++f) { aV[0][f] = MFMA(xa[0], W0[9 + f], aV[0][f]); aV[1][f] = MFMA(xa[1], W0[9 + f], aV[1][f]); }
        hv = MFMA(xa[2], W0[9 + W], hv);
        if (W == 1) he = MFMA(xa[2], W0[13], he);
#pragma unroll
        for (int f = 0; f < 4; ++f) { aK[0][f] = MFMA(W1[f], xb[0], aK[0][f]); aK[1][f] = MFMA(W1[f], xb[1], aK[1][f]); }
        hk = MFMA(W1[W], xb[2], hk);
        aKQ[0] = MFMA(W1[4], xb[0], aKQ[0]); aKQ[1] = MFMA(W1[4], xb[1], aKQ[1]);
        if (W == 0) he = MFMA(W1[4], xb[2], he);
#pragma unroll
        for (int f = 0; f < 4; ++f) { aQ[0][f] = MFMA(W1[5 + f], xb[0], aQ[0][f]); aQ[1][f] = MFMA(W1[5 + f], xb[1], aQ[1][f]); }
        const int ds = slot == 2 ? -2 * STG : STG, dg = kc == 8 ? -8 * 512 : 512;
        slot = slot == 2 ? 0 : slot + 1;
        kc = kc == 8 ? 0 : kc + 1;
        const int dv = p < 2 ? ds : dg;
        cW += ds; cKQ += ds; cX0 += ds; cX1 += ds;
#pragma unroll
        for (int i = 0; i < 4; ++i) cV[i] += dv;
        SGB(0x100, 2);
#pragma unroll
        for (int i = 0; i < 54; ++i) {
          SGB(0x8, 1);
          if (i < 3 || i == 16 || i == 28 || i == 40) SGB(0x100, 2); else SGB(0x100, 1);
          if (i >= 3 && i < 11) SGB(0x4, 1);
          if (i >= 51) SGB(0x2, 1);
        }
#pragma unroll
        for (int i = 0; i < 4; ++i) { SGB(0x8, 1); SGB(0x2, 2); }
        SGB(0x8, W < 2 ? 4 : 2);
        SCHED0();
        WAIT_LGKM0();
      }
      hv = MFMA(xb[2], cv[W], hv);
      if (W == 1) he = MFMA(xb[2], cv[4], he);
      if (W < 2) {
        u32x2 v = pack4(he);
        if (W == 0 && tile == 0) v = u32x2{0u, 0u};
        st64(L + EX_OFF + lane * 8 + (W == 0 ? 4 : 9) * 512, v);
      }
    };
    if (wave == 0) mainloop(std::integral_constant<int, 0>{});
    else if (wave == 1) mainloop(std::integral_constant<int, 1>{});
    else if (wave == 2) mainloop(std::integral_constant<int, 2>{});
    else mainloop(std::integral_constant<int, 3>{});
    WAIT_VM0();
    TAG_SCALAR();
    const bool g0 = g == 0;
    frag KN[4], Q0[4], V[5];
    u32x2 kb[5], vb[5], ksa, qs[2];
    aV[1][4] = MFMA(xb[1], cv[4], aV[1][4]); PIN(aV[1][4]);
    aV[0][4] = MFMA(xb[0], cv[4], aV[0][4]); PIN(aV[0][4]);
    {
      u32x2 k1 = pack4(aKQ[1]), k0 = pack4(aKQ[0]);
      auto r0 = __builtin_amdgcn_permlane32_swap(k1[0], 0u, false, false);
      auto r1 = __builtin_amdgcn_permlane32_swap(k1[1], 0u, false, false);
      auto r2 = __builtin_amdgcn_permlane32_swap(k0[0], 0u, false, false);
      auto r3 = __builtin_amdgcn_permlane32_swap(k0[1], 0u, false, false);
      qs[1] = u32x2{r0[1], r1[1]}; qs[0] = u32x2{r2[1], r3[1]};
      ksa = u32x2{r2[0], r3[0]}; kb[4] = u32x2{r0[0], r1[0]};
    }
#pragma unroll
    for (int f = 0; f < 4; ++f) { aV[1][f] = MFMA(xb[1], cv[f], aV[1][f]); PIN(aV[1][f]); }
#pragma unroll
    for (int f = 0; f < 4; ++f) kb[f] = pack4(aK[1][f]);
    {
      u32x2 h = pack4(hk), hh = pack4(hv);
      if (tile == 0) h = u32x2{0u, 0u};
      WR2(exh, h, hh, 0, 5);
    }
    if (c >= 8) { *(LDSP f32x4*)(size_t)(exgw) = aV[0][4]; *(LDSP f32x4*)(size_t)(exgw + 64) = aV[1][4]; }
#pragma unroll
    for (int f = 0; f < 4; ++f) { aV[0][f] = MFMA(xb[0], cv[f], aV[0][f]); PIN(aV[0][f]); }
#pragma unroll
    for (int f = 0; f < 5; ++f) vb[f] = pack4(aV[1][f]);
    if (wave < 3) {
#pragma unroll
      for (int f = 0; f < 5; ++f) WR2(exr, kb[f], vb[f], 10 + f, 15 + f);
    }
    u32x2 gz;
    asm volatile("ds_read2_b32 %0, %1 offset0:0 offset1:16" : "=v"(gz) : "v"(exgr) : "memory");
    f32x4 sc[2][4], sp[2][4];
#pragma unroll
    for (int h = 0; h < 4; ++h) {
      u32x2 q1 = pack4(aQ[1][h]), q0 = pack4(aQ[0][h]);
      unsigned e1 = ((h < 2) == g0) ? qs[1][h & 1] : 0u, e0 = ((h < 2) == g0) ? qs[0][h & 1] : 0u;
      frag Q1 = (h & 1) ? frag{q1[0], q1[1], 0u, e1} : frag{q1[0], q1[1], e1, 0u};
      Q0[h] = (h & 1) ? frag{q0[0], q0[1], 0u, e0} : frag{q0[0], q0[1], e0, 0u};
      frag KA = cat(pack4(aK[0][h]), ksa);
      sc[1][h] = MFMA(cat(kb[h], kb[4]), Q1, maskC); PIN(sc[1][h]);
      sp[1][h] = MFMA(KA, Q1, maskP); PIN(sp[1][h]);
      sc[0][h] = MFMA(KA, Q0[h], maskC); PIN(sc[0][h]);
      if (h == 1) {
        SCHED0();
        asm volatile("s_waitcnt lgkmcnt(0)" : "+v"(gz)::"memory");
        BARRIER();
        SCHED0();
#pragma unroll
        for (int k = 0; k < 4; ++k) asm volatile("ds_read2st64_b64 %0, %1 offset0:%2 offset1:4" : "=v"(KN[k]) : "v"(exr), "i"(k) : "memory");
        SCHED0();
      }
    }
    float v[2][4][2];
#pragma unroll
    for (int h = 0; h < 4; ++h) {
      v[1][h][0] = fmx(fmx3(sp[1][h][0], sp[1][h][1], sc[1][h][0]), sc[1][h][1]);
      v[1][h][1] = fmx(fmx3(sp[1][h][2], sp[1][h][3], sc[1][h][2]), sc[1][h][3]);
    }
    asm volatile("s_waitcnt lgkmcnt(0)" : "+v"(KN[0]), "+v"(KN[1]), "+v"(KN[2]), "+v"(KN[3]), "+v"(v[1][0][0]), "+v"(v[1][0][1]), "+v"(v[1][1][0]), "+v"(v[1][1][1]),
                 "+v"(v[1][2][0]), "+v"(v[1][2][1]), "+v"(v[1][3][0]), "+v"(v[1][3][1])::"memory");
#pragma unroll
    for (int h = 0; h < 4; ++h) { sp[0][h] = MFMA(KN[h], Q0[h], maskP0); PIN(sp[0][h]); }
#pragma unroll
    for (int f = 0; f < 5; ++f) V[f] = cat(vb[f], pack4(aV[0][f]));
    u32x4 pk[2];
    auto softmax = [&](float (&w)[4][2], float gate_logit) {
      swap32(w[0][0], w[2][0]); swap32(w[1][0], w[3][0]); swap32(w[0][1], w[2][1]); swap32(w[1][1], w[3][1]);
      swap16(w[0][0], w[1][0]); swap16(w[2][0], w[3][0]); swap16(w[0][1], w[1][1]); swap16(w[2][1], w[3][1]);
      const float cs = 0.23570226039551584f * 1.4426950408889634f;
      float m = fmx(fmx3(fmx3(w[0][0], w[1][0], w[2][0]), w[3][0], w[0][1]), fmx3(w[1][1], w[2][1], w[3][1]));
      f32x2 e[4];
#pragma unroll
      for (int i = 0; i < 4; ++i) {
        f32x2 a = (f32x2{w[i][0], w[i][1]} - m) * cs;
        e[i] = f32x2{__builtin_amdgcn_exp2f(a[0]), __builtin_amdgcn_exp2f(a[1])};
      }
      f32x2 s2 = (e[0] + e[1]) + (e[2] + e[3]);
      float s = s2[0] + s2[1];
      float r = __builtin_amdgcn_rcpf(__builtin_fmaf(s, __builtin_amdgcn_exp2f(gate_logit * sg), s));
      float pf[4];
#pragma unroll
      for (int i = 0; i < 4; ++i) { f32x2 pp = e[i] * r; pf[i] = __uint_as_float(cvtpk(pp[0], pp[1])); }
      swap32(pf[0], pf[2]); swap32(pf[1], pf[3]);
      swap16(pf[0], pf[1]); swap16(pf[2], pf[3]);
      return u32x4{__float_as_uint(pf[0]), __float_as_uint(pf[1]), __float_as_uint(pf[2]), __float_as_uint(pf[3])};
    };
    SCHED0();
#pragma unroll
    for (int h = 0; h < 4; ++h) {
      v[0][h][0] = fmx(fmx3(sp[0][h][0], sp[0][h][1], sc[0][h][0]), sc[0][h][1]);
      v[0][h][1] = fmx(fmx3(sp[0][h][2], sp[0][h][3], sc[0][h][2]), sc[0][h][3]);
    }
    pk[1] = softmax(v[1], __uint_as_float(gz[1]));
#pragma unroll
    for (int h = 0; h < 4; ++h) { PIN(v[0][h][0]); PIN(v[0][h][1]); }
    SCHED0();
    pv_store(pk[1], V, u32x4{sel[2], sel[3], sel[0], sel[1]}, vmain, vleft, ors, osoff1, oloff1, g0);
#pragma unroll
    for (int f = 0; f < 5; ++f) { u32x2 n = *(volatile LDSP u32x2*)(size_t)(exr + (5 + f) * 512); V[f][0] = n[0]; V[f][1] = n[1]; }
    SCHED0();
    pk[0] = softmax(v[0], __uint_as_float(gz[0]));
    SCHED0();
    pv_store(pk[0], V, sel, vmain, vleft, ors, osoff, oloff, g0);
    p2setup(2, rt * 5 + 3, 48);
    SCHED0();
    WAIT_VM0();
    if (lane == 0) __hip_atomic_store(myflag, tag, RLX_AGENT);
    BARRIER();
    if (!act2) { retire(); return; }
    if (cw == 3) {
#pragma unroll
      for (int j = 0; j < 4; ++j) glds16(offl, l8 + ((j & 1) * 144 + (j >> 1) * 36) * XROW, L + 35 * 4096 + j * 1024);
    }
    phase2<2>(A, ors, tag, lane);
    asm volatile(".Lfk_tail:");
  }
  WAIT_LGKM0();
  BARRIER();
  if (wave < 5 && rt * 5 + wave < 128) {
    f32x4 u[2][2][4];
#pragma unroll
    for (int it = 0; it < 2; ++it)
#pragma unroll
      for (int hf = 0; hf < 2; ++hf)
#pragma unroll
        for (int w = 0; w < 4; ++w) u[it][hf][w] = *(LDSP f32x4*)(size_t)(A.ra[it][hf][w >> 1] + (w & 1) * 36864);
    SCHED0();
#pragma unroll
    for (int it = 0; it < 2; ++it) {
      f32x4 s0 = (u[it][0][0] + u[it][0][1]) + (u[it][0][2] + u[it][0][3]), s1 = (u[it][1][0] + u[it][1][1]) + (u[it][1][2] + u[it][1][3]);
      u32x4 o = {cvtpk(s0[0], s0[1]), cvtpk(s0[2], s0[3]), cvtpk(s1[0], s1[1]), cvtpk(s1[2], s1[3])};
      *(GAS u32x4*)(it ? A.op1 : A.op) = o;
      SCHED0();
    }
  }
}

#undef x
#undef wA
#undef wB
#undef woA
#undef woB
#undef gw
#undef ws
#undef out
#undef ors

extern "C" void kernel_launch(void* const* d_in, const int* in_sizes, int n_in, void* d_out, int out_size, void* d_ws, size_t ws_size,
                              hipStream_t stream) {
  fused_kernel<<<256, 512, 0, stream>>>((const u16*)d_in[0], (const u16*)d_in[1], (const u16*)d_in[4], (const u16*)d_in[2], (const u16*)d_in[5],
                                        (const u16*)d_in[6], (char*)d_ws, (u16*)d_out);
}
```
